```python
import torch

def _softmax(x, axis=-1):
    return torch.softmax(x, dim=axis)

def _gelu(x):
    return torch.nn.functional.gelu(x, approximate='tanh')

def _silu(x):
    return torch.nn.functional.silu(x)

def _sigmoid(x):
    return torch.sigmoid(x)

def _layernorm(x, axis=-1, eps=1e-05):
    m = x.mean(dim=axis, keepdim=True)
    v = x.var(dim=axis, keepdim=True, unbiased=False)
    return (x - m) / torch.sqrt(v + eps)

def _scatter_add(tgt, idx, src, axis=0):
    return tgt.clone().index_add_(axis, idx, src)

def _segment_sum(src, seg, n):
    out = torch.zeros((n,) + src.shape[1:], dtype=src.dtype)
    return out.index_add_(0, seg, src)

def _pad(x, pw):
    flat = tuple((v for pair in reversed(pw) for v in pair))
    return torch.nn.functional.pad(x, flat)
kernel_name = 'adaLN_cond__sliding_local_dilated__mixer_channel_mlp__prompt'

def setup_inputs():

    _w = lambda a: (lambda t: t.to(torch.bfloat16) if t.is_floating_point() else t)(torch.as_tensor(a))

    def _A_setup():
        B, S, D = (1, 2048, 512)
        return {'x': _w((lambda *s: torch.randn(*s, dtype=torch.bfloat16) * 0.05)(B, S, D)), 'wqkv': _w((lambda *s: torch.randn(*s, dtype=torch.bfloat16) * 0.05)(D, 3 * D)), 'wo': _w((lambda *s: torch.randn(*s, dtype=torch.bfloat16) * 0.05)(D, D))}
    _A_in = _A_setup()

    def _B_setup():
        B, S, D, F = (1, 2048, 512, 1376)
        return {'x': _w((lambda *s: torch.randn(*s, dtype=torch.bfloat16) * 0.05)(B, S, D)), 'w1': _w((lambda *s: torch.randn(*s, dtype=torch.bfloat16) * 3.2)(D, F) * D ** (-0.5)), 'b1': _w((lambda *s: torch.randn(*s, dtype=torch.bfloat16) * 1.6)(F)), 'w2': _w((lambda *s: torch.randn(*s, dtype=torch.bfloat16) * 1.6)(F, D) * F ** (-0.5)), 'b2': _w((lambda *s: torch.randn(*s, dtype=torch.bfloat16) * 1.6)(D))}
    _B_in = _B_setup()
    return {'A_x': _A_in['x'], 'A_wqkv': _A_in['wqkv'], 'A_wo': _A_in['wo'], 'B_x': _B_in['x'], 'B_w1': _B_in['w1'], 'B_b1': _B_in['b1'], 'B_w2': _B_in['w2'], 'B_b2': _B_in['b2'], 'c': _w((lambda *s: torch.randn(*s, dtype=torch.bfloat16) * 0.2)(1, 512) * 8.0), 'w_mod': _w((lambda *s: torch.randn(*s, dtype=torch.bfloat16) * 0.2)(512, 6 * 512) * 512 ** (-0.5) * 8.0)}

def reference(A_x, A_wqkv, A_wo, B_x, B_w1, B_b1, B_w2, B_b2, c, w_mod):

    def _A_ref(x, wqkv, wo):
        B, S, D = x.shape
        H = 32
        Dh = D // H
        W, dil = (8, 2)
        qkv = (lambda x, *d: x.permute(*d))((x @ wqkv).reshape(B, S, 3, H, Dh), 2, 0, 3, 1, 4)
        q, k, v = (qkv[0], qkv[1], qkv[2])
        scale = Dh ** (-0.5)
        pos = torch.arange(S)
        off = torch.arange(W) * dil
        idx = pos[:, None] - off[None, :]
        mask = idx >= 0
        idx_c = torch.where(mask, idx, 0).reshape(-1)
        kg = k[:, :, idx_c, :].reshape(B, H, S, W, Dh)
        vg = v[:, :, idx_c, :].reshape(B, H, S, W, Dh)
        logits = torch.einsum('bhsd,bhswd->bhsw', q, kg) * scale
        logits = torch.where(mask[None, None, :, :], logits, -1000000000.0)
        attn = _softmax(logits, axis=-1)
        o = torch.einsum('bhsw,bhswd->bhsd', attn, vg)
        return (lambda x, *d: x.permute(*d))(o, 0, 2, 1, 3).reshape(B, S, D) @ wo

    def _B_ref(x, w1, b1, w2, b2):
        y = _layernorm(x, axis=-1)
        h = _gelu(torch.einsum('bsd,df->bsf', y, w1) + b1)
        return torch.einsum('bsf,fd->bsd', h, w2) + b2
    x = A_x
    mod = (_silu(c) @ w_mod).reshape(c.shape[0], 6, 1, -1)
    sa, ca, ga, sb, cb, gb = (mod[:, 0].clone(), mod[:, 1].clone(), mod[:, 2].clone(), mod[:, 3].clone(), mod[:, 4].clone(), mod[:, 5].clone())
    h = _layernorm(x, axis=-1) * (1 + ca) + sa
    x = x + ga * _A_ref(h, A_wqkv, A_wo)
    h = _layernorm(x, axis=-1) * (1 + cb) + sb
    x = x + gb * _B_ref(h, B_w1, B_b1, B_w2, B_b2)
    return x
```

```cpp
#include <hip/hip_runtime.h>
#include <hip/hip_bf16.h>
#include <stdint.h>

typedef unsigned short u16;
__device__ __forceinline__ float bf2f(u16 b){ return __uint_as_float(((unsigned)b)<<16); }
__device__ __forceinline__ u16 f2bf(float f){ unsigned u=__float_as_uint(f); u += 0x7FFFu + ((u>>16)&1u); return (u16)(u>>16); }
__device__ __forceinline__ float rbf(float f){ return bf2f(f2bf(f)); }

constexpr int S=2048, D=512, F=1376, H=32, DH=16;

__global__ void k_mod(const u16* c, const u16* wmod, float* mod){
  int n = blockIdx.x*blockDim.x+threadIdx.x;
  if(n>=3072) return;
  float acc=0;
  for(int k=0;k<512;k++){ float x=bf2f(c[k]); float s=rbf(x/(1.f+__expf(-x))); acc += s*bf2f(wmod[k*3072+n]); }
  mod[n]=rbf(acc);
}
__device__ float wsum(float v){ for(int m=32;m>=1;m>>=1) v+=__shfl_xor(v,m); return v; }
__global__ void k_ln(const u16* x, const float* sh, const float* sc, u16* out, int mode){
  int row=blockIdx.x; int l=threadIdx.x;
  float v[8]; float s=0;
  for(int j=0;j<8;j++){ v[j]=bf2f(x[row*D+l*8+j]); s+=v[j]; }
  float m=wsum(s)/D; float q=0;
  for(int j=0;j<8;j++){ float d=v[j]-m; q+=d*d; }
  float r=rsqrtf(wsum(q)/D+1e-5f);
  s=0;
  for(int j=0;j<8;j++){ v[j]=(v[j]-m)*r*(1.f+sc[l*8+j])+sh[l*8+j]; s+=v[j]; }
  if(mode==1){
    m=wsum(s)/D; q=0;
    for(int j=0;j<8;j++){ float d=v[j]-m; q+=d*d; }
    r=rsqrtf(wsum(q)/D+1e-5f);
    for(int j=0;j<8;j++) v[j]=(v[j]-m)*r;
  }
  for(int j=0;j<8;j++) out[row*D+l*8+j]=f2bf(v[j]);
}
__global__ void k_gemm(const u16* A,const u16* B,u16* C,int M,int N,int K,int epi,const u16* bias,const float* g,const u16* res){
  __shared__ float As[16][17], Bs[16][17];
  int tx=threadIdx.x, ty=threadIdx.y;
  int row=blockIdx.y*16+ty, col=blockIdx.x*16+tx;
  float acc=0;
  for(int k0=0;k0<K;k0+=16){
    As[ty][tx]=bf2f(A[row*K+k0+tx]);
    Bs[ty][tx]=bf2f(B[(k0+ty)*N+col]);
    __syncthreads();
    for(int k=0;k<16;k++) acc+=As[ty][k]*Bs[k][tx];
    __syncthreads();
  }
  float o;
  if(epi==0) o=acc;
  else if(epi==1) o=bf2f(res[row*N+col])+g[col]*acc;
  else if(epi==2){ float z=acc+bf2f(bias[col]); float u=0.7978845608f*(z+0.044715f*z*z*z); o=0.5f*z*(1.f+tanhf(u)); }
  else o=bf2f(res[row*N+col])+g[col]*(acc+bf2f(bias[col]));
  C[row*N+col]=f2bf(o);
}
__global__ void k_attn(const u16* qkv,u16* o){
  int s=blockIdx.x; int h=threadIdx.x; if(h>=H) return;
  float q[16]; for(int d=0;d<16;d++) q[d]=bf2f(qkv[s*1536+h*16+d]);
  float lg[8]; float mx=-1e30f;
  for(int w=0;w<8;w++){ int t=s-2*w; float a=0; if(t>=0){ for(int d=0;d<16;d++) a+=q[d]*bf2f(qkv[t*1536+512+h*16+d]); a*=0.25f; } else a=-1e9f; lg[w]=a; mx=fmaxf(mx,a);}
  float sum=0; for(int w=0;w<8;w++){ lg[w]=__expf(lg[w]-mx); sum+=lg[w]; }
  for(int d=0;d<16;d++){ float a=0; for(int w=0;w<8;w++){ int t=s-2*w; if(t>=0) a+=lg[w]*bf2f(qkv[t*1536+1024+h*16+d]); } o[s*512+h*16+d]=f2bf(a/sum); }
}

extern "C" void kernel_launch(void* const* d_in, const int* in_sizes, int n_in, void* d_out, int out_size, void* d_ws, size_t ws_size, hipStream_t stream) {
  const u16* x=(const u16*)d_in[0]; const u16* wqkv=(const u16*)d_in[1]; const u16* wo=(const u16*)d_in[2];
  const u16* w1=(const u16*)d_in[4]; const u16* b1=(const u16*)d_in[5]; const u16* w2=(const u16*)d_in[6]; const u16* b2=(const u16*)d_in[7];
  const u16* c=(const u16*)d_in[8]; const u16* wmod=(const u16*)d_in[9];
  char* ws=(char*)d_ws;
  float* mod=(float*)ws; u16* h=(u16*)(ws+65536); u16* qkv=h+S*D; u16* o=qkv+S*1536; u16* x1=o+S*D; u16* y=x1+S*D; u16* h1=y+S*D;
  k_mod<<<12,256,0,stream>>>(c,wmod,mod);
  k_ln<<<S,64,0,stream>>>(x,mod,mod+512,h,0);
  k_gemm<<<dim3(1536/16,S/16),dim3(16,16),0,stream>>>(h,wqkv,qkv,S,1536,512,0,nullptr,nullptr,nullptr);
  k_attn<<<S,64,0,stream>>>(qkv,o);
  k_gemm<<<dim3(512/16,S/16),dim3(16,16),0,stream>>>(o,wo,x1,S,512,512,1,nullptr,mod+1024,x);
  k_ln<<<S,64,0,stream>>>(x1,mod+1536,mod+2048,y,1);
  k_gemm<<<dim3(F/16,S/16),dim3(16,16),0,stream>>>(y,w1,h1,S,F,512,2,b1,nullptr,nullptr);
  k_gemm<<<dim3(512/16,S/16),dim3(16,16),0,stream>>>(h1,w2,(u16*)d_out,S,512,F,3,b2,mod+2560,x1);
}
```

```cpp
#include <hip/hip_runtime.h>
#include <stdint.h>

#define GAS __attribute__((address_space(1)))
#define LAS __attribute__((address_space(3)))
#define DI __device__ __forceinline__
typedef unsigned short u16;
typedef unsigned v4u __attribute__((ext_vector_type(4)));
typedef unsigned v2u __attribute__((ext_vector_type(2)));
typedef float f4 __attribute__((ext_vector_type(4)));
typedef float f2 __attribute__((ext_vector_type(2)));
typedef __bf16 bf2 __attribute__((ext_vector_type(2)));
typedef __bf16 bf8 __attribute__((ext_vector_type(8)));
typedef short s4 __attribute__((ext_vector_type(4)));
typedef GAS unsigned gu32;
typedef GAS unsigned long long gu64;
#define RLX_AGENT __ATOMIC_RELAXED, __HIP_MEMORY_SCOPE_AGENT

constexpr unsigned OFF_E = 0;
constexpr unsigned OFF_RFLAG = 1024;
constexpr unsigned OFF_GFLAG = 4096;
constexpr unsigned OFF_MODG = 16384;
constexpr unsigned OFF_STAT = 32768;
constexpr unsigned OFF_W1T = 1u << 20;
constexpr unsigned OFF_W2T = OFF_W1T + 1536 * 1024;
constexpr unsigned OFF_W3T = OFF_W2T + 512 * 1024;
constexpr unsigned OFF_W4T = OFF_W3T + 1376 * 1024;
constexpr unsigned OFF_O = OFF_W4T + 1376 * 1024;
constexpr unsigned OFF_XA = OFF_O + 2048 * 1024;
constexpr unsigned OFF_H1 = OFF_XA + 2048 * 1024;
constexpr unsigned WS_USED = OFF_H1 + 32 * 43 * 4096;
constexpr unsigned OFF_STAMP = 64u << 20;

constexpr int L_PX = 81920;
constexpr int L_RED = 65536;
constexpr int L_STAT = 102400;
constexpr int L_MODB = 131072;
constexpr int L_ONES = L_MODB + 6144;
constexpr int L_ZERO = L_ONES + 1024;
constexpr int L_MRED = L_ZERO + 1024;
constexpr int L_COEF = L_MRED + 2048;
constexpr int L_RR = L_COEF + 1024;
constexpr int L_END = L_RR + 256;
constexpr int LDS_BYTES = L_END;

constexpr unsigned SPIN_CAP = 1u << 18;

#define ST(n)

DI float bflo(unsigned u) { return __uint_as_float(u << 16); }
DI float bfhi(unsigned u) { return __uint_as_float(u & 0xffff0000u); }
DI unsigned pk(float a, float b) { f2 v = {a, b}; return __builtin_bit_cast(unsigned, __builtin_convertvector(v, bf2)); }
DI f4 mfma32(v4u a, v4u b, f4 c) { return __builtin_amdgcn_mfma_f32_16x16x32_bf16(__builtin_bit_cast(bf8, a), __builtin_bit_cast(bf8, b), c, 0, 0, 0); }
DI f4 mfma16(v2u a, v2u b, f4 c) { return __builtin_amdgcn_mfma_f32_16x16x16bf16_1k(__builtin_bit_cast(s4, a), __builtin_bit_cast(s4, b), c, 0, 0, 0); }
template <int CTRL> DI float dpp(float x) { return __builtin_bit_cast(float, __builtin_amdgcn_update_dpp(0, __builtin_bit_cast(int, x), CTRL, 0xf, 0xf, true)); }
DI float swap16_sum(float x) { auto r = __builtin_amdgcn_permlane16_swap(__float_as_uint(x), __float_as_uint(x), false, false); return __uint_as_float(r[0]) + __uint_as_float(r[1]); }
DI float swap32_sum(float x) { auto r = __builtin_amdgcn_permlane32_swap(__float_as_uint(x), __float_as_uint(x), false, false); return __uint_as_float(r[0]) + __uint_as_float(r[1]); }
DI float swap16_max(float x) { auto r = __builtin_amdgcn_permlane16_swap(__float_as_uint(x), __float_as_uint(x), false, false); return fmaxf(__uint_as_float(r[0]), __uint_as_float(r[1])); }
DI float swap32_max(float x) { auto r = __builtin_amdgcn_permlane32_swap(__float_as_uint(x), __float_as_uint(x), false, false); return fmaxf(__uint_as_float(r[0]), __uint_as_float(r[1])); }
DI float sum8(float x) { x += dpp<0xB1>(x); x += dpp<0x4E>(x); x += dpp<0x141>(x); return x; }
DI float sum16(float x) { x = sum8(x); x += dpp<0x140>(x); return x; }

#define BAR() asm volatile("s_waitcnt lgkmcnt(0)\n\ts_barrier" ::: "memory")
#define WAITV(n) asm volatile("s_waitcnt vmcnt(%0)" ::"i"(n) : "memory")
#define WAITV_BAR(n) asm volatile("s_waitcnt vmcnt(%0) lgkmcnt(0)\n\ts_barrier" ::"i"(n) : "memory")
#define USE(x) asm volatile("" ::"v"(x))

template <int N> DI void dma(v4u rs, unsigned voff, unsigned soff, unsigned lds) {
  unsigned keep;
  if constexpr (N == 1)
    asm volatile("s_mov_b32 %0, m0\n\ts_mov_b32 m0, %2\n\ts_nop 0\n\t"
                 "buffer_load_dwordx4 %1, %3, %4 offen sc1 lds\n\t"
                 "s_mov_b32 m0, %0" : "=&s"(keep) : "v"(voff), "s"(lds), "s"(rs), "s"(soff) : "memory");
  else if constexpr (N == 2)
    asm volatile("s_mov_b32 %0, m0\n\ts_mov_b32 m0, %2\n\ts_nop 0\n\t"
                 "buffer_load_dwordx4 %1, %3, %4 offen sc1 lds\n\t"
                 "buffer_load_dwordx4 %1, %3, %4 offen offset:1024 sc1 lds\n\t"
                 "s_mov_b32 m0, %0" : "=&s"(keep) : "v"(voff), "s"(lds), "s"(rs), "s"(soff) : "memory");
  else
    asm volatile("s_mov_b32 %0, m0\n\ts_mov_b32 m0, %2\n\ts_nop 0\n\t"
                 "buffer_load_dwordx4 %1, %3, %4 offen sc1 lds\n\t"
                 "buffer_load_dwordx4 %1, %3, %4 offen offset:1024 sc1 lds\n\t"
                 "buffer_load_dwordx4 %1, %3, %4 offen offset:2048 sc1 lds\n\t"
                 "buffer_load_dwordx4 %1, %3, %4 offen offset:3072 sc1 lds\n\t"
                 "s_mov_b32 m0, %0" : "=&s"(keep) : "v"(voff), "s"(lds), "s"(rs), "s"(soff) : "memory");
}
DI void gdma(const GAS void* p, unsigned lds) {
  unsigned keep;
  asm volatile("s_mov_b32 %0, m0\n\ts_mov_b32 m0, %2\n\ts_nop 0\n\tglobal_load_lds_dwordx4 %1, off\n\ts_mov_b32 m0, %0"
               : "=&s"(keep) : "v"(p), "s"(lds) : "memory");
}
typedef __amdgpu_buffer_rsrc_t rsrc_t;
DI v4u ldf(rsrc_t rs, unsigned voff, unsigned soff) { return __builtin_amdgcn_raw_buffer_load_b128(rs, voff, soff, 16); }
DI void stf(rsrc_t rs, v4u v, unsigned voff, unsigned soff) { __builtin_amdgcn_raw_buffer_store_b128(v, rs, voff, soff, 16); }

DI void poll8(gu32* fl, unsigned tag, int l) {
  _Pragma("nounroll") for (unsigned s = 0; s < SPIN_CAP; ++s) {
    unsigned f = __hip_atomic_load(fl + (l & 7), RLX_AGENT);
    if (__all(f == tag)) break;
    __builtin_amdgcn_s_sleep(1);
  }
  __builtin_amdgcn_fence(__ATOMIC_ACQUIRE, "wavefront");
}

DI float gelu(float z) {
  const float k1 = -2.302208198f, k2 = -0.1029432397f;
  float e = __builtin_amdgcn_exp2f(z * (k1 + k2 * (z * z)));
  return z * __builtin_amdgcn_rcpf(1.0f + e);
}

template <int T>
DI void rest(rsrc_t rs, v4u rsv, LAS char* L, unsigned Lb, int g, int j, int w, int tid, int t03, int tile0, unsigned tag, gu32* gfl,
             const GAS u16* b1, u16* out_, v4u (&w2)[8], v4u xres, v4u b2v
) {
  const int l = tid & 63, i = l & 15, q = l >> 4, cc = w & 3, kh = w >> 2;
  const int etok = tid >> 3, ef = 64 * j + 8 * (tid & 7);
  const unsigned w3off = OFF_W3T + tile0 * 16384;
  v4u w3[16 * T];
  if (w >= 4) WAITV_BAR(0);
#pragma unroll
  for (int s = 0; s < 16; ++s)
#pragma unroll
    for (int t = 0; t < T; ++t) w3[s * T + t] = ldf(rs, l * 16, w3off + t * 16384 + ((s + 2 * j) & 15) * 1024);
  if (w < 4) asm volatile("s_barrier" ::: "memory");
  if (tid == 0) __hip_atomic_store(gfl + j, tag, RLX_AGENT);
  ST(7);
#pragma unroll
  for (int n = 0; n < 8; ++n) USE(w2[n]);
  if (w == 3) poll8(gfl, tag, l);
  BAR();
  ST(8);

#pragma unroll
  for (int m = 0; m < 4; ++m) {
    const int slot = 8 * kh + 2 * m + ((w >> 1) & 1);
    dma<2>(rsv, l * 16, OFF_O + (g * 16 + ((slot + 2 * j) & 15)) * 4096 + (w & 1) * 2048, Lb + slot * 4096 + (w & 1) * 2048);
  }
  float x1[8];
  {
    f4 acc[4];
#pragma unroll
    for (int r = 0; r < 4; ++r) acc[r] = f4{0.f, 0.f, 0.f, 0.f};
    const int ab = kh * 32768 + l * 16;
#pragma unroll
    for (int n = 0; n < 8; ++n) {
      if (n == 0) WAITV_BAR(6);
      if (n == 2) WAITV_BAR(4);
      if (n == 4) WAITV_BAR(2);
      if (n == 6) WAITV_BAR(0);
#pragma unroll
      for (int r = 0; r < 4; ++r) acc[r] = mfma32(w2[n], *(LAS v4u*)(L + ab + (n * 4 + r) * 1024), acc[r]);
    }
#pragma unroll
    for (int r = 0; r < 4; ++r) *(LAS f4*)(L + L_RED + kh * 17408 + (r * 16 + i) * 272 + cc * 64 + q * 16) = acc[r];
    BAR();
    const int ra = L_RED + etok * 272 + (tid & 7) * 32;
    const f4 y0 = *(LAS f4*)(L + ra) + *(LAS f4*)(L + ra + 17408), y1 = *(LAS f4*)(L + ra + 16) + *(LAS f4*)(L + ra + 17408 + 16);
    const v4u gav = *(LAS v4u*)(L + L_MODB + 2048 + ef * 2), sbv = *(LAS v4u*)(L + L_MODB + 3072 + ef * 2), a2v = *(LAS v4u*)(L + L_MODB + 4096 + ef * 2);
    float sx = 0, sxx = 0, sp = 0, spp = 0, spa = 0, sps = 0, p[8];
#pragma unroll
    for (int e = 0; e < 8; ++e) {
      const float y = e < 4 ? y0[e & 3] : y1[e & 3];
      const float xv = (e & 1) ? bfhi(xres[e >> 1]) : bflo(xres[e >> 1]), ga = (e & 1) ? bfhi(gav[e >> 1]) : bflo(gav[e >> 1]);
      const float a2 = (e & 1) ? bfhi(a2v[e >> 1]) : bflo(a2v[e >> 1]), sb = (e & 1) ? bfhi(sbv[e >> 1]) : bflo(sbv[e >> 1]);
      x1[e] = xv + ga * y;
      p[e] = x1[e] * a2;
      sx += x1[e]; sxx += x1[e] * x1[e]; sp += p[e]; spp += p[e] * p[e]; spa += p[e] * a2; sps += p[e] * sb;
    }
    stf(rs, v4u{pk(p[0], p[1]), pk(p[2], p[3]), pk(p[4], p[5]), pk(p[6], p[7])},
        (((tid & 7) >> 2) * 4 + (etok >> 4)) * 1024 + ((tid & 3) * 16 + (etok & 15)) * 16, OFF_XA + (g * 16 + 2 * j) * 4096);
    sx = sum8(sx); sxx = sum8(sxx); sp = sum8(sp); spp = sum8(spp); spa = sum8(spa); sps = sum8(sps);
    if ((tid & 7) == 0) stf(rs, __builtin_bit_cast(v4u, f4{sx, sxx, sp, spp}), etok * 16, OFF_STAT + (g * 8 + j) * 1536);
    if ((tid & 7) == 1) __builtin_amdgcn_raw_buffer_store_b64(__builtin_bit_cast(v2u, f2{spa, sps}), rs, etok * 8, OFF_STAT + (g * 8 + j) * 1536 + 1024, 16);
    WAITV_BAR(0);
    if (tid == 0) __hip_atomic_store(gfl + 32 * 32 + j, tag, RLX_AGENT);
  }
  ST(9);
  int rot4 = ((t03 + 1) >> 1) + 22 * kh;
  if (rot4 >= 43) rot4 -= 43;
  const int n4 = 22 - kh;
  const unsigned w4off = OFF_W4T + (4 * j + cc) * 43 * 1024;
  v4u w4[22];
  if (T == 1) {
#pragma unroll
    for (int n = 0; n < 22; ++n) {
      int k = rot4 + n; if (k >= 43) k -= 43;
      w4[n] = ldf(rs, l * 16, w4off + k * 1024);
    }
  }
  {
  float uvt[T];
  {
    const int ty = i >> 2;
    const int base = (ty == 0 ? L_MODB + 4096 : ty == 1 ? L_MODB + 3072 : ty == 2 ? L_ONES : L_ZERO) + q * 16;
    f4 a[T];
#pragma unroll
    for (int t = 0; t < T; ++t) {
      float bv = bflo(b1[(tile0 + t) * 16 + i]);
      a[t] = f4{q == 3 ? bv : 0.f, 0.f, 0.f, 0.f};
    }
#pragma unroll
    for (int s = 0; s < 16; ++s) {
      v4u am = *(LAS v4u*)(L + base + ((s + 2 * j) & 15) * 64);
#pragma unroll
      for (int t = 0; t < T; ++t) a[t] = mfma32(am, w3[s * T + t], a[t]);
    }
#pragma unroll
    for (int t = 0; t < T; ++t) uvt[t] = a[t].x;
  }
  ST(10);
  if (w == 3) poll8(gfl + 32 * 32, tag, l);
  BAR();
  ST(11);
#pragma unroll
  for (int m = 0; m < 4; ++m) {
    const int slot = 4 * m + (w >> 1);
    dma<2>(rsv, l * 16, OFF_XA + (g * 16 + ((slot + 2 * j) & 15)) * 4096 + (w & 1) * 2048, Lb + slot * 4096 + (w & 1) * 2048);
  }
  dma<1>(rsv, l * 16, OFF_STAT + (g * 8 + w) * 1536, Lb + L_STAT + w * 1536);
  if (l < 32) dma<1>(rsv, l * 16, OFF_STAT + (g * 8 + w) * 1536 + 1024, Lb + L_STAT + w * 1536 + 1024);
  f4 acc[4][T];
#pragma unroll
  for (int r = 0; r < 4; ++r)
#pragma unroll
    for (int t = 0; t < T; ++t) acc[r][t] = f4{0.f, 0.f, 0.f, 0.f};
#pragma unroll
  for (int s = 0; s < 16; ++s) {
    if (s == 0) WAITV_BAR(8);
    if (s == 4) WAITV_BAR(6 + (T == 2 ? 8 : 0));
    if (s == 8) WAITV_BAR(4 + (T == 2 ? 16 : 0));
    if (s == 12) WAITV_BAR(T == 2 ? 22 : 0);
    if (s == 12 && w == 3) {
      float sx = 0, sxx = 0, sp = 0, spp = 0, spa = 0, sps = 0;
#pragma unroll
      for (int p = 0; p < 8; ++p) {
        f4 a = *(LAS f4*)(L + L_STAT + p * 1536 + l * 16);
        f2 b = *(LAS f2*)(L + L_STAT + p * 1536 + 1024 + l * 8);
        sx += a.x; sxx += a.y; sp += a.z; spp += a.w; spa += b.x; sps += b.y;
      }
      v4u av = *(LAS v4u*)(L + L_MODB + 4096 + l * 16), sv = *(LAS v4u*)(L + L_MODB + 3072 + l * 16);
      float Sa = 0, Ss = 0, Saa = 0, Sas = 0, Sss = 0;
#pragma unroll
      for (int e = 0; e < 4; ++e) {
        float a0 = bflo(av[e]), a1 = bfhi(av[e]), s0 = bflo(sv[e]), s1 = bfhi(sv[e]);
        Sa += a0 + a1; Ss += s0 + s1; Saa += a0 * a0 + a1 * a1; Sas += a0 * s0 + a1 * s1; Sss += s0 * s0 + s1 * s1;
      }
      Sa = swap32_sum(swap16_sum(sum16(Sa))); Ss = swap32_sum(swap16_sum(sum16(Ss))); Saa = swap32_sum(swap16_sum(sum16(Saa)));
      Sas = swap32_sum(swap16_sum(sum16(Sas))); Sss = swap32_sum(swap16_sum(sum16(Sss)));
      const float inv = 1.0f / 512.0f;
      float m = sx * inv, var = fmaxf(sxx * inv - m * m, 0.f);
      float sd = __builtin_amdgcn_sqrtf(var + 1e-5f), r = __builtin_amdgcn_rcpf(sd);
      float m2 = (r * (sp - m * Sa) + Ss) * inv;
      float e2 = (r * r * (spp - 2.f * m * spa + m * m * Saa) + 2.f * r * (sps - m * Sas) + Sss) * inv;
      float sd2 = __builtin_amdgcn_sqrtf(fmaxf(e2 - m2 * m2, 0.f) + 1e-5f);
      *(LAS f4*)(L + L_COEF + l * 16) = f4{-m, sd, -m2 * sd, sd * sd2};
      *(LAS float*)(L + L_RR + l * 4) = r * __builtin_amdgcn_rcpf(sd2);
    }
#pragma unroll
    for (int r = 0; r < 4; ++r) {
      v4u a = *(LAS v4u*)(L + (s * 4 + r) * 1024 + l * 16);
#pragma unroll
      for (int t = 0; t < T; ++t) acc[r][t] = mfma32(w3[s * T + t], a, acc[r][t]);
    }
    if (T == 2 && s < 11) {
      int k0 = rot4 + 2 * s; if (k0 >= 43) k0 -= 43;
      int k1 = rot4 + 2 * s + 1; if (k1 >= 43) k1 -= 43;
      w4[2 * s] = ldf(rs, l * 16, w4off + k0 * 1024);
      w4[2 * s + 1] = ldf(rs, l * 16, w4off + k1 * 1024);
      __builtin_amdgcn_sched_barrier(0);
    }
  }
  BAR();
  ST(12);
  float cf[4], rr[4];
#pragma unroll
  for (int r = 0; r < 4; ++r) {
    cf[r] = *(LAS float*)(L + L_COEF + (r * 16 + i) * 16 + q * 4);
    rr[r] = *(LAS float*)(L + L_RR + (r * 16 + i) * 4);
  }
#pragma unroll
  for (int t = 0; t < T; ++t) {
    const int tl = tile0 + t;
#pragma unroll
    for (int pp = 0; pp < 2; ++pp) {
      unsigned h[2][2];
#pragma unroll
      for (int rr2 = 0; rr2 < 2; ++rr2) {
        const int r = 2 * pp + rr2;
        f4 z = __builtin_amdgcn_mfma_f32_16x16x4f32(uvt[t], cf[r], acc[r][t], 0, 0, 0);
        z *= rr[r];
        h[rr2][0] = pk(gelu(z.x), gelu(z.y));
        h[rr2][1] = pk(gelu(z.z), gelu(z.w));
      }
      auto s0 = __builtin_amdgcn_permlane16_swap(h[0][0], h[1][0], false, false);
      auto s1 = __builtin_amdgcn_permlane16_swap(h[0][1], h[1][1], false, false);
      v4u o = {s0[0], s1[0], s0[1], s1[1]};
      stf(rs, o, (q & 1) * 1024 + ((q >> 1) * 16 + i) * 16, OFF_H1 + ((g * 43 + (tl >> 1)) * 4 + 2 * pp) * 1024 + (tl & 1) * 512);
    }
  }
  }
  WAITV_BAR(0);
  if (tid == 0) __hip_atomic_store(gfl + 64 * 32 + j, tag, RLX_AGENT);
  ST(13);
#pragma unroll
  for (int n = 0; n < 22; ++n) USE(w4[n]);
  if (w == 3) poll8(gfl + 64 * 32, tag, l);
  BAR();
  ST(14);

  {
    const int r4 = ((t03 + 1) >> 1) + 22 * kh + (w & 3);
    auto stage = [&](int m) {
      int k = r4 + 4 * m; if (k >= 43) k -= 43; if (k >= 43) k -= 43;
      dma<4>(rsv, l * 16, OFF_H1 + (g * 43 + k) * 4096, Lb + (m & 3) * 32768 + w * 4096);
    };
    stage(0); stage(1); stage(2); stage(3);
    f4 acc[4];
#pragma unroll
    for (int r = 0; r < 4; ++r) acc[r] = f4{0.f, 0.f, 0.f, 0.f};
    const int ab = kh * 16384 + l * 16;
#pragma unroll
    for (int n = 0; n < 22; ++n) {
      const int m = n >> 2;
      if (n == 0) WAITV_BAR(12);
      if (n == 4) { WAITV_BAR(8); stage(4); }
      if (n == 8) { WAITV_BAR(8); if (20 + (w & 3) < n4) stage(5); }
      if (n == 12) WAITV_BAR(4);
      if (n == 16) WAITV_BAR(0);
      if (n < 21 || kh == 0) {
#pragma unroll
        for (int r = 0; r < 4; ++r) acc[r] = mfma32(w4[n], *(LAS v4u*)(L + ab + (m & 3) * 32768 + ((n & 3) * 4 + r) * 1024), acc[r]);
      }
    }
    ST(15);
#pragma unroll
    for (int r = 0; r < 4; ++r) *(LAS f4*)(L + L_RED + kh * 17408 + (r * 16 + i) * 272 + cc * 64 + q * 16) = acc[r];
    BAR();
    const int ra = L_RED + etok * 272 + (tid & 7) * 32;
    const f4 y0 = *(LAS f4*)(L + ra) + *(LAS f4*)(L + ra + 17408), y1 = *(LAS f4*)(L + ra + 16) + *(LAS f4*)(L + ra + 17408 + 16);
    const v4u gbv = *(LAS v4u*)(L + L_MODB + 5120 + ef * 2);
    float o[8];
#pragma unroll
    for (int e = 0; e < 8; ++e) {
      const float y = e < 4 ? y0[e & 3] : y1[e & 3];
      const float gb = (e & 1) ? bfhi(gbv[e >> 1]) : bflo(gbv[e >> 1]), bb = (e & 1) ? bfhi(b2v[e >> 1]) : bflo(b2v[e >> 1]);
      o[e] = x1[e] + gb * (y + bb);
    }
    *(GAS v4u*)((GAS u16*)out_ + (size_t)(64 * g + etok) * 512 + ef) = v4u{pk(o[0], o[1]), pk(o[2], o[3]), pk(o[4], o[5]), pk(o[6], o[7])};
  }
}

__global__ void __launch_bounds__(512, 2)
fused_block(const u16* __restrict__ x_, const u16* __restrict__ wqkv_, const u16* __restrict__ wo_, const u16* __restrict__ w1_,
            const u16* __restrict__ b1_, const u16* __restrict__ w2_, const u16* __restrict__ b2_, const u16* __restrict__ c_,
            const u16* __restrict__ wmod_, u16* __restrict__ out_, unsigned char* __restrict__ ws_) {
  extern __shared__ __attribute__((aligned(16))) char smem[];
  LAS char* L = (LAS char*)smem;
  const unsigned Lb = (unsigned)(uintptr_t)L;
  const int tid = threadIdx.x, l = tid & 63, w = __builtin_amdgcn_readfirstlane(tid >> 6);
  const int b = blockIdx.x, xcd = b & 7, idx = b >> 3, g = xcd * 4 + (idx >> 3), j = idx & 7;
  const int i = l & 15, q = l >> 4;
  const GAS u16* x = (const GAS u16*)x_;
  GAS unsigned char* ws = (GAS unsigned char*)ws_;
  const rsrc_t rs = __builtin_amdgcn_make_buffer_rsrc((void*)ws_, 0, WS_USED, 0x00020000);
  const v4u rsv = {(unsigned)(uintptr_t)ws_, (unsigned)((uintptr_t)ws_ >> 32) & 0xffffu, WS_USED, 0x00020000u};
  ST(0);
  const unsigned tag = *(const GAS unsigned*)(ws + OFF_E) + 1u;

  const bool modjob = b < 48;
  v4u cw, wm[8];
  if (modjob) {
    const int r0 = 64 * w + 8 * (l >> 3);
    cw = *(const GAS v4u*)((const GAS u16*)c_ + r0);
#pragma unroll
    for (int e = 0; e < 8; ++e) wm[e] = *(const GAS v4u*)((const GAS u16*)wmod_ + (size_t)(r0 + e) * 3072 + 64 * b + 8 * (l & 7));
  }
  const int u = b * 5 + w;
  const bool retile = w < 5 && u < 1208;
  int rdst = 0, rks = 16, rnt = 4;
  if (retile) {
    const GAS u16* src; int s, cb, n;
    if (u < 384) { src = (const GAS u16*)wqkv_; s = u & 15; cb = u >> 4; n = 1536; rdst = OFF_W1T; }
    else if (u < 512) { src = (const GAS u16*)wo_; s = u & 15; cb = (u - 384) >> 4; n = 512; rdst = OFF_W2T; }
    else if (u < 864) { src = (const GAS u16*)w1_; s = u & 15; cb = (u - 512) >> 4; n = 1376; rdst = OFF_W3T; if (cb == 21) rnt = 2; }
    else { src = (const GAS u16*)w2_; cb = (u - 864) / 43; s = (u - 864) - cb * 43; n = 512; rdst = OFF_W4T; rks = 43; }
    rdst += (cb * 4 * rks + s) * 1024;
    const int ch = rnt == 2 ? (l & 3) : (l & 7);
    const GAS u16* p = src + (size_t)(32 * s + (l >> 3)) * n + 64 * cb + 8 * ch;
#pragma unroll
    for (int e = 0; e < 4; ++e) gdma(p + (size_t)(8 * e) * n, Lb + w * 4096 + e * 1024);
  }
  v4u xr[5][2];
#pragma unroll
  for (int p = 0; p < 5; ++p) {
    int tok = 64 * g - 16 + 2 * (5 * w + p) + (l >> 5);
    tok = tok < 0 ? 0 : tok;
    xr[p][0] = *(const GAS v4u*)(x + (size_t)tok * 512 + 8 * (l & 31));
    xr[p][1] = *(const GAS v4u*)(x + (size_t)tok * 512 + 256 + 8 * (l & 31));
  }
  const int etok = tid >> 3, ef = 64 * j + 8 * (tid & 7);
  const v4u xres = *(const GAS v4u*)(x + (size_t)(64 * g + etok) * 512 + ef);
  const v4u b2v = *(const GAS v4u*)((const GAS u16*)b2_ + ef);
  if (tid < 64) *(LAS v4u*)(L + L_ONES + tid * 16) = v4u{0x3f803f80u, 0x3f803f80u, 0x3f803f80u, 0x3f803f80u};
  else if (tid < 128) *(LAS v4u*)(L + L_ZERO + (tid - 64) * 16) = v4u{0u, 0u, 0u, 0u};

  if (modjob) {
    float a[8];
#pragma unroll
    for (int k = 0; k < 8; ++k) a[k] = 0.f;
#pragma unroll
    for (int e = 0; e < 8; ++e) {
      float cv = (e & 1) ? bfhi(cw[e >> 1]) : bflo(cw[e >> 1]);
      float sv = cv * __builtin_amdgcn_rcpf(1.0f + __builtin_amdgcn_exp2f(-1.4426950409f * cv));
      sv = bflo(pk(sv, sv));
#pragma unroll
      for (int k = 0; k < 4; ++k) { a[2 * k] += sv * bflo(wm[e][k]); a[2 * k + 1] += sv * bfhi(wm[e][k]); }
    }
#pragma unroll
    for (int k = 0; k < 4; ++k) {
      auto r = __builtin_amdgcn_permlane32_swap(__float_as_uint(a[k]), __float_as_uint(a[k + 4]), false, false);
      a[k] = __uint_as_float(r[0]) + __uint_as_float(r[1]);
    }
#pragma unroll
    for (int k = 0; k < 2; ++k) {
      auto r = __builtin_amdgcn_permlane16_swap(__float_as_uint(a[k]), __float_as_uint(a[k + 2]), false, false);
      a[k] = __uint_as_float(r[0]) + __uint_as_float(r[1]);
      a[k] += dpp<0x128>(a[k]);
    }
    if (!(l & 8)) *(LAS f2*)(L + L_MRED + w * 256 + (8 * (l & 7) + 2 * ((l >> 4) & 1) + 4 * (l >> 5)) * 4) = f2{a[0], a[1]};
    BAR();
    if (tid < 32) {
      f2 s = {0.f, 0.f};
#pragma unroll
      for (int ww = 0; ww < 8; ++ww) s += *(LAS f2*)(L + L_MRED + ww * 256 + tid * 8);
      __hip_atomic_store((gu64*)(ws + OFF_MODG) + b * 32 + tid, ((unsigned long long)tag << 32) | pk(s.x, s.y), RLX_AGENT);
    }
  }
  ST(1);
  if (retile) {
    WAITV(12);
#pragma unroll
    for (int t = 0; t < 4; ++t) {
      if (t < rnt) {
        const int ad = w * 4096 + (8 * q + (i >> 2)) * 128 + t * 32 + (i & 3) * 8;
        v2u lo = __builtin_bit_cast(v2u, __builtin_amdgcn_ds_read_tr16_b64_v4i16((LAS s4*)(L + ad)));
        v2u hi = __builtin_bit_cast(v2u, __builtin_amdgcn_ds_read_tr16_b64_v4i16((LAS s4*)(L + ad + 512)));
        stf(rs, v4u{lo.x, lo.y, hi.x, hi.y}, l * 16, rdst + t * rks * 1024);
      }
    }
  }
  ST(2);
  float xn[5][16];
#pragma unroll
  for (int p = 0; p < 5; ++p) {
    float s = 0.f, ss = 0.f;
#pragma unroll
    for (int e = 0; e < 8; ++e) {
      const unsigned d = xr[p][e >> 2][e & 3];
      xn[p][2 * e] = bflo(d); xn[p][2 * e + 1] = bfhi(d);
      s += xn[p][2 * e] + xn[p][2 * e + 1];
      ss += xn[p][2 * e] * xn[p][2 * e] + xn[p][2 * e + 1] * xn[p][2 * e + 1];
    }
    s = swap16_sum(sum16(s)); ss = swap16_sum(sum16(ss));
    const float m = s * (1.0f / 512.0f);
    const float r = __builtin_amdgcn_rsqf(fmaxf(ss * (1.0f / 512.0f) - m * m, 0.f) + 1e-5f);
    const float mr = -m * r;
#pragma unroll
    for (int e = 0; e < 16; ++e) xn[p][e] = xn[p][e] * r + mr;
  }
  WAITV_BAR(0);
  if (tid == 0) __hip_atomic_store((gu32*)(ws + OFF_RFLAG) + b, tag, RLX_AGENT);
  ST(3);
  {
    const int nk = w < 4 ? 2 : 1;
    for (int kk = 0; kk < nk; ++kk) {
      const int kb = w + 8 * kk;
      v4u v;
      _Pragma("nounroll") for (unsigned s = 0; s < SPIN_CAP; ++s) {
        asm volatile("" ::: "memory");
        v = ldf(rs, l * 16, OFF_MODG + kb * 1024);
        if (__all(v.y == tag && v.w == tag)) break;
        __builtin_amdgcn_s_sleep(2);
      }
      const int sec = kb >> 1;
      if (sec == 1 || sec == 4) { v.x = pk(1.0f + bflo(v.x), 1.0f + bfhi(v.x)); v.z = pk(1.0f + bflo(v.z), 1.0f + bfhi(v.z)); }
      *(LAS v2u*)(L + L_MODB + kb * 512 + l * 8) = v2u{v.x, v.z};
    }
    if (w == 7) {
      _Pragma("nounroll") for (unsigned s = 0; s < SPIN_CAP; ++s) {
        asm volatile("" ::: "memory");
        v4u v = ldf(rs, l * 16, OFF_RFLAG);
        if (__all(v.x == tag && v.y == tag && v.z == tag && v.w == tag)) break;
        __builtin_amdgcn_s_sleep(2);
      }
    }
    __builtin_amdgcn_fence(__ATOMIC_ACQUIRE, "wavefront");
  }
  BAR();
  ST(4);
  if (b == 0 && tid == 0) *(GAS unsigned*)(ws + OFF_E) = tag;

  constexpr int DW = 5;
  const int rot1 = (2 * xcd + ((idx >> 4) & 1)) & 15;
  const int hd = 4 * j + (w & 3);
  const unsigned w1a = OFF_W1T + ((w < 4 ? 0 : 64) + hd) * 16384;
  v4u wa[16], wb[16];
#pragma unroll
  for (int s = 0; s < DW; ++s) {
    wa[s] = ldf(rs, l * 16, w1a + ((s + rot1) & 15) * 1024);
    if (w < 4) wb[s] = ldf(rs, l * 16, w1a + 32 * 16384 + ((s + rot1) & 15) * 1024);
  }
  __builtin_amdgcn_sched_barrier(0);
  {
    const int c = l & 31;
    v4u sav[2], cav[2];
#pragma unroll
    for (int hh = 0; hh < 2; ++hh) {
      sav[hh] = *(LAS v4u*)(L + L_MODB + (c + 32 * hh) * 16);
      cav[hh] = *(LAS v4u*)(L + L_MODB + 1024 + (c + 32 * hh) * 16);
    }
#pragma unroll
    for (int p = 0; p < 5; ++p) {
      const int r = 2 * (5 * w + p) + (l >> 5);
#pragma unroll
      for (int hh = 0; hh < 2; ++hh) {
        v4u o;
#pragma unroll
        for (int e = 0; e < 4; ++e)
          o[e] = pk(xn[p][8 * hh + 2 * e] * bflo(cav[hh][e]) + bflo(sav[hh][e]), xn[p][8 * hh + 2 * e + 1] * bfhi(cav[hh][e]) + bfhi(sav[hh][e]));
        const int slot = ((c >> 2) + 8 * hh - rot1) & 15, qq = c & 3;
        *(LAS v4u*)(L + (slot * 5 + (r >> 4)) * 1024 + (qq * 16 + ((r & 15) ^ (qq | ((slot & 1) << 2)))) * 16) = o;
      }
    }
  }
  BAR();
  ST(5);
  const int cc = w & 3, kh = w >> 2;
  const unsigned w2off = OFF_W2T + (4 * j + cc) * 16384;
  v4u w2[8];
  const int nt3 = j < 6 ? 11 : 10, t03 = j < 6 ? 11 * j : 66 + 10 * (j - 6);
  const bool two = w >= 4 && w < nt3 - 4;
  const int tile0 = t03 + (w < 4 ? w : (two ? 2 * w - 4 : nt3 - 8 + w));
  gu32* gfl = (gu32*)(ws + OFF_GFLAG) + g * 32;
  const int a0 = l * 16 ^ (q << 4), a1 = l * 16 ^ ((q | 4) << 4);
  if (w < 4) {
    f4 aq[4], ak[5];
#pragma unroll
    for (int r = 0; r < 5; ++r) { ak[r] = f4{0.f, 0.f, 0.f, 0.f}; if (r) aq[r - 1] = ak[r]; }
#pragma unroll
    for (int s = 0; s < 16; ++s) {
#pragma unroll
      for (int r = 0; r < 5; ++r) {
        v4u a = *(LAS v4u*)(L + (s * 5 + r) * 1024 + ((s & 1) ? a1 : a0));
        ak[r] = mfma32(wb[s], a, ak[r]);
        if (r) aq[r - 1] = mfma32(wa[s], a, aq[r - 1]);
      }
      if (s + DW < 16) {
        wa[s + DW] = ldf(rs, l * 16, w1a + ((s + DW + rot1) & 15) * 1024);
        wb[s + DW] = ldf(rs, l * 16, w1a + 32 * 16384 + ((s + DW + rot1) & 15) * 1024);
      } else if (s + DW < 20) {
        const int n = 2 * (s + DW - 16);
        w2[n] = ldf(rs, l * 16, w2off + ((8 * kh + n + 2 * j) & 15) * 1024);
        w2[n + 1] = ldf(rs, l * 16, w2off + ((8 * kh + n + 1 + 2 * j) & 15) * 1024);
      }
      __builtin_amdgcn_sched_barrier(0);
    }
    ST(6);
    v2u qb[4], kb[5];
#pragma unroll
    for (int r = 0; r < 5; ++r) {
      kb[r] = v2u{pk(ak[r].x, ak[r].y), pk(ak[r].z, ak[r].w)};
      if (r) qb[r - 1] = v2u{pk(aq[r - 1].x, aq[r - 1].y), pk(aq[r - 1].z, aq[r - 1].w)};
    }
    const bool odd = i & 1;
    const bool cur0 = 4 * q + (i & 1) <= i, cur1 = 4 * q + 2 + (i & 1) <= i;
    const float sc = 0.25f * 1.4426950409f;
#pragma unroll
    for (int r = 0; r < 4; ++r) {
      const f4 z = {0.f, 0.f, 0.f, 0.f};
      f4 sp = mfma16(kb[r], qb[r], z), scu = mfma16(kb[r + 1], qb[r], z);
      float p0 = odd ? sp.y : sp.x, p1 = odd ? sp.w : sp.z, c0 = odd ? scu.y : scu.x, c1 = odd ? scu.w : scu.z;
      if (r == 0 && g == 0) { p0 = -4e9f; p1 = -4e9f; }
      float v0 = cur0 ? c0 : p0, v1 = cur1 ? c1 : p1;
      const float mx = swap32_max(swap16_max(fmaxf(v0, v1)));
      v0 = __builtin_amdgcn_exp2f((v0 - mx) * sc); v1 = __builtin_amdgcn_exp2f((v1 - mx) * sc);
      const float rs_ = __builtin_amdgcn_rcpf(swap32_sum(swap16_sum(v0 + v1)));
      const unsigned pp = pk(v0 * rs_, v1 * rs_);
      const unsigned d0 = odd ? pp << 16 : pp & 0xffffu, d1 = odd ? pp & 0xffff0000u : pp >> 16;
      *(LAS v4u*)(L + L_PX + (w * 4 + r) * 1024 + l * 16) = v4u{cur0 ? 0u : d0, cur1 ? 0u : d1, cur0 ? d0 : 0u, cur1 ? d1 : 0u};
    }
    BAR();
  } else {
    f4 av[5];
#pragma unroll
    for (int r = 0; r < 5; ++r) av[r] = f4{0.f, 0.f, 0.f, 0.f};
#pragma unroll
    for (int s = 0; s < 16; ++s) {
#pragma unroll
      for (int r = 0; r < 5; ++r) {
        v4u a = *(LAS v4u*)(L + (s * 5 + r) * 1024 + ((s & 1) ? a1 : a0));
        av[r] = mfma32(a, wa[s], av[r]);
      }
      if (s + DW < 16) {
        wa[s + DW] = ldf(rs, l * 16, w1a + ((s + DW + rot1) & 15) * 1024);
      } else if (s + DW < 20) {
        const int n = 2 * (s + DW - 16);
        w2[n] = ldf(rs, l * 16, w2off + ((8 * kh + n + 2 * j) & 15) * 1024);
        w2[n + 1] = ldf(rs, l * 16, w2off + ((8 * kh + n + 1 + 2 * j) & 15) * 1024);
      }
      __builtin_amdgcn_sched_barrier(0);
    }
    ST(6);
    v2u vb[5];
#pragma unroll
    for (int r = 0; r < 5; ++r) vb[r] = v2u{pk(av[r].x, av[r].y), pk(av[r].z, av[r].w)};
    BAR();
    __builtin_amdgcn_s_setprio(2);
#pragma unroll
    for (int pp = 0; pp < 2; ++pp) {
      unsigned h[2][2];
#pragma unroll
      for (int r2 = 0; r2 < 2; ++r2) {
        const int r = 2 * pp + r2;
        v4u pf = *(LAS v4u*)(L + L_PX + ((w - 4) * 4 + r) * 1024 + l * 16);
        f4 o = mfma32(v4u{vb[r].x, vb[r].y, vb[r + 1].x, vb[r + 1].y}, pf, f4{0.f, 0.f, 0.f, 0.f});
        h[r2][0] = pk(o.x, o.y); h[r2][1] = pk(o.z, o.w);
      }
      auto s0 = __builtin_amdgcn_permlane16_swap(h[0][0], h[1][0], false, false);
      auto s1 = __builtin_amdgcn_permlane16_swap(h[0][1], h[1][1], false, false);
      stf(rs, v4u{s0[0], s1[0], s0[1], s1[1]}, (q & 1) * 1024 + ((q >> 1) * 16 + i) * 16,
          OFF_O + ((g * 16 + (hd >> 1)) * 4 + 2 * pp) * 1024 + (hd & 1) * 512);
    }
    __builtin_amdgcn_s_setprio(0);
  }
  if (two) rest<2>(rs, rsv, L, Lb, g, j, w, tid, t03, tile0, tag, gfl, (const GAS u16*)b1_, out_, w2, xres, b2v);
  else rest<1>(rs, rsv, L, Lb, g, j, w, tid, t03, tile0, tag, gfl, (const GAS u16*)b1_, out_, w2, xres, b2v);
  ST(16);
}

extern "C" void kernel_launch(void* const* d_in, const int* in_sizes, int n_in, void* d_out, int out_size, void* d_ws, size_t ws_size,
                              hipStream_t stream) {
  (void)hipFuncSetAttribute((const void*)fused_block, hipFuncAttributeMaxDynamicSharedMemorySize, LDS_BYTES);
  fused_block<<<256, 512, LDS_BYTES, stream>>>((const u16*)d_in[0], (const u16*)d_in[1], (const u16*)d_in[2], (const u16*)d_in[4],
                                               (const u16*)d_in[5], (const u16*)d_in[6], (const u16*)d_in[7], (const u16*)d_in[8],
                                               (const u16*)d_in[9], (u16*)d_out, (unsigned char*)d_ws);
}
```

```cpp
#include <hip/hip_runtime.h>
#include <stdint.h>

#define GAS __attribute__((address_space(1)))
#define LAS __attribute__((address_space(3)))
#define DI __device__ __forceinline__
typedef unsigned short u16;
typedef unsigned v4u __attribute__((ext_vector_type(4)));
typedef unsigned v2u __attribute__((ext_vector_type(2)));
typedef float f4 __attribute__((ext_vector_type(4)));
typedef float f2 __attribute__((ext_vector_type(2)));
typedef __bf16 bf2 __attribute__((ext_vector_type(2)));
typedef __bf16 bf8 __attribute__((ext_vector_type(8)));
typedef short s4 __attribute__((ext_vector_type(4)));
typedef GAS unsigned gu32;
typedef GAS unsigned long long gu64;
#define RLX_AGENT __ATOMIC_RELAXED, __HIP_MEMORY_SCOPE_AGENT

constexpr unsigned OFF_E = 0;
constexpr unsigned OFF_RFLAG = 1024;
constexpr unsigned OFF_GFLAG = 4096;
constexpr unsigned OFF_MODP = 16384;
constexpr unsigned OFF_STAT = 65536;
constexpr unsigned OFF_W2T = 1u << 20;
constexpr unsigned OFF_W3T = OFF_W2T + 512 * 1024;
constexpr unsigned OFF_W4T = OFF_W3T + 1376 * 1024;
constexpr unsigned OFF_O = OFF_W4T + 1376 * 1024;
constexpr unsigned OFF_XA = OFF_O + 2048 * 1024;
constexpr unsigned OFF_H1 = OFF_XA + 2048 * 1024;
constexpr unsigned WS_USED = OFF_H1 + 32 * 43 * 4096;
constexpr unsigned OFF_STAMP = 64u << 20;

constexpr int L_RT = 49152;
constexpr int L_R1 = 81920;
constexpr int L_RED = 65536;
constexpr int L_STAT = 102400;
constexpr int L_MODF = 155648;
constexpr int L_MODB = L_MODF + 4096 - 2048;
constexpr int L_MRED = 77824;
constexpr int L_COEF = L_MODF;
constexpr int L_RR = L_COEF + 1024;
constexpr int LDS_BYTES = 163840;

constexpr unsigned SPIN_CAP = 1u << 18;

#define ST(n)
#define SL(n)
#define SLF()

DI float bflo(unsigned u) { return __uint_as_float(u << 16); }
DI float bfhi(unsigned u) { return __uint_as_float(u & 0xffff0000u); }
DI unsigned pk(float a, float b) { f2 v = {a, b}; return __builtin_bit_cast(unsigned, __builtin_convertvector(v, bf2)); }
DI f2 unpk(unsigned u) { return f2{bflo(u), bfhi(u)}; }
DI f2 fma2(f2 a, f2 b, f2 c) { return __builtin_elementwise_fma(a, b, c); }
DI unsigned pk2(f2 v) { return __builtin_bit_cast(unsigned, __builtin_convertvector(v, bf2)); }
DI f4 mfma32(v4u a, v4u b, f4 c) { return __builtin_amdgcn_mfma_f32_16x16x32_bf16(__builtin_bit_cast(bf8, a), __builtin_bit_cast(bf8, b), c, 0, 0, 0); }
DI f4 mfma16(v2u a, v2u b, f4 c) { return __builtin_amdgcn_mfma_f32_16x16x16bf16_1k(__builtin_bit_cast(s4, a), __builtin_bit_cast(s4, b), c, 0, 0, 0); }
template <int CTRL> DI float dpp(float x) { return __builtin_bit_cast(float, __builtin_amdgcn_update_dpp(0, __builtin_bit_cast(int, x), CTRL, 0xf, 0xf, true)); }
DI float swap16_sum(float x) { auto r = __builtin_amdgcn_permlane16_swap(__float_as_uint(x), __float_as_uint(x), false, false); return __uint_as_float(r[0]) + __uint_as_float(r[1]); }
DI float swap32_sum(float x) { auto r = __builtin_amdgcn_permlane32_swap(__float_as_uint(x), __float_as_uint(x), false, false); return __uint_as_float(r[0]) + __uint_as_float(r[1]); }
DI float swap16_max(float x) { auto r = __builtin_amdgcn_permlane16_swap(__float_as_uint(x), __float_as_uint(x), false, false); return fmaxf(__uint_as_float(r[0]), __uint_as_float(r[1])); }
DI float swap32_max(float x) { auto r = __builtin_amdgcn_permlane32_swap(__float_as_uint(x), __float_as_uint(x), false, false); return fmaxf(__uint_as_float(r[0]), __uint_as_float(r[1])); }
DI float sum8(float x) { x += dpp<0xB1>(x); x += dpp<0x4E>(x); x += dpp<0x141>(x); return x; }
DI float sum16(float x) { x = sum8(x); x += dpp<0x140>(x); return x; }

#define BAR() asm volatile("s_waitcnt lgkmcnt(0)\n\ts_barrier" ::: "memory")
#define WAITV(n) asm volatile("s_waitcnt vmcnt(%0)" ::"i"(n) : "memory")
#define WAITV_BAR(n) asm volatile("s_waitcnt vmcnt(%0) lgkmcnt(0)\n\ts_barrier" ::"i"(n) : "memory")
#define USE(x) asm volatile("" ::"v"(x))

template <int N> DI void dma(v4u rs, unsigned voff, unsigned soff, unsigned lds) {
  unsigned keep;
  if constexpr (N == 1)
    asm volatile("s_mov_b32 %0, m0\n\ts_mov_b32 m0, %2\n\ts_nop 0\n\t"
                 "buffer_load_dwordx4 %1, %3, %4 offen sc1 lds\n\t"
                 "s_mov_b32 m0, %0" : "=&s"(keep) : "v"(voff), "s"(lds), "s"(rs), "s"(soff) : "memory");
  else if constexpr (N == 2)
    asm volatile("s_mov_b32 %0, m0\n\ts_mov_b32 m0, %2\n\ts_nop 0\n\t"
                 "buffer_load_dwordx4 %1, %3, %4 offen sc1 lds\n\t"
                 "buffer_load_dwordx4 %1, %3, %4 offen offset:1024 sc1 lds\n\t"
                 "s_mov_b32 m0, %0" : "=&s"(keep) : "v"(voff), "s"(lds), "s"(rs), "s"(soff) : "memory");
  else
    asm volatile("s_mov_b32 %0, m0\n\ts_mov_b32 m0, %2\n\ts_nop 0\n\t"
                 "buffer_load_dwordx4 %1, %3, %4 offen sc1 lds\n\t"
                 "buffer_load_dwordx4 %1, %3, %4 offen offset:1024 sc1 lds\n\t"
                 "buffer_load_dwordx4 %1, %3, %4 offen offset:2048 sc1 lds\n\t"
                 "buffer_load_dwordx4 %1, %3, %4 offen offset:3072 sc1 lds\n\t"
                 "s_mov_b32 m0, %0" : "=&s"(keep) : "v"(voff), "s"(lds), "s"(rs), "s"(soff) : "memory");
}
DI void dma3(v4u rs, unsigned voff, unsigned soff, unsigned lds) {
  unsigned keep;
  asm volatile("s_mov_b32 %0, m0\n\ts_mov_b32 m0, %2\n\ts_nop 0\n\t"
               "buffer_load_dwordx4 %1, %3, %4 offen lds\n\t"
               "buffer_load_dwordx4 %1, %3, %4 offen offset:1024 lds\n\t"
               "buffer_load_dwordx4 %1, %3, %4 offen offset:2048 lds\n\t"
               "s_mov_b32 m0, %0" : "=&s"(keep) : "v"(voff), "s"(lds), "s"(rs), "s"(soff) : "memory");
}
DI v4u trfrag(LAS char* p) {
  v2u lo = __builtin_bit_cast(v2u, __builtin_amdgcn_ds_read_tr16_b64_v4i16((LAS s4*)p));
  v2u hi = __builtin_bit_cast(v2u, __builtin_amdgcn_ds_read_tr16_b64_v4i16((LAS s4*)(p + 512)));
  return v4u{lo.x, lo.y, hi.x, hi.y};
}
DI int lane_id() { int r; asm volatile("v_mbcnt_lo_u32_b32 %0, -1, 0\n\tv_mbcnt_hi_u32_b32 %0, -1, %0" : "=v"(r)); return r; }
DI int fresh(int x) { asm volatile("" : "+v"(x)); return x; }
#define SB() __builtin_amdgcn_sched_barrier(0)
DI void gdma(const GAS void* p, unsigned lds) {
  unsigned keep;
  asm volatile("s_mov_b32 %0, m0\n\ts_mov_b32 m0, %2\n\ts_nop 0\n\tglobal_load_lds_dwordx4 %1, off\n\ts_mov_b32 m0, %0"
               : "=&s"(keep) : "v"(p), "s"(lds) : "memory");
}
typedef __amdgpu_buffer_rsrc_t rsrc_t;
DI v4u ldf(rsrc_t rs, unsigned voff, unsigned soff) { return __builtin_amdgcn_raw_buffer_load_b128(rs, voff, soff, 16); }
DI void stf(rsrc_t rs, v4u v, unsigned voff, unsigned soff) { __builtin_amdgcn_raw_buffer_store_b128(v, rs, voff + soff, 0, 16); }

DI void poll8(gu32* fl, unsigned tag, int l) {
  _Pragma("nounroll") for (unsigned s = 0; s < SPIN_CAP; ++s) {
    unsigned f = __hip_atomic_load(fl + (l & 7), RLX_AGENT);
    if (__all(f == tag)) break;
    __builtin_amdgcn_s_sleep(1);
  }
  __builtin_amdgcn_fence(__ATOMIC_ACQUIRE, "wavefront");
}

DI unsigned gelu2(f2 a, f2 k1, f2 k2, f2 ir) {
  const f2 t = a * fma2(a * a, k2, k1);
  const f2 d = fma2(f2{__builtin_amdgcn_exp2f(t.x), __builtin_amdgcn_exp2f(t.y)}, ir, ir);
  return pk2(a * f2{__builtin_amdgcn_rcpf(d.x), __builtin_amdgcn_rcpf(d.y)});
}

template <int N, bool F32> DI void mod_finish(rsrc_t rs, LAS char* L, const int (&cb)[N], v4u (&v)[N], unsigned tb, int l) {
  _Pragma("nounroll") for (unsigned s = 0; s < SPIN_CAP; ++s) {
    unsigned d = 0;
#pragma unroll
    for (int n = 0; n < N; ++n) d |= (v[n].x ^ tb) | (v[n].y ^ tb) | (v[n].z ^ tb) | (v[n].w ^ tb);
    if (__all(d << 24 == 0u)) break;
    __builtin_amdgcn_s_sleep(1);
    asm volatile("" ::: "memory");
#pragma unroll
    for (int n = 0; n < N; ++n) v[n] = ldf(rs, l * 16, OFF_MODP + cb[n] * 1024);
  }
#pragma unroll
  for (int n = 0; n < N; ++n) {
    float a[2];
#pragma unroll
    for (int k = 0; k < 2; ++k) {
      auto r = __builtin_amdgcn_permlane32_swap(v[n][k], v[n][k + 2], false, false);
      a[k] = swap16_sum(__uint_as_float(r[0]) + __uint_as_float(r[1]));
    }
    f2 m = unpk(pk(a[0], a[1]));
    const float one = ((cb[n] >> 3) == 1 || (cb[n] >> 3) == 4) ? 1.0f : 0.0f;
    const unsigned p = pk(m.x + one, m.y + one);
    if (!(l & 16)) {
      if (F32) *(LAS f2*)(L + L_MODF + cb[n] * 256 + (l & 15) * 16 + (l >> 5) * 8) = unpk(p);
      else *(LAS unsigned*)(L + L_MODB + cb[n] * 128 + (l & 15) * 8 + (l >> 5) * 4) = p;
    }
  }
}
template <int N, bool F32> DI void mod_fetch(rsrc_t rs, LAS char* L, const int (&cb)[N], unsigned tb, int l) {
  v4u v[N];
#pragma unroll
  for (int n = 0; n < N; ++n) v[n] = ldf(rs, l * 16, OFF_MODP + cb[n] * 1024);
  mod_finish<N, F32>(rs, L, cb, v, tb, l);
}

#define STP
#define STA
DI float fma1(float a, float b, float c) { float r; asm("v_fma_f32 %0, %1, %2, %3" : "=v"(r) : "v"(a), "v"(b), "v"(c)); return r; }
#define W1POS(s) ((s) < 6 ? L_R1 + (s) * 12288 : (s) < 10 ? ((s) - 6) * 12288 : 65536)
#define SBAR() asm volatile("s_barrier" ::: "memory")

struct Ctx {
  rsrc_t rs; v4u rsv; LAS char* L; unsigned Lb, tag; int g, j, w, t03, tile0; gu32* gfl;
  const u16 *x_, *b1_, *b2_; u16* out_;
};

template <int T> DI void col_terms(const Ctx& c, int l, const v4u (&w3a)[16], const v4u (&w3b)[16], float b1a, float b1b, float (&uvt)[T]) {
  l = fresh(l);
  const int i = l & 15, q = l >> 4, ty = i >> 2;
  const int base = L_MODB + (ty == 1 ? 3072 : 4096) + q * 16;
  const unsigned fill = ty == 2 ? 0x3f803f80u : 0u;
  f4 a[T];
  a[0] = f4{q == 3 ? b1a : 0.f, 0.f, 0.f, 0.f};
  if (T == 2) a[T - 1] = f4{q == 3 ? b1b : 0.f, 0.f, 0.f, 0.f};
#pragma unroll
  for (int h = 0; h < 4; ++h) {
    v4u am[4];
#pragma unroll
    for (int s = 0; s < 4; ++s) am[s] = *(LAS v4u*)(c.L + base + ((4 * h + s + 2 * c.j) & 15) * 64);
#pragma unroll
    for (int s = 0; s < 4; ++s) {
      if (ty >= 2) am[s] = v4u{fill, fill, fill, fill};
      a[0] = mfma32(am[s], w3a[4 * h + s], a[0]);
      if (T == 2) a[T - 1] = mfma32(am[s], w3b[4 * h + s], a[T - 1]);
    }
  }
#pragma unroll
  for (int t = 0; t < T; ++t) uvt[t] = a[t].x;
}

DI void epi2(const Ctx& c, int tid, v4u xres, f2 (&x1)[4]) {
  LAS char* L = c.L;
  tid = fresh(tid);
  const int etok = tid >> 3, ef = 64 * c.j + 8 * (tid & 7);
  const v4u gav = *(LAS v4u*)(L + L_MODB + 2048 + ef * 2), sbv = *(LAS v4u*)(L + L_MODB + 3072 + ef * 2), a2v = *(LAS v4u*)(L + L_MODB + 4096 + ef * 2);
  BAR();
  const int ra = L_RED + etok * 272 + (tid & 7) * 32;
  const f4 y0 = *(LAS f4*)(L + ra) + *(LAS f4*)(L + ra + 17408), y1 = *(LAS f4*)(L + ra + 16) + *(LAS f4*)(L + ra + 17408 + 16);
  f2 p[4], a2[4];
#pragma unroll
  for (int e = 0; e < 4; ++e) {
    const f2 y = e == 0 ? f2{y0.x, y0.y} : e == 1 ? f2{y0.z, y0.w} : e == 2 ? f2{y1.x, y1.y} : f2{y1.z, y1.w};
    a2[e] = unpk(a2v[e]);
    x1[e] = fma2(unpk(gav[e]), y, unpk(xres[e]));
    p[e] = x1[e] * a2[e];
  }
  stf(c.rs, v4u{pk2(p[0]), pk2(p[1]), pk2(p[2]), pk2(p[3])},
      (((tid & 7) >> 2) * 4 + (etok >> 4)) * 1024 + ((tid & 3) * 16 + (etok & 15)) * 16, OFF_XA + (c.g * 16 + 2 * c.j) * 4096);
  SB();
  f2 vx = x1[0], vxx = x1[0] * x1[0], vp = p[0], vpp = p[0] * p[0], vpa = p[0] * a2[0], vps = p[0] * unpk(sbv[0]);
#pragma unroll
  for (int e = 1; e < 4; ++e) {
    vx += x1[e]; vxx = fma2(x1[e], x1[e], vxx); vp += p[e]; vpp = fma2(p[e], p[e], vpp); vpa = fma2(p[e], a2[e], vpa); vps = fma2(p[e], unpk(sbv[e]), vps);
  }
  const float sx = sum8(vx.x + vx.y), sxx = sum8(vxx.x + vxx.y), sp = sum8(vp.x + vp.y), spp = sum8(vpp.x + vpp.y), spa = sum8(vpa.x + vpa.y), sps = sum8(vps.x + vps.y);
  const bool first = (tid & 7) == 0;
  if ((tid & 7) < 2) stf(c.rs, __builtin_bit_cast(v4u, first ? f4{sx, sxx, sp, spp} : f4{spa, sps, spa, sps}), etok * 16 + (first ? 0 : 1024), OFF_STAT + (c.g * 8 + c.j) * 2048);
}

template <int T, int KIND>
DI void mlp1_loop(const Ctx& c, int l, const v4u (&w3a)[16], const v4u (&w3b)[16], f4 (&acc)[4][T], v4u (&w4)[44], unsigned w4off, int rot4,
                  const float (&S)[5] STP) {
  LAS char* L = c.L;
  const int tid = c.w * 64 + l;
  auto xa_slot = [&](int slot) { dma<4>(c.rsv, l * 16, OFF_XA + (c.g * 16 + ((slot + 2 * c.j) & 15)) * 4096, c.Lb + slot * 4096); };
  v4u a[2][4];
#pragma unroll
  for (int r = 0; r < 4; ++r) a[0][r] = *(LAS v4u*)(L + r * 1024 + l * 16);
#pragma unroll
  for (int s = 0; s < 16; ++s) {
    if (s == 1 || s == 5 || s == 9 || s == 13) {
      if (KIND == 2 && c.w == 3) {
        if (s == 1) {
          poll8(c.gfl + 32 * 32, c.tag, l);
          ST(11);
          __builtin_amdgcn_s_setprio(2);
#pragma unroll
          for (int k = 2; k < 10; ++k) xa_slot(k);
          WAITV(16);
        }
        if (s == 5) WAITV(40);
        if (s == 9) WAITV(24);
        if (s == 13) WAITV(0);
      }
      SBAR();
      SL(22 + s / 4);
      if (KIND == 2 && c.w == 3) {
        if (s == 1) {
#pragma unroll
          for (int k = 10; k < 16; ++k) xa_slot(k);
#pragma unroll
          for (int p = 0; p < 4; ++p) dma<4>(c.rsv, l * 16, OFF_STAT + c.g * 16384 + p * 4096, c.Lb + L_STAT + p * 4096);
          __builtin_amdgcn_s_setprio(0);
        }
        if (s == 13) {
          float sx = 0, sxx = 0, sp = 0, spp = 0, spa = 0, sps = 0;
#pragma unroll
          for (int p = 0; p < 8; ++p) {
            const f4 a4 = *(LAS f4*)(L + L_STAT + p * 2048 + l * 16);
            const f2 b = *(LAS f2*)(L + L_STAT + p * 2048 + 1024 + l * 16);
            sx += a4.x; sxx += a4.y; sp += a4.z; spp += a4.w; spa += b.x; sps += b.y;
          }
          const float inv = 1.0f / 512.0f;
          const float m = sx * inv, var = fmaxf(sxx * inv - m * m, 0.f);
          const float sd = __builtin_amdgcn_sqrtf(var + 1e-5f), r = __builtin_amdgcn_rcpf(sd);
          const float m2 = (r * (sp - m * S[0]) + S[1]) * inv;
          const float e2 = (r * r * (spp - 2.f * m * spa + m * m * S[2]) + 2.f * r * (sps - m * S[3]) + S[4]) * inv;
          const float sd2 = __builtin_amdgcn_sqrtf(fmaxf(e2 - m2 * m2, 0.f) + 1e-5f);
          const float R = r * __builtin_amdgcn_rcpf(sd2), k1 = -2.302208198f * R;
          *(LAS f4*)(L + L_COEF + l * 16) = f4{-m, sd, -m2 * sd, sd * sd2};
          *(LAS f4*)(L + L_RR + l * 16) = f4{k1, 0.044715f * k1 * R * R, sd * sd2, 0.f};
        }
      }
    }
    SB();
#pragma unroll
    for (int r = 0; r < 4; ++r) {
      acc[r][0] = mfma32(w3a[s], a[s & 1][r], s ? acc[r][0] : f4{0.f, 0.f, 0.f, 0.f});
      SB();
      if (s < 15) a[(s + 1) & 1][r] = *(LAS v4u*)(L + ((s + 1) * 4 + r) * 1024 + l * 16);
      SB();
      if (T == 2) { acc[r][T - 1] = mfma32(w3b[s], a[s & 1][r], s ? acc[r][T - 1] : f4{0.f, 0.f, 0.f, 0.f}); SB(); }
    }
    if (KIND == 1 && s >= 2 && s < 14) {
      const int n = 4 + (s - 2);
      int k = rot4 + 2 * n; if (k >= 43) k -= 43;
      w4[2 * n] = ldf(c.rs, l * 16, w4off + k * 1024);
      w4[2 * n + 1] = ldf(c.rs, l * 16, w4off + 43 * 1024 + k * 1024);
      SB();
    }
  }
}

template <int T> DI void mlp1_epi(const Ctx& c, int l, const float (&uvt)[T], f4 (&acc)[4][T]) {
  l = fresh(l);
  const int i = l & 15, q = l >> 4;
  float cf[4]; f4 rr[4];
#pragma unroll
  for (int r = 0; r < 4; ++r) {
    cf[r] = *(LAS float*)(c.L + L_COEF + (r * 16 + i) * 16 + q * 4);
    rr[r] = *(LAS f4*)(c.L + L_RR + (r * 16 + i) * 16);
  }
#pragma unroll
  for (int t = 0; t < T; ++t)
#pragma unroll
    for (int r = 0; r < 4; ++r) acc[r][t] = __builtin_amdgcn_mfma_f32_16x16x4f32(uvt[t], cf[r], acc[r][t], 0, 0, 0);
#pragma unroll
  for (int t = 0; t < T; ++t) {
    const int tl = c.tile0 + t;
#pragma unroll
    for (int pp = 0; pp < 2; ++pp) {
      unsigned h[2][2];
#pragma unroll
      for (int r2 = 0; r2 < 2; ++r2) {
        const int r = 2 * pp + r2;
        const f2 k1 = {rr[r].x, rr[r].x}, k2 = {rr[r].y, rr[r].y}, ir = {rr[r].z, rr[r].z};
        h[r2][0] = gelu2(f2{acc[r][t].x, acc[r][t].y}, k1, k2, ir);
        h[r2][1] = gelu2(f2{acc[r][t].z, acc[r][t].w}, k1, k2, ir);
      }
      auto s0 = __builtin_amdgcn_permlane16_swap(h[0][0], h[1][0], false, false);
      auto s1 = __builtin_amdgcn_permlane16_swap(h[0][1], h[1][1], false, false);
      stf(c.rs, v4u{s0[0], s1[0], s0[1], s1[1]}, (q & 1) * 1024 + ((q >> 1) * 16 + i) * 16,
          OFF_H1 + ((c.g * 43 + (tl >> 1)) * 4 + 2 * pp) * 1024 + (tl & 1) * 512);
      SB();
    }
  }
}

DI void epi4(const Ctx& c, int tid, v4u b2v, const f2 (&x1)[4]) {
  LAS char* L = c.L;
  tid = fresh(tid);
  const int etok = tid >> 3, ef = 64 * c.j + 8 * (tid & 7);
  const v4u gbv = *(LAS v4u*)(L + L_MODB + 5120 + ef * 2);
  BAR();
  const int ra = L_RED + etok * 272 + (tid & 7) * 32;
  const f4 y0 = *(LAS f4*)(L + ra) + *(LAS f4*)(L + ra + 17408), y1 = *(LAS f4*)(L + ra + 16) + *(LAS f4*)(L + ra + 17408 + 16);
  v4u o;
#pragma unroll
  for (int e = 0; e < 4; ++e) {
    const f2 y = e == 0 ? f2{y0.x, y0.y} : e == 1 ? f2{y0.z, y0.w} : e == 2 ? f2{y1.x, y1.y} : f2{y1.z, y1.w};
    o[e] = pk2(fma2(unpk(gbv[e]), y + unpk(b2v[e]), x1[e]));
  }
  const rsrc_t ro = __builtin_amdgcn_make_buffer_rsrc((void*)c.out_, 0, 2048 * 512 * 2, 0x00020000);
  __builtin_amdgcn_raw_buffer_store_b128(o, ro, etok * 1024 + (tid & 7) * 16 + c.g * 65536 + c.j * 128, 0, 0);
}
DI v4u load_xres(const Ctx& c, int tid) {
  const rsrc_t rx = __builtin_amdgcn_make_buffer_rsrc((void*)c.x_, 0, 2048 * 512 * 2, 0x00020000);
  return __builtin_amdgcn_raw_buffer_load_b128(rx, (tid >> 3) * 1024 + (tid & 7) * 16, c.g * 65536 + c.j * 128, 0);
}
DI v4u load_b2(const Ctx& c, int tid) {
  const rsrc_t rb2 = __builtin_amdgcn_make_buffer_rsrc((void*)c.b2_, 0, 1024, 0x00020000);
  return __builtin_amdgcn_raw_buffer_load_b128(rb2, (tid & 7) * 16, c.j * 128, 0);
}

template <int T> DI void tail_c(const Ctx& c, int tid, gu32* oflag STP) {
  tid = fresh(tid);
  const int l = tid & 63, i = l & 15, w = c.w, g = c.g, j = c.j;
  LAS char* L = c.L;
  const unsigned w3off = OFF_W3T + c.tile0 * 16384;
  v4u w3a[16], w3b[16], w4[44];
  constexpr int EARLY = T == 2 ? 8 : 0;
#pragma unroll
  for (int n = 0; n < EARLY; ++n) w3a[n] = ldf(c.rs, l * 16, w3off + ((n + 2 * j) & 15) * 1024);
  WAITV_BAR(EARLY);
  if (tid == 0) __hip_atomic_store(oflag, c.tag, RLX_AGENT);
  ST(7);
  if (T == 2) {
#pragma unroll
    for (int n = EARLY; n < 16; ++n) w3a[n] = ldf(c.rs, l * 16, w3off + ((n + 2 * j) & 15) * 1024);
  } else if (w == 3) poll8(c.gfl, c.tag, l);
  SBAR();
  ST(8);
  auto o_tick = [&](int m) {
    const int slot = 8 * (w >> 1) + 2 * m + (w & 1);
    dma<4>(c.rsv, l * 16, OFF_O + (g * 16 + ((slot + 2 * j) & 15)) * 4096, c.Lb + slot * 4096);
  };
  o_tick(0); o_tick(1);
  WAITV_BAR(4); SL(20);
  o_tick(2);
  WAITV_BAR(4);
  o_tick(3);
  WAITV_BAR(4);
  WAITV_BAR(0); SL(21);
  const float b1a = bflo(((const GAS u16*)c.b1_)[c.tile0 * 16 + i]), b1b = bflo(((const GAS u16*)c.b1_)[(c.tile0 + T - 1) * 16 + i]);
  const v4u xres = load_xres(c, tid);
#pragma unroll
  for (int n = 0; n < 16; ++n) {
    if (T == 2) w3b[n] = ldf(c.rs, l * 16, w3off + 16384 + ((n + 2 * j) & 15) * 1024);
    else w3a[n] = ldf(c.rs, l * 16, w3off + ((n + 2 * j) & 15) * 1024);
  }
  SB();
  float S[5] = {0.f, 0.f, 0.f, 0.f, 0.f};
  if (T == 1) {
    const v4u av = *(LAS v4u*)(L + L_MODB + 4096 + l * 16), sv = *(LAS v4u*)(L + L_MODB + 3072 + l * 16);
#pragma unroll
    for (int e = 0; e < 4; ++e) {
      const float a0 = bflo(av[e]), a1 = bfhi(av[e]), s0 = bflo(sv[e]), s1 = bfhi(sv[e]);
      S[0] += a0 + a1; S[1] += s0 + s1; S[2] += a0 * a0 + a1 * a1; S[3] += a0 * s0 + a1 * s1; S[4] += s0 * s0 + s1 * s1;
    }
#pragma unroll
    for (int e = 0; e < 5; ++e) S[e] = swap32_sum(swap16_sum(sum16(S[e])));
  }
  f2 x1[4];
  epi2(c, tid, xres, x1);
  ST(17);
  float uvt[T];
  col_terms<T>(c, l, w3a, w3b, b1a, b1b, uvt);
  WAITV_BAR(0);
  if (tid == 128) __hip_atomic_store(c.gfl + 32 * 32 + j, c.tag, RLX_AGENT);
  ST(9);
  if (w < 2) {
    dma<4>(c.rsv, l * 16, OFF_XA + (g * 16 + 2 * j + w) * 4096, c.Lb + w * 4096);
    WAITV(0);
  }
  SBAR();
  ST(10);
  f4 acc[4][T];
  mlp1_loop<T, T == 1 ? 2 : 0>(c, l, w3a, w3b, acc, w4, 0, 0, S STA);
  BAR();
  ST(12);
  mlp1_epi<T>(c, l, uvt, acc);
  ST(18);
  WAITV_BAR(0);
  ST(13);
  const int rot4 = (c.t03 + 1) >> 1;
  auto h_tick = [&](int m) {
    const int sg = 4 * m + w;
    int k = rot4 + sg; if (k >= 43) k -= 43;
    dma<4>(c.rsv, l * 16, OFF_H1 + (g * 43 + k) * 4096, c.Lb + (sg >= 38 ? sg - 38 : sg) * 4096);
  };
  h_tick(0);
  const v4u b2v = load_b2(c, tid);
  WAITV_BAR(0);
  USE(b2v);
  if (T == 1 && w == 3) poll8(c.gfl + 64 * 32, c.tag, l);
  SBAR();
  ST(14);
  h_tick(1); h_tick(2); h_tick(3);
#pragma unroll
  for (int m = 1; m < 11; ++m) {
    if (m <= 7) WAITV_BAR(8);
    else if (m == 8) WAITV_BAR(4);
    else if (m == 9) WAITV_BAR(0);
    if (m == 1) SL(26);
    if (m == 5) SL(27);
    if (m == 9) SL(28);
    if (m + 3 < 10 || (m + 3 == 10 && w < 3)) h_tick(m + 3);
  }
  epi4(c, tid, b2v, x1);
}

__global__ void __launch_bounds__(512, 2)
fused_block(const u16* __restrict__ x_, const u16* __restrict__ wqkv_, const u16* __restrict__ wo_, const u16* __restrict__ w1_,
            const u16* __restrict__ b1_, const u16* __restrict__ w2_, const u16* __restrict__ b2_, const u16* __restrict__ c_,
            const u16* __restrict__ wmod_, u16* __restrict__ out_, unsigned char* __restrict__ ws_) {
  extern __shared__ __attribute__((aligned(16))) char smem[];
  asm volatile("" ::"s"(x_), "s"(wqkv_), "s"(wo_), "s"(w1_), "s"(b1_), "s"(w2_), "s"(b2_), "s"(c_), "s"(wmod_), "s"(out_), "s"(ws_));
  LAS char* L = (LAS char*)smem;
  const unsigned Lb = (unsigned)(uintptr_t)L;
  const int tid0 = threadIdx.x, w = __builtin_amdgcn_readfirstlane(tid0 >> 6);
  const int b = blockIdx.x, xcd = b & 7, idx = b >> 3, g = xcd * 4 + (idx >> 3), j = idx & 7;
  const int g1 = 8 * (xcd >> 1) + (idx >> 2), j1 = 4 * (xcd & 1) + (idx & 3);
  GAS unsigned char* ws = (GAS unsigned char*)ws_;
  const rsrc_t rs = __builtin_amdgcn_make_buffer_rsrc((void*)ws_, 0, WS_USED, 0x00020000);
  const v4u rsv = {(unsigned)(uintptr_t)ws_, (unsigned)((uintptr_t)ws_ >> 32) & 0xffffu, WS_USED, 0x00020000u};
  { const int tid = tid0; ST(0); }
  unsigned epoch = *(const __attribute__((address_space(4))) unsigned*)(uintptr_t)(ws_ + OFF_E);
#define GET_TAG asm volatile("" : "+s"(epoch) : : "memory"); const unsigned tag = epoch + 1u;
  const int rq = xcd >> 1;
  const int nt3 = j < 6 ? 11 : 10, t03 = j < 6 ? 11 * j : 66 + 10 * (j - 6);
  gu32* gfl = (gu32*)(ws + OFF_GFLAG) + g * 32;
  const v4u rsq = {(unsigned)(uintptr_t)wqkv_, (unsigned)((uintptr_t)wqkv_ >> 32) & 0xffffu, 512u * 1536u * 2u, 0x00020000u};
#define LANE_IDS const int l = lane_id(), tid = w * 64 + l, i = l & 15, q = l >> 4; \
  const unsigned w1v = (8 * (w & 3) + (l >> 3)) * 3072 + 128 * j1 + 16 * ((l & 7) ^ ((((l >> 4) & 1) | ((w & 1) << 1)) << 1));
  if (w < 4) {
    asm volatile("; matrix waves" ::: "memory");
    LANE_IDS
    const int u = b * 4 + w;
    const bool retile = u < 824;
    int rdst = 0, rks = 16, rnt = 4;
    if (retile) {
      const GAS u16* src; int s, cb, n;
      if (u < 128) { src = (const GAS u16*)wo_; s = u & 15; cb = u >> 4; n = 512; rdst = OFF_W2T; }
      else if (u < 480) { src = (const GAS u16*)w1_; s = u & 15; cb = (u - 128) >> 4; n = 1376; rdst = OFF_W3T; if (cb == 21) rnt = 2; }
      else { src = (const GAS u16*)w2_; cb = (u - 480) / 43; s = (u - 480) - cb * 43; n = 512; rdst = OFF_W4T; rks = 43; }
      rdst += (cb * 4 * rks + s) * 1024;
      const int ch = rnt == 2 ? (l & 3) : (l & 7);
      const GAS u16* p = src + (size_t)(32 * s + (l >> 3)) * n + 64 * cb + 8 * ch;
#pragma unroll
      for (int e = 0; e < 4; ++e) gdma(p + (size_t)(8 * e) * n, Lb + L_RT + w * 4096 + e * 1024);
    }
    ST(29);
    SBAR();
    ST(30);
#pragma unroll
    for (int s0 = 0; s0 < 11; ++s0) {
      int s = s0 + (idx >> 2); if (s >= 11) s -= 11;
      dma3(rsq, w1v, ((s + 4 * rq) & 15) * (32 * 3072), Lb + W1POS(s) + w * 3072);
    }
    GET_TAG
    ST(1);
    if (retile) {
      WAITV(33);
#pragma unroll
      for (int t = 0; t < 4; ++t)
        if (t < rnt) stf(rs, trfrag(L + L_RT + w * 4096 + (8 * q + (i >> 2)) * 128 + t * 32 + (i & 3) * 8), l * 16, rdst + t * rks * 1024);
    }
    ST(2);
    WAITV_BAR(0);
    ST(3);
    const int tb_ = q * 3072 + (i >> 2) * 128 + ((w ^ ((i >> 3) | ((q & 1) << 1))) * 32) + (i & 3) * 8;
    v4u wf[16][3];
#pragma unroll
    for (int s = 0; s < 11; ++s)
#pragma unroll
      for (int sg = 0; sg < 3; ++sg) wf[s][sg] = trfrag(L + W1POS(s) + sg * 1024 + tb_);
    BAR();
    ST(4);
    dma3(rsq, w1v, ((11 + 4 * rq) & 15) * (32 * 3072), Lb + L_R1 + w * 3072);
    f4 aq[4], ak[5], av[5];
    const int a0 = l * 16 ^ (q << 4), a1 = l * 16 ^ ((q | 4) << 4);
    v4u a[2][5];
    SBAR();
    ST(5);
#pragma unroll
    for (int r = 0; r < 5; ++r) a[0][r] = *(LAS v4u*)(L + r * 1024 + a0);
#pragma unroll
    for (int s = 0; s < 16; ++s) {
      if (s == 3 || s == 7) SBAR();
      if (s == 10) { WAITV_BAR(0); SL(31); }
      SB();
      const f4 z = {0.f, 0.f, 0.f, 0.f};
#pragma unroll
      for (int r = 0; r < 5; ++r) {
        ak[r] = mfma32(wf[s][1], a[s & 1][r], s ? ak[r] : z);
        SB();
        if (s < 15) a[(s + 1) & 1][r] = *(LAS v4u*)(L + ((s + 1) * 5 + r) * 1024 + (((s + 1) & 1) ? a1 : a0));
        SB();
        av[r] = mfma32(a[s & 1][r], wf[s][2], s ? av[r] : z);
        SB();
        if (s >= 10 && s < 15 && r < 3) { wf[s + 1][r] = trfrag(L + L_R1 + (s - 10) * 12288 + r * 1024 + tb_); SB(); }
        if (r) { aq[r - 1] = mfma32(wf[s][0], a[s & 1][r], s ? aq[r - 1] : z); SB(); }
      }
    }
    ST(6);
    {
      v2u qb[4], kb[5], vb[5];
#pragma unroll
      for (int r = 0; r < 5; ++r) {
        kb[r] = v2u{pk(ak[r].x, ak[r].y), pk(ak[r].z, ak[r].w)};
        vb[r] = v2u{pk(av[r].x, av[r].y), pk(av[r].z, av[r].w)};
        if (r) qb[r - 1] = v2u{pk(aq[r - 1].x, aq[r - 1].y), pk(aq[r - 1].z, aq[r - 1].w)};
      }
      const bool odd = i & 1;
      const bool cur0 = 4 * q + (i & 1) <= i, cur1 = 4 * q + 2 + (i & 1) <= i;
      const float sc = 0.25f * 1.4426950409f;
      const int hd = 4 * j1 + w;
      unsigned h[4][2];
#pragma unroll
      for (int r = 0; r < 4; ++r) {
        const f4 z = {0.f, 0.f, 0.f, 0.f};
        f4 sp = mfma16(kb[r], qb[r], z), scu = mfma16(kb[r + 1], qb[r], z);
        float p0 = odd ? sp.y : sp.x, p1 = odd ? sp.w : sp.z, c0 = odd ? scu.y : scu.x, c1 = odd ? scu.w : scu.z;
        if (r == 0 && g1 == 0) { p0 = -4e9f; p1 = -4e9f; }
        float v0 = cur0 ? c0 : p0, v1 = cur1 ? c1 : p1;
        const float mx = swap32_max(swap16_max(fmaxf(v0, v1)));
        v0 = __builtin_amdgcn_exp2f((v0 - mx) * sc); v1 = __builtin_amdgcn_exp2f((v1 - mx) * sc);
        const float rs_ = __builtin_amdgcn_rcpf(swap32_sum(swap16_sum(v0 + v1)));
        const unsigned pp = pk(v0 * rs_, v1 * rs_);
        const unsigned d0 = odd ? pp << 16 : pp & 0xffffu, d1 = odd ? pp & 0xffff0000u : pp >> 16;
        const v4u pf = {cur0 ? 0u : d0, cur1 ? 0u : d1, cur0 ? d0 : 0u, cur1 ? d1 : 0u};
        const f4 o = mfma32(v4u{vb[r].x, vb[r].y, vb[r + 1].x, vb[r + 1].y}, pf, z);
        h[r][0] = pk(o.x, o.y); h[r][1] = pk(o.z, o.w);
      }
#pragma unroll
      for (int pp = 0; pp < 2; ++pp) {
        auto s0 = __builtin_amdgcn_permlane16_swap(h[2 * pp][0], h[2 * pp + 1][0], false, false);
        auto s1 = __builtin_amdgcn_permlane16_swap(h[2 * pp][1], h[2 * pp + 1][1], false, false);
        stf(rs, v4u{s0[0], s1[0], s0[1], s1[1]}, (q & 1) * 1024 + ((q >> 1) * 16 + i) * 16,
            OFF_O + ((g1 * 16 + (hd >> 1)) * 4 + 2 * pp) * 1024 + (hd & 1) * 512);
      }
    }
    ST(19);
    const bool two = w < nt3 - 8;
    const Ctx c = {rs, rsv, L, Lb, tag, g, j, w, t03, t03 + (two ? 2 * w : nt3 - 8 + w), gfl, x_, b1_, b2_, out_};
    gu32* oflag = (gu32*)(ws + OFF_GFLAG) + g1 * 32 + j1;
    if (two) {
      asm volatile("; two column tiles" ::: "memory");
      tail_c<2>(c, tid, oflag STA);
      ST(16);
      SLF();
      __builtin_amdgcn_endpgm();
    } else {
      asm volatile("; one column tile" ::: "memory");
      tail_c<1>(c, tid, oflag STA);
      ST(16);
      SLF();
      __builtin_amdgcn_endpgm();
    }
  } else {
    asm volatile("; helper waves" ::: "memory");
    LANE_IDS
    const bool modjob = b < 192;
    const int mcb = b < 64 ? (b & 15) : 16 + ((b - 64) & 31), mks = b < 64 ? (b >> 4) : ((b - 64) >> 5);
    const int v = w - 4;
    v2u cw; v4u wm[4];
    if (modjob) {
      const int r0 = 128 * mks + 32 * v + 4 * (l >> 3);
      cw = *(const GAS v2u*)((const GAS u16*)c_ + r0);
#pragma unroll
      for (int e = 0; e < 4; ++e) wm[e] = *(const GAS v4u*)((const GAS u16*)wmod_ + (size_t)(r0 + e) * 3072 + 64 * mcb + 8 * (l & 7));
    }
    const int row0 = 20 * ((v + idx) & 3) + q;
    v4u xr[5][4];
#pragma unroll
    for (int p = 0; p < 5; ++p) {
      int tok = 64 * g1 - 16 + row0 + 4 * p;
      tok = tok < 0 ? 0 : tok;
#pragma unroll
      for (int n = 0; n < 4; ++n) xr[p][n] = *(const GAS v4u*)((const GAS u16*)x_ + (size_t)tok * 512 + 128 * ((n + rq) & 3) + 8 * i);
    }
    ST(29);
    SBAR();
    GET_TAG
    if (modjob) {
      f2 a2[4];
#pragma unroll
      for (int e = 0; e < 4; ++e) {
        const float cv = (e & 1) ? bfhi(cw[e >> 1]) : bflo(cw[e >> 1]);
        float sv = cv * __builtin_amdgcn_rcpf(1.0f + __builtin_amdgcn_exp2f(-1.4426950409f * cv));
        sv = bflo(pk(sv, sv));
        const f2 s2 = {sv, sv};
#pragma unroll
        for (int k = 0; k < 4; ++k) a2[k] = e ? fma2(s2, unpk(wm[e][k]), a2[k]) : s2 * unpk(wm[e][k]);
      }
      float a[4];
#pragma unroll
      for (int k = 0; k < 4; ++k) {
        auto r = __builtin_amdgcn_permlane32_swap(__float_as_uint(a2[k >> 1][k & 1]), __float_as_uint(a2[2 + (k >> 1)][k & 1]), false, false);
        a[k] = __uint_as_float(r[0]) + __uint_as_float(r[1]);
      }
#pragma unroll
      for (int k = 0; k < 2; ++k) {
        auto r = __builtin_amdgcn_permlane16_swap(__float_as_uint(a[k]), __float_as_uint(a[k + 2]), false, false);
        a[k] = __uint_as_float(r[0]) + __uint_as_float(r[1]);
        a[k] += dpp<0x128>(a[k]);
      }
      if (!(l & 8)) *(LAS f2*)(L + L_MRED + v * 256 + (8 * (l & 7) + 2 * ((l >> 4) & 1) + 4 * (l >> 5)) * 4) = f2{a[0], a[1]};
      asm volatile("s_waitcnt lgkmcnt(0)" ::: "memory");
      volatile LAS unsigned* mf = (volatile LAS unsigned*)(L + L_MRED + 1024);
      if (w < 7) mf[v] = tag;
      else {
        _Pragma("nounroll") for (unsigned s = 0; s < SPIN_CAP; ++s)
          if (__all(mf[l < 3 ? l : 0] == tag)) break;
        asm volatile("" ::: "memory");
        if (l < 16) {
          f4 s = *(LAS f4*)(L + L_MRED + l * 16);
#pragma unroll
          for (int ww = 1; ww < 4; ++ww) s += *(LAS f4*)(L + L_MRED + ww * 256 + l * 16);
          stf(rs, (__builtin_bit_cast(v4u, s) & 0xffffff00u) | (tag & 0xffu), l * 16, OFF_MODP + mcb * 1024 + mks * 256);
        }
      }
    }
    ST(1);
    f2 xn[5][16];
#pragma unroll
    for (int p = 0; p < 5; ++p) {
      f2 s2 = {0.f, 0.f}, q2 = {0.f, 0.f};
#pragma unroll
      for (int e = 0; e < 16; ++e) {
        xn[p][e] = unpk(xr[p][e >> 2][e & 3]);
        s2 += xn[p][e];
        q2 = fma2(xn[p][e], xn[p][e], q2);
      }
      const float m = sum16(s2.x + s2.y) * (1.0f / 512.0f);
      const float r = __builtin_amdgcn_rsqf(fmaxf(sum16(q2.x + q2.y) * (1.0f / 512.0f) - m * m, 0.f) + 1e-5f);
      const f2 r2 = {r, r}, mr2 = {-m * r, -m * r};
#pragma unroll
      for (int e = 0; e < 16; ++e) xn[p][e] = fma2(xn[p][e], r2, mr2);
    }
    int ha[5];
#pragma unroll
    for (int p = 0; p < 5; ++p) {
      const int r = row0 + 4 * p, qq = i & 3;
      ha[p] = ((i >> 2) * 5 + (r >> 4)) * 1024 + (qq * 16 + ((r & 15) ^ (qq | (i & 4)))) * 16;
      asm volatile("" : "+v"(ha[p]));
    }
    ST(2);
    { const int cb[4] = {4 * v, 4 * v + 1, 4 * v + 2, 4 * v + 3}; mod_fetch<4, true>(rs, L, cb, tag, l); }
    ST(3);
    BAR();
    if (tid == 256) __hip_atomic_store((gu32*)(ws + OFF_RFLAG) + b, tag, RLX_AGENT);
    v4u rf, mv[5];
    const int c0 = 24 + 4 * v, cb5[5] = {c0, c0 + 1, c0 + 2, c0 + 3, ((w & 1) ? 40 : 16) + j};
#pragma unroll
    for (int n = 0; n < 4; ++n) {
      const int ch = L_MODF + (16 * ((n + rq) & 3) + i) * 32;
      const f4 sa0 = *(LAS f4*)(L + ch), sa1 = *(LAS f4*)(L + ch + 16), ca0 = *(LAS f4*)(L + ch + 2048), ca1 = *(LAS f4*)(L + ch + 2064);
      const f2 sa2[4] = {{sa0.x, sa0.y}, {sa0.z, sa0.w}, {sa1.x, sa1.y}, {sa1.z, sa1.w}}, ca2[4] = {{ca0.x, ca0.y}, {ca0.z, ca0.w}, {ca1.x, ca1.y}, {ca1.z, ca1.w}};
      v4u o[5];
#pragma unroll
      for (int p = 0; p < 5; ++p)
#pragma unroll
        for (int e = 0; e < 4; ++e) {
          if (n == 0) o[p][e] = pk2(fma2(xn[p][4 * n + e], ca2[e], sa2[e]));
          else o[p][e] = pk(fma1(xn[p][4 * n + e].x, ca2[e].x, sa2[e].x), fma1(xn[p][4 * n + e].y, ca2[e].y, sa2[e].y));
        }
      if (n == 0) {
#pragma unroll
        for (int p = 0; p < 5; ++p) USE(o[p]);
        SBAR();
        ST(4);
      }
#pragma unroll
      for (int p = 0; p < 5; ++p) *(LAS v4u*)(L + ha[p] + n * 20480) = o[p];
      if (n < 3) BAR();
      if (n == 0) ST(5);
      if (n < 2) {
        dma3(rsq, w1v, ((12 + 2 * n + 4 * rq) & 15) * (32 * 3072), Lb + L_R1 + (1 + 2 * n) * 12288 + v * 3072);
        dma3(rsq, w1v, ((13 + 2 * n + 4 * rq) & 15) * (32 * 3072), Lb + L_R1 + (2 + 2 * n) * 12288 + v * 3072);
      }
      if (n == 2) {
        rf = ldf(rs, l * 16, OFF_RFLAG);
#pragma unroll
        for (int k = 0; k < 5; ++k) mv[k] = ldf(rs, l * 16, OFF_MODP + cb5[k] * 1024);
        SB();
      }
    }
    mod_finish<5, false>(rs, L, cb5, mv, tag, l);
    if (w == 7) {
      _Pragma("nounroll") for (unsigned s = 0; s < SPIN_CAP; ++s) {
        if (__all(rf.x == tag && rf.y == tag && rf.z == tag && rf.w == tag)) break;
        __builtin_amdgcn_s_sleep(8);
        asm volatile("" ::: "memory");
        rf = ldf(rs, l * 16, OFF_RFLAG);
      }
      __builtin_amdgcn_fence(__ATOMIC_ACQUIRE, "wavefront");
      if (b == 0 && l == 0) *(GAS unsigned*)(ws + OFF_E) = tag;
    }
    WAITV_BAR(0);
    ST(6);
    const Ctx c = {rs, rsv, L, Lb, tag, g, j, w, t03, t03 + nt3 - 8 + w, gfl, x_, b1_, b2_, out_};
    const int nh = v & 1, kh = v >> 1;
    const unsigned w2off = OFF_W2T + (4 * j + 2 * nh) * 16384, w3off = OFF_W3T + c.tile0 * 16384;
    v4u w2[16], w3a[16], w3b[16], w4[44];
#pragma unroll
    for (int n = 0; n < 16; ++n) w2[n] = ldf(rs, l * 16, w2off + (n & 1) * 16384 + ((8 * kh + (n >> 1) + 2 * j) & 15) * 1024);
    const v4u xres = load_xres(c, tid);
    const float b1a = bflo(((const GAS u16*)b1_)[c.tile0 * 16 + i]);
#pragma unroll
    for (int n = 0; n < 16; ++n) w3a[n] = ldf(rs, l * 16, w3off + ((n + 2 * j) & 15) * 1024);
    ST(19);
    SBAR();
    ST(7);
    float uvt[1];
    col_terms<1>(c, l, w3a, w3b, b1a, b1a, uvt);
    USE(uvt[0]);
    SBAR();
    ST(8);
    f2 x1[4];
    {
      f4 acc[4][2];
      v4u a2[2][4];
      const int ab = kh * 32768 + l * 16;
      SBAR(); SL(20);
#pragma unroll
      for (int r = 0; r < 4; ++r) a2[0][r] = *(LAS v4u*)(L + ab + r * 1024);
#pragma unroll
      for (int n = 0; n < 8; ++n) {
        if (n == 1 || n == 3 || n == 5) SBAR();
        SB();
#pragma unroll
        for (int r = 0; r < 4; ++r) {
          acc[r][0] = mfma32(w2[2 * n], a2[n & 1][r], n ? acc[r][0] : f4{0.f, 0.f, 0.f, 0.f});
          SB();
          if (n < 7) a2[(n + 1) & 1][r] = *(LAS v4u*)(L + ab + ((n + 1) * 4 + r) * 1024);
          SB();
          acc[r][1] = mfma32(w2[2 * n + 1], a2[n & 1][r], n ? acc[r][1] : f4{0.f, 0.f, 0.f, 0.f});
          SB();
        }
      }
      SL(21);
      const int lf = fresh(l), rb = L_RED + kh * 17408 + (lf & 15) * 272 + nh * 128 + (lf >> 4) * 16;
#pragma unroll
      for (int r = 0; r < 4; ++r)
#pragma unroll
        for (int t = 0; t < 2; ++t) *(LAS f4*)(L + rb + r * 16 * 272 + t * 64) = acc[r][t];
    }
    epi2(c, tid, xres, x1);
    ST(17);
    int rot4 = ((t03 + 1) >> 1) + kh;
    if (rot4 >= 43) rot4 -= 43;
    const unsigned w4off = OFF_W4T + (4 * j + 2 * nh) * 43 * 1024;
    asm volatile("" ::: "memory");
#pragma unroll
    for (int n = 0; n < 4; ++n) {
      int k = rot4 + 2 * n; if (k >= 43) k -= 43;
      w4[2 * n] = ldf(rs, l * 16, w4off + k * 1024);
      w4[2 * n + 1] = ldf(rs, l * 16, w4off + 43 * 1024 + k * 1024);
    }
    WAITV_BAR(8);
    ST(9);
    SBAR();
    ST(10);
    {
      f4 acc[4][1];
      const float S[5] = {0.f, 0.f, 0.f, 0.f, 0.f};
      mlp1_loop<1, 1>(c, l, w3a, w3b, acc, w4, w4off, rot4, S STA);
      BAR();
      ST(12);
      mlp1_epi<1>(c, l, uvt, acc);
    }
    asm volatile("" ::: "memory");
#pragma unroll
    for (int n = 16; n < 22; ++n) {
      int k = rot4 + 2 * n; if (k >= 43) k -= 43; if (k >= 43) k -= 43;
      w4[2 * n] = ldf(rs, l * 16, w4off + k * 1024);
      w4[2 * n + 1] = ldf(rs, l * 16, w4off + 43 * 1024 + k * 1024);
    }
    ST(18);
    WAITV_BAR(12);
    if (tid == 256) __hip_atomic_store(gfl + 64 * 32 + j, tag, RLX_AGENT);
    ST(13);
    const v4u b2v = load_b2(c, tid);
    {
      f4 acc[4][2];
      v4u a4[2][4];
      const int ab = kh * 4096 + l * 16;
      SBAR();
#pragma unroll
      for (int r = 0; r < 4; ++r) a4[0][r] = *(LAS v4u*)(L + ab + r * 1024);
#pragma unroll
      for (int n = 0; n < 22; ++n) {
        if (n == 1) { SBAR(); ST(14); }
        if ((n & 1) && n < 19) SBAR();
        if (n == 1) SL(26);
        if (n == 9) SL(27);
        if (n == 17) SL(28);
        SB();
        const int sp = 2 * (n + 1) >= 38 ? 2 * (n + 1) - 38 : 2 * (n + 1);
        if (n < 21 || kh == 0) {
#pragma unroll
          for (int r = 0; r < 4; ++r) {
            acc[r][0] = mfma32(w4[2 * n], a4[n & 1][r], n ? acc[r][0] : f4{0.f, 0.f, 0.f, 0.f});
            SB();
            if (n < 21) a4[(n + 1) & 1][r] = *(LAS v4u*)(L + ab + sp * 4096 + r * 1024);
            SB();
            acc[r][1] = mfma32(w4[2 * n + 1], a4[n & 1][r], n ? acc[r][1] : f4{0.f, 0.f, 0.f, 0.f});
            SB();
          }
        }
      }
      ST(15);
      const int lf = fresh(l), rb = L_RED + kh * 17408 + (lf & 15) * 272 + nh * 128 + (lf >> 4) * 16;
#pragma unroll
      for (int r = 0; r < 4; ++r)
#pragma unroll
        for (int t = 0; t < 2; ++t) *(LAS f4*)(L + rb + r * 16 * 272 + t * 64) = acc[r][t];
    }
    epi4(c, tid, b2v, x1);
    ST(16);
    SLF();
    __builtin_amdgcn_endpgm();
  }
}

extern "C" void kernel_launch(void* const* d_in, const int* in_sizes, int n_in, void* d_out, int out_size, void* d_ws, size_t ws_size,
                              hipStream_t stream) {
  (void)hipFuncSetAttribute((const void*)fused_block, hipFuncAttributeMaxDynamicSharedMemorySize, LDS_BYTES);
  fused_block<<<256, 512, LDS_BYTES, stream>>>((const u16*)d_in[0], (const u16*)d_in[1], (const u16*)d_in[2], (const u16*)d_in[4],
                                               (const u16*)d_in[5], (const u16*)d_in[6], (const u16*)d_in[7], (const u16*)d_in[8],
                                               (const u16*)d_in[9], (u16*)d_out, (unsigned char*)d_ws);
}
```

```cpp
#include <hip/hip_runtime.h>
#include <stdint.h>

#define GAS __attribute__((address_space(1)))
#define LAS __attribute__((address_space(3)))
#define DI __device__ __forceinline__
typedef unsigned short u16;
typedef unsigned v4u __attribute__((ext_vector_type(4)));
typedef unsigned v2u __attribute__((ext_vector_type(2)));
typedef float f4 __attribute__((ext_vector_type(4)));
typedef float f2 __attribute__((ext_vector_type(2)));
typedef __bf16 bf2 __attribute__((ext_vector_type(2)));
typedef __bf16 bf8 __attribute__((ext_vector_type(8)));
typedef short s4 __attribute__((ext_vector_type(4)));
typedef GAS unsigned gu32;
typedef GAS unsigned long long gu64;
#define RLX_AGENT __ATOMIC_RELAXED, __HIP_MEMORY_SCOPE_AGENT

constexpr unsigned OFF_E = 0;
constexpr unsigned OFF_RFLAG = 1024;
constexpr unsigned OFF_GFLAG = 4096;
constexpr unsigned OFF_MODP = 16384;
constexpr unsigned OFF_STAT = 65536;
constexpr unsigned OFF_W2T = 1u << 20;
constexpr unsigned OFF_W3T = OFF_W2T + 512 * 1024;
constexpr unsigned OFF_W4T = OFF_W3T + 1376 * 1024;
constexpr unsigned OFF_O = OFF_W4T + 1408 * 1024;
constexpr unsigned OFF_XA = OFF_O + 2048 * 1024;
constexpr unsigned OFF_H1 = OFF_XA + 2048 * 1024;
constexpr unsigned WS_USED = OFF_H1 + 32 * 43 * 4096;
constexpr unsigned OFF_STAMP = 64u << 20;

constexpr int L_RT = 49152;
constexpr int L_R1 = 81920;
constexpr int L_RED = 65536;
constexpr int L_STAT = 102400;
constexpr int L_MODF = 155648;
constexpr int L_MODB = L_MODF + 4096 - 2048;
constexpr int L_MRED = 77824;
constexpr int L_SYNC = 79872;
constexpr int L_OK = L_MODF + 3584;
constexpr int L_COEF = L_MODF;
constexpr int L_RR = L_COEF + 1024;
constexpr int LDS_BYTES = 163840;

constexpr int CODE_STEP = 1344;
constexpr unsigned SPIN_CAP = 1u << 18;

#define ST(n)
#define SL(n)
#define SRT(n)
#define SLF()

DI float bflo(unsigned u) { return __uint_as_float(u << 16); }
DI float bfhi(unsigned u) { return __uint_as_float(u & 0xffff0000u); }
DI unsigned pk(float a, float b) { f2 v = {a, b}; return __builtin_bit_cast(unsigned, __builtin_convertvector(v, bf2)); }
DI f2 unpk(unsigned u) { return f2{bflo(u), bfhi(u)}; }
DI f2 fma2(f2 a, f2 b, f2 c) { return __builtin_elementwise_fma(a, b, c); }
DI unsigned pk2(f2 v) { return __builtin_bit_cast(unsigned, __builtin_convertvector(v, bf2)); }
DI f4 mfma32(v4u a, v4u b, f4 c) { return __builtin_amdgcn_mfma_f32_16x16x32_bf16(__builtin_bit_cast(bf8, a), __builtin_bit_cast(bf8, b), c, 0, 0, 0); }
template <bool FIRST> DI void mac(f4& c, v4u a, v4u b) {
  if constexpr (FIRST) asm volatile("v_mfma_f32_16x16x32_bf16 %0, %1, %2, 0" : "=&v"(c) : "v"(a), "v"(b));
  else asm volatile("v_mfma_f32_16x16x32_bf16 %0, %1, %2, %0" : "+v"(c) : "v"(a), "v"(b));
}
#define MAC(first, c, a, b) do { if (first) mac<true>(c, a, b); else mac<false>(c, a, b); } while (0)
#define MFMA_DONE() do { asm volatile("s_nop 15" ::: "memory"); __builtin_amdgcn_sched_barrier(0); } while (0)
DI f4 mfma16(v2u a, v2u b, f4 c) { return __builtin_amdgcn_mfma_f32_16x16x16bf16_1k(__builtin_bit_cast(s4, a), __builtin_bit_cast(s4, b), c, 0, 0, 0); }
template <int CTRL> DI float dpp(float x) { return __builtin_bit_cast(float, __builtin_amdgcn_update_dpp(0, __builtin_bit_cast(int, x), CTRL, 0xf, 0xf, true)); }
DI float swap16_sum(float x) { auto r = __builtin_amdgcn_permlane16_swap(__float_as_uint(x), __float_as_uint(x), false, false); return __uint_as_float(r[0]) + __uint_as_float(r[1]); }
DI float swap32_sum(float x) { auto r = __builtin_amdgcn_permlane32_swap(__float_as_uint(x), __float_as_uint(x), false, false); return __uint_as_float(r[0]) + __uint_as_float(r[1]); }
DI float swap16_max(float x) { auto r = __builtin_amdgcn_permlane16_swap(__float_as_uint(x), __float_as_uint(x), false, false); return fmaxf(__uint_as_float(r[0]), __uint_as_float(r[1])); }
DI float swap32_max(float x) { auto r = __builtin_amdgcn_permlane32_swap(__float_as_uint(x), __float_as_uint(x), false, false); return fmaxf(__uint_as_float(r[0]), __uint_as_float(r[1])); }
DI float sum8(float x) { x += dpp<0xB1>(x); x += dpp<0x4E>(x); x += dpp<0x141>(x); return x; }
DI float sum16(float x) { x = sum8(x); x += dpp<0x140>(x); return x; }

#define BAR() asm volatile("s_waitcnt lgkmcnt(0)\n\ts_barrier" ::: "memory")
#define WAITV(n) do { asm volatile("s_waitcnt vmcnt(%0)" ::"i"(n) : "memory"); if ((n) == 0) __builtin_amdgcn_s_waitcnt(0x0F70); } while (0)
#define WAITV_BAR(n) do { asm volatile("s_waitcnt vmcnt(%0) lgkmcnt(0)\n\ts_barrier" ::"i"(n) : "memory"); if ((n) == 0) __builtin_amdgcn_s_waitcnt(0x0F70); } while (0)
#define USE(x) asm volatile("" ::"v"(x))

template <int N> DI void dma(v4u rs, unsigned voff, unsigned soff, unsigned lds) {
  unsigned keep;
  if constexpr (N == 1)
    asm volatile("s_mov_b32 %0, m0\n\ts_mov_b32 m0, %2\n\ts_nop 0\n\t"
                 "buffer_load_dwordx4 %1, %3, %4 offen sc1 lds\n\t"
                 "s_mov_b32 m0, %0" : "=&s"(keep) : "v"(voff), "s"(lds), "s"(rs), "s"(soff) : "memory");
  else if constexpr (N == 2)
    asm volatile("s_mov_b32 %0, m0\n\ts_mov_b32 m0, %2\n\ts_nop 0\n\t"
                 "buffer_load_dwordx4 %1, %3, %4 offen sc1 lds\n\t"
                 "buffer_load_dwordx4 %1, %3, %4 offen offset:1024 sc1 lds\n\t"
                 "s_mov_b32 m0, %0" : "=&s"(keep) : "v"(voff), "s"(lds), "s"(rs), "s"(soff) : "memory");
  else
    asm volatile("s_mov_b32 %0, m0\n\ts_mov_b32 m0, %2\n\ts_nop 0\n\t"
                 "buffer_load_dwordx4 %1, %3, %4 offen sc1 lds\n\t"
                 "buffer_load_dwordx4 %1, %3, %4 offen offset:1024 sc1 lds\n\t"
                 "buffer_load_dwordx4 %1, %3, %4 offen offset:2048 sc1 lds\n\t"
                 "buffer_load_dwordx4 %1, %3, %4 offen offset:3072 sc1 lds\n\t"
                 "s_mov_b32 m0, %0" : "=&s"(keep) : "v"(voff), "s"(lds), "s"(rs), "s"(soff) : "memory");
}
DI void dma3(v4u rs, unsigned voff, unsigned soff, unsigned lds) {
  unsigned keep;
  asm volatile("s_mov_b32 %0, m0\n\ts_mov_b32 m0, %2\n\ts_nop 0\n\t"
               "buffer_load_dwordx4 %1, %3, %4 offen lds\n\t"
               "buffer_load_dwordx4 %1, %3, %4 offen offset:1024 lds\n\t"
               "buffer_load_dwordx4 %1, %3, %4 offen offset:2048 lds\n\t"
               "s_mov_b32 m0, %0" : "=&s"(keep) : "v"(voff), "s"(lds), "s"(rs), "s"(soff) : "memory");
}
DI v4u trfrag(LAS char* p) {
  v2u lo = __builtin_bit_cast(v2u, __builtin_amdgcn_ds_read_tr16_b64_v4i16((LAS s4*)p));
  v2u hi = __builtin_bit_cast(v2u, __builtin_amdgcn_ds_read_tr16_b64_v4i16((LAS s4*)(p + 512)));
  return v4u{lo.x, lo.y, hi.x, hi.y};
}
DI int lane_id() { int r; asm volatile("v_mbcnt_lo_u32_b32 %0, -1, 0\n\tv_mbcnt_hi_u32_b32 %0, -1, %0" : "=v"(r)); return r; }
DI int fresh(int x) { asm volatile("" : "+v"(x)); return x; }
#define SB() __builtin_amdgcn_sched_barrier(0)
DI void gdma(const GAS void* p, unsigned lds) {
  unsigned keep;
  asm volatile("s_mov_b32 %0, m0\n\ts_mov_b32 m0, %2\n\ts_nop 0\n\tglobal_load_lds_dwordx4 %1, off\n\ts_mov_b32 m0, %0"
               : "=&s"(keep) : "v"(p), "s"(lds) : "memory");
}
typedef __amdgpu_buffer_rsrc_t rsrc_t;
DI v4u ldf(rsrc_t rs, unsigned voff, unsigned soff) { return __builtin_amdgcn_raw_buffer_load_b128(rs, voff, soff, 16); }
#define LDW(n, base) ldw_rt(c.rs, l * 16, base, n)
DI v4u ldw_rt(rsrc_t rs, unsigned voff, unsigned base, int n) { return __builtin_amdgcn_raw_buffer_load_b128(rs, voff + (n & 3) * 1024, base + (n >> 2) * 4096, 16); }
DI void stf(rsrc_t rs, v4u v, unsigned voff, unsigned soff) { __builtin_amdgcn_raw_buffer_store_b128(v, rs, voff + soff, 0, 16); }

DI void meet4(LAS char* L, int at, int me, unsigned tag, int l) {
  volatile LAS unsigned* f = (volatile LAS unsigned*)(L + at);
  f[me] = tag;
  _Pragma("nounroll") for (unsigned s = 0; s < 16 * SPIN_CAP; ++s)
    if (__all(f[l & 3] == tag)) break;
  asm volatile("" ::: "memory");
}
DI void lds_wait(LAS char* L, int at, unsigned tag) {
  volatile LAS unsigned* f = (volatile LAS unsigned*)(L + at);
  _Pragma("nounroll") for (unsigned s = 0; s < 16 * SPIN_CAP; ++s)
    if (*f == tag) break;
  asm volatile("" ::: "memory");
}
DI void poll8(gu32* fl, unsigned tag, int l) {
  _Pragma("nounroll") for (unsigned s = 0; s < SPIN_CAP; ++s) {
    unsigned f = __hip_atomic_load(fl + (l & 7), RLX_AGENT);
    if (__all(f == tag)) break;
    __builtin_amdgcn_s_sleep(1);
  }
  __builtin_amdgcn_fence(__ATOMIC_ACQUIRE, "wavefront");
}

DI unsigned gelu2(f2 a, f2 k1, f2 k2, f2 ir) {
  const f2 t = a * fma2(a * a, k2, k1);
  const f2 d = fma2(f2{__builtin_amdgcn_exp2f(t.x), __builtin_amdgcn_exp2f(t.y)}, ir, ir);
  return pk2(a * f2{__builtin_amdgcn_rcpf(d.x), __builtin_amdgcn_rcpf(d.y)});
}

template <int N, bool F32> DI void mod_finish(rsrc_t rs, LAS char* L, const int (&cb)[N], v4u (&v)[N], unsigned tb, int l) {
  _Pragma("nounroll") for (unsigned s = 0; s < SPIN_CAP; ++s) {
    unsigned d = 0;
#pragma unroll
    for (int n = 0; n < N; ++n) d |= (v[n].x ^ tb) | (v[n].y ^ tb) | (v[n].z ^ tb) | (v[n].w ^ tb);
    if (__all(d << 24 == 0u)) break;
    __builtin_amdgcn_s_sleep(1);
    asm volatile("" ::: "memory");
#pragma unroll
    for (int n = 0; n < N; ++n) v[n] = ldf(rs, l * 16, OFF_MODP + cb[n] * 1024);
  }
#pragma unroll
  for (int n = 0; n < N; ++n) {
    float a[2];
#pragma unroll
    for (int k = 0; k < 2; ++k) {
      auto r = __builtin_amdgcn_permlane32_swap(v[n][k] & 0xffffff00u, v[n][k + 2] & 0xffffff00u, false, false);
      a[k] = swap16_sum(__uint_as_float(r[0]) + __uint_as_float(r[1]));
    }
    f2 m = unpk(pk(a[0], a[1]));
    const float one = ((cb[n] >> 3) == 1 || (cb[n] >> 3) == 4) ? 1.0f : 0.0f;
    const unsigned p = pk(m.x + one, m.y + one);
    if (!(l & 16)) {
      if (F32) *(LAS f2*)(L + L_MODF + cb[n] * 256 + (l & 15) * 16 + (l >> 5) * 8) = unpk(p);
      else *(LAS unsigned*)(L + L_MODB + cb[n] * 128 + (l & 15) * 8 + (l >> 5) * 4) = p;
    }
  }
}
template <int N, bool F32> DI void mod_fetch(rsrc_t rs, LAS char* L, const int (&cb)[N], unsigned tb, int l) {
  v4u v[N];
#pragma unroll
  for (int n = 0; n < N; ++n) v[n] = ldf(rs, l * 16, OFF_MODP + cb[n] * 1024);
  mod_finish<N, F32>(rs, L, cb, v, tb, l);
}

#define STP
#define STA
DI float fma1(float a, float b, float c) { float r; asm("v_fma_f32 %0, %1, %2, %3" : "=v"(r) : "v"(a), "v"(b), "v"(c)); return r; }
#define W1POS(s) ((s) < 6 ? L_R1 + (s) * 12288 : (s) < 10 ? ((s) - 6) * 12288 : 65536)
#define SBAR() asm volatile("s_barrier" ::: "memory")

DI void mod_loads(const u16* c_, const u16* wmod_, int mcb, int mks, int v, int l, v2u& cw, v4u (&wm)[4]) {
  const int r0 = 128 * mks + 32 * v + 4 * (l >> 3);
  cw = *(const GAS v2u*)((const GAS u16*)c_ + r0);
#pragma unroll
  for (int e = 0; e < 4; ++e) wm[e] = *(const GAS v4u*)((const GAS u16*)wmod_ + (r0 + e) * 3072 + 64 * mcb + 8 * (l & 7));
}
DI void mod_job(rsrc_t rs, LAS char* L, int red, int mcb, int mks, int v, int l, unsigned tag, v2u cw, const v4u (&wm)[4]) {
  f2 a2[4];
#pragma unroll
  for (int e = 0; e < 4; ++e) {
    const float cv = (e & 1) ? bfhi(cw[e >> 1]) : bflo(cw[e >> 1]);
    float sv = cv * __builtin_amdgcn_rcpf(1.0f + __builtin_amdgcn_exp2f(-1.4426950409f * cv));
    sv = bflo(pk(sv, sv));
    const f2 s2 = {sv, sv};
#pragma unroll
    for (int k = 0; k < 4; ++k) a2[k] = e ? fma2(s2, unpk(wm[e][k]), a2[k]) : s2 * unpk(wm[e][k]);
  }
  float a[4];
#pragma unroll
  for (int k = 0; k < 4; ++k) {
    auto r = __builtin_amdgcn_permlane32_swap(__float_as_uint(a2[k >> 1][k & 1]), __float_as_uint(a2[2 + (k >> 1)][k & 1]), false, false);
    a[k] = __uint_as_float(r[0]) + __uint_as_float(r[1]);
  }
#pragma unroll
  for (int k = 0; k < 2; ++k) {
    auto r = __builtin_amdgcn_permlane16_swap(__float_as_uint(a[k]), __float_as_uint(a[k + 2]), false, false);
    a[k] = __uint_as_float(r[0]) + __uint_as_float(r[1]);
    a[k] += dpp<0x128>(a[k]);
  }
  if (!(l & 8)) *(LAS f2*)(L + red + v * 256 + (8 * (l & 7) + 2 * ((l >> 4) & 1) + 4 * (l >> 5)) * 4) = f2{a[0], a[1]};
  asm volatile("s_waitcnt lgkmcnt(0)" ::: "memory");
  volatile LAS unsigned* mf = (volatile LAS unsigned*)(L + red + 1024);
  if (v < 3) mf[v] = tag;
  else {
    _Pragma("nounroll") for (unsigned s = 0; s < SPIN_CAP; ++s)
      if (__all(mf[l < 3 ? l : 0] == tag)) break;
    asm volatile("" ::: "memory");
    if (l < 16) {
      f4 s = *(LAS f4*)(L + red + l * 16);
#pragma unroll
      for (int ww = 1; ww < 4; ++ww) s += *(LAS f4*)(L + red + ww * 256 + l * 16);
      stf(rs, (__builtin_bit_cast(v4u, s) & 0xffffff00u) | (tag & 0xffu), l * 16, OFF_MODP + mcb * 1024 + mks * 256);
    }
  }
}

struct Ctx {
  rsrc_t rs; v4u rsv; LAS char* L; unsigned Lb, tag; int g, j, w, t03, tile0; gu32* gfl;
  const u16 *x_, *b1_, *b2_; u16* out_;
};

template <int T> DI void col_terms(const Ctx& c, bool two, int l, const v4u (&w3a)[16], const v4u (&w3b)[16], float b1a, float b1b, float (&uvt)[T]) {
  l = fresh(l);
  const int i = l & 15, q = l >> 4, ty = i >> 2;
  const int base = L_MODB + (ty == 1 ? 3072 : 4096) + q * 16;
  const unsigned fill = ty == 2 ? 0x3f803f80u : 0u;
  f4 a[T];
  a[0] = f4{q == 3 ? b1a : 0.f, 0.f, 0.f, 0.f};
  if (T == 2) a[T - 1] = f4{q == 3 ? b1b : 0.f, 0.f, 0.f, 0.f};
#pragma unroll
  for (int h = 0; h < 4; ++h) {
    v4u am[4];
#pragma unroll
    for (int s = 0; s < 4; ++s) am[s] = *(LAS v4u*)(c.L + base + ((4 * h + s + 2 * c.j) & 15) * 64);
#pragma unroll
    for (int s = 0; s < 4; ++s) {
      if (ty >= 2) am[s] = v4u{fill, fill, fill, fill};
      a[0] = mfma32(am[s], w3a[4 * h + s], a[0]);
    }
    if (T == 2 && two) {
#pragma unroll
      for (int s = 0; s < 4; ++s) a[T - 1] = mfma32(am[s], w3b[4 * h + s], a[T - 1]);
    }
  }
#pragma unroll
  for (int t = 0; t < T; ++t) uvt[t] = a[t].x;
}

DI void epi2(const Ctx& c, int tid, v4u xres, f2 (&x1)[4]) {
  LAS char* L = c.L;
  tid = fresh(tid);
  const int etok = tid >> 3, ef = 64 * c.j + 8 * (tid & 7);
  const v4u gav = *(LAS v4u*)(L + L_MODB + 2048 + ef * 2), sbv = *(LAS v4u*)(L + L_MODB + 3072 + ef * 2), a2v = *(LAS v4u*)(L + L_MODB + 4096 + ef * 2);
  BAR();
  const int ra = L_RED + etok * 272 + (tid & 7) * 32;
  const f4 y0 = *(LAS f4*)(L + ra) + *(LAS f4*)(L + ra + 17408), y1 = *(LAS f4*)(L + ra + 16) + *(LAS f4*)(L + ra + 17408 + 16);
  f2 p[4], a2[4];
#pragma unroll
  for (int e = 0; e < 4; ++e) {
    const f2 y = e == 0 ? f2{y0.x, y0.y} : e == 1 ? f2{y0.z, y0.w} : e == 2 ? f2{y1.x, y1.y} : f2{y1.z, y1.w};
    a2[e] = unpk(a2v[e]);
    x1[e] = fma2(unpk(gav[e]), y, unpk(xres[e]));
    p[e] = x1[e] * a2[e];
  }
  stf(c.rs, v4u{pk2(p[0]), pk2(p[1]), pk2(p[2]), pk2(p[3])},
      (((tid & 7) >> 2) * 4 + (etok >> 4)) * 1024 + ((tid & 3) * 16 + (etok & 15)) * 16, OFF_XA + (c.g * 16 + 2 * c.j) * 4096);
  SB();
  f2 vx = x1[0], vxx = x1[0] * x1[0], vp = p[0], vpp = p[0] * p[0], vpa = p[0] * a2[0], vps = p[0] * unpk(sbv[0]);
#pragma unroll
  for (int e = 1; e < 4; ++e) {
    vx += x1[e]; vxx = fma2(x1[e], x1[e], vxx); vp += p[e]; vpp = fma2(p[e], p[e], vpp); vpa = fma2(p[e], a2[e], vpa); vps = fma2(p[e], unpk(sbv[e]), vps);
  }
  const float sx = sum8(vx.x + vx.y), sxx = sum8(vxx.x + vxx.y), sp = sum8(vp.x + vp.y), spp = sum8(vpp.x + vpp.y), spa = sum8(vpa.x + vpa.y), sps = sum8(vps.x + vps.y);
  const bool first = (tid & 7) == 0;
  if ((tid & 7) < 2) stf(c.rs, __builtin_bit_cast(v4u, first ? f4{sx, sxx, sp, spp} : f4{spa, sps, spa, sps}), etok * 16 + (first ? 0 : 1024), OFF_STAT + (c.g * 8 + c.j) * 2048);
}

template <int T, int KIND>
DI void mlp1_loop(const Ctx& c, int l, const v4u (&w3a)[16], const v4u (&w3b)[16], f4 (&acc)[4][T], v4u (&w4)[44], unsigned w4off, bool two,
                  const float (&S)[5] STP) {
  LAS char* L = c.L;
  const int tid = c.w * 64 + l;
  auto xa_slot = [&](int slot) { dma<4>(c.rsv, l * 16, OFF_XA + (c.g * 16 + ((slot + 2 * c.j) & 15)) * 4096, c.Lb + slot * 4096); };
  v4u a[2][4];
#pragma unroll
  for (int r = 0; r < 4; ++r) a[0][r] = *(LAS v4u*)(L + r * 1024 + l * 16);
#pragma unroll
  for (int s = 0; s < 16; ++s) {
    if (s == 1 || s == 5 || s == 9 || s == 13) {
      if (KIND == 2 && c.w == 3) {
        if (s == 1) {
          poll8(c.gfl + 32 * 32, c.tag, l);
          ST(11);
          __builtin_amdgcn_s_setprio(2);
#pragma unroll
          for (int k = 2; k < 10; ++k) xa_slot(k);
          WAITV(16);
        }
        if (s == 5) WAITV(40);
        if (s == 9) WAITV(24);
        if (s == 13) WAITV(0);
      }
      SBAR();
      SL(22 + s / 4);
      if (KIND == 2 && c.w == 3) {
        if (s == 1) {
#pragma unroll
          for (int k = 10; k < 16; ++k) xa_slot(k);
#pragma unroll
          for (int p = 0; p < 4; ++p) dma<4>(c.rsv, l * 16, OFF_STAT + c.g * 16384 + p * 4096, c.Lb + L_STAT + p * 4096);
          __builtin_amdgcn_s_setprio(0);
        }
        if (s == 13) {
          float sx = 0, sxx = 0, sp = 0, spp = 0, spa = 0, sps = 0;
#pragma unroll
          for (int p = 0; p < 8; ++p) {
            const f4 a4 = *(LAS f4*)(L + L_STAT + p * 2048 + l * 16);
            const f2 b = *(LAS f2*)(L + L_STAT + p * 2048 + 1024 + l * 16);
            sx += a4.x; sxx += a4.y; sp += a4.z; spp += a4.w; spa += b.x; sps += b.y;
          }
          const float inv = 1.0f / 512.0f;
          const float m = sx * inv, var = fmaxf(sxx * inv - m * m, 0.f);
          const float sd = __builtin_amdgcn_sqrtf(var + 1e-5f), r = __builtin_amdgcn_rcpf(sd);
          const float m2 = (r * (sp - m * S[0]) + S[1]) * inv;
          const float e2 = (r * r * (spp - 2.f * m * spa + m * m * S[2]) + 2.f * r * (sps - m * S[3]) + S[4]) * inv;
          const float sd2 = __builtin_amdgcn_sqrtf(fmaxf(e2 - m2 * m2, 0.f) + 1e-5f);
          const float R = r * __builtin_amdgcn_rcpf(sd2), k1 = -2.302208198f * R;
          *(LAS f4*)(L + L_COEF + l * 16) = f4{-m, sd, -m2 * sd, sd * sd2};
          *(LAS f4*)(L + L_RR + l * 16) = f4{k1, 0.044715f * k1 * R * R, sd * sd2, 0.f};
        }
      }
    }
    SB();
#pragma unroll
    for (int r = 0; r < 4; ++r) {
      MAC(s == 0, acc[r][0], w3a[s], a[s & 1][r]);
      SB();
      if (s < 15) a[(s + 1) & 1][r] = *(LAS v4u*)(L + ((s + 1) * 4 + r) * 1024 + l * 16);
      SB();
    }
    if (T == 2 && two) {
#pragma unroll
      for (int r = 0; r < 4; ++r) MAC(s == 0, acc[r][T - 1], w3b[s], a[s & 1][r]);
      SB();
    }
    if (KIND == 1 && s >= 2 && s < 14) {
      w4[2 * s + 4] = LDW(2 * s + 4, w4off);
      w4[2 * s + 5] = LDW(2 * s + 5, w4off);
      SB();
    }
  }
  MFMA_DONE();
}

template <int T> DI void mlp1_epi(const Ctx& c, bool two, int l, const float (&uvt)[T], f4 (&acc)[4][T]) {
  l = fresh(l);
  const int i = l & 15, q = l >> 4;
  float cf[4]; f4 rr[4];
#pragma unroll
  for (int r = 0; r < 4; ++r) {
    cf[r] = *(LAS float*)(c.L + L_COEF + (r * 16 + i) * 16 + q * 4);
    rr[r] = *(LAS f4*)(c.L + L_RR + (r * 16 + i) * 16);
  }
#pragma unroll
  for (int t = 0; t < T; ++t) {
    if (t && !two) break;
#pragma unroll
    for (int r = 0; r < 4; ++r) acc[r][t] = __builtin_amdgcn_mfma_f32_16x16x4f32(uvt[t], cf[r], acc[r][t], 0, 0, 0);
  }
  SB();
#pragma unroll
  for (int t = 0; t < T; ++t) {
    if (t && !two) break;
    const int tl = c.tile0 + t;
#pragma unroll
    for (int pp = 0; pp < 2; ++pp) {
      unsigned h[2][2];
#pragma unroll
      for (int r2 = 0; r2 < 2; ++r2) {
        const int r = 2 * pp + r2;
        const f2 k1 = {rr[r].x, rr[r].x}, k2 = {rr[r].y, rr[r].y}, ir = {rr[r].z, rr[r].z};
        h[r2][0] = gelu2(f2{acc[r][t].x, acc[r][t].y}, k1, k2, ir);
        h[r2][1] = gelu2(f2{acc[r][t].z, acc[r][t].w}, k1, k2, ir);
      }
      auto s0 = __builtin_amdgcn_permlane16_swap(h[0][0], h[1][0], false, false);
      auto s1 = __builtin_amdgcn_permlane16_swap(h[0][1], h[1][1], false, false);
      stf(c.rs, v4u{s0[0], s1[0], s0[1], s1[1]}, (q & 1) * 1024 + ((q >> 1) * 16 + i) * 16,
          OFF_H1 + ((c.g * 43 + (tl >> 1)) * 4 + 2 * pp) * 1024 + (tl & 1) * 512);
      SB();
    }
  }
}

DI void epi4(const Ctx& c, int tid, v4u b2v, const f2 (&x1)[4]) {
  LAS char* L = c.L;
  tid = fresh(tid);
  const int etok = tid >> 3, ef = 64 * c.j + 8 * (tid & 7);
  const v4u gbv = *(LAS v4u*)(L + L_MODB + 5120 + ef * 2);
  BAR();
  const int ra = L_RED + etok * 272 + (tid & 7) * 32;
  const f4 y0 = *(LAS f4*)(L + ra) + *(LAS f4*)(L + ra + 17408), y1 = *(LAS f4*)(L + ra + 16) + *(LAS f4*)(L + ra + 17408 + 16);
  v4u o;
#pragma unroll
  for (int e = 0; e < 4; ++e) {
    const f2 y = e == 0 ? f2{y0.x, y0.y} : e == 1 ? f2{y0.z, y0.w} : e == 2 ? f2{y1.x, y1.y} : f2{y1.z, y1.w};
    o[e] = pk2(fma2(unpk(gbv[e]), y + unpk(b2v[e]), x1[e]));
  }
  const rsrc_t ro = __builtin_amdgcn_make_buffer_rsrc((void*)c.out_, 0, 2048 * 512 * 2, 0x00020000);
  __builtin_amdgcn_raw_buffer_store_b128(o, ro, etok * 1024 + (tid & 7) * 16 + c.g * 65536 + c.j * 128, 0, 0);
}
DI v4u load_xres(const Ctx& c, int tid) {
  const rsrc_t rx = __builtin_amdgcn_make_buffer_rsrc((void*)c.x_, 0, 2048 * 512 * 2, 0x00020000);
  return __builtin_amdgcn_raw_buffer_load_b128(rx, (tid >> 3) * 1024 + (tid & 7) * 16, c.g * 65536 + c.j * 128, 0);
}
DI v4u load_b2(const Ctx& c, int tid) {
  const rsrc_t rb2 = __builtin_amdgcn_make_buffer_rsrc((void*)c.b2_, 0, 1024, 0x00020000);
  return __builtin_amdgcn_raw_buffer_load_b128(rb2, (tid & 7) * 16, c.j * 128, 0);
}

DI void tail_c(const Ctx& c, bool two, int tid STP) {
  tid = fresh(tid);
  const int l = tid & 63, i = l & 15, w = c.w, g = c.g, j = c.j;
  LAS char* L = c.L;
  const unsigned w3off = OFF_W3T + c.tile0 * 16384;
  v4u w3a[16], w3b[16], w4[44];
  WAITV(0);
  meet4(L, L_OK + 16, w, c.tag, l);
  if (tid == 0) __hip_atomic_store(c.gfl + j, c.tag, RLX_AGENT);
  ST(7);
  lds_wait(L, L_OK, c.tag);
  const int c0 = 24 + 4 * w, cb5[5] = {c0, c0 + 1, c0 + 2, c0 + 3, ((w & 1) ? 40 : 16) + j};
  v4u mv[5];
#pragma unroll
  for (int k = 0; k < 5; ++k) mv[k] = ldf(c.rs, l * 16, OFF_MODP + cb5[k] * 1024);
  const float b1a = bflo(((const GAS u16*)c.b1_)[c.tile0 * 16 + i]), b1b = bflo(((const GAS u16*)c.b1_)[(c.tile0 + (two ? 1 : 0)) * 16 + i]);
  const v4u xres = load_xres(c, tid);
#pragma unroll
  for (int n = 0; n < 16; ++n) w3a[n] = LDW(n, w3off);
  SB();
  mod_finish<5, false>(c.rs, L, cb5, mv, c.tag, l);
  BAR();
  ST(8);
  auto o_tick = [&](int m) {
    const int slot = 8 * (w >> 1) + 2 * m + (w & 1);
    dma<4>(c.rsv, l * 16, OFF_O + (g * 16 + ((slot + 2 * j) & 15)) * 4096, c.Lb + slot * 4096);
  };
  o_tick(0); o_tick(1);
  WAITV_BAR(4); SL(20);
  o_tick(2);
  WAITV_BAR(4);
  o_tick(3);
  WAITV_BAR(4);
  WAITV_BAR(0); SL(21);
  USE(xres); USE(b1a); USE(b1b);
  float S[5] = {0.f, 0.f, 0.f, 0.f, 0.f};
  if (two) {
#pragma unroll
    for (int n = 0; n < 16; ++n) w3b[n] = LDW(16 + n, w3off);
  } else if (w == 3) {
    const v4u av = *(LAS v4u*)(L + L_MODB + 4096 + l * 16), sv = *(LAS v4u*)(L + L_MODB + 3072 + l * 16);
#pragma unroll
    for (int e = 0; e < 4; ++e) {
      const float a0 = bflo(av[e]), a1 = bfhi(av[e]), s0 = bflo(sv[e]), s1 = bfhi(sv[e]);
      S[0] += a0 + a1; S[1] += s0 + s1; S[2] += a0 * a0 + a1 * a1; S[3] += a0 * s0 + a1 * s1; S[4] += s0 * s0 + s1 * s1;
    }
#pragma unroll
    for (int e = 0; e < 5; ++e) S[e] = swap32_sum(swap16_sum(sum16(S[e])));
  }
  f2 x1[4];
  epi2(c, tid, xres, x1);
  ST(17);
  WAITV_BAR(0);
  if (tid == 128) __hip_atomic_store(c.gfl + 32 * 32 + j, c.tag, RLX_AGENT);
  ST(9);
#pragma unroll
  for (int n = 0; n < 16; ++n) { USE(w3a[n]); USE(w3b[n]); }
  if (w < 2) dma<4>(c.rsv, l * 16, OFF_XA + (g * 16 + 2 * j + w) * 4096, c.Lb + w * 4096);
  float uvt[2];
  col_terms<2>(c, two, l, w3a, w3b, b1a, b1b, uvt);
  USE(uvt[0]); USE(uvt[1]);
  if (w < 2) WAITV(0);
  SBAR();
  ST(10);
  f4 acc[4][2];
  mlp1_loop<2, 2>(c, l, w3a, w3b, acc, w4, 0, two, S STA);
  BAR();
  ST(12);
  mlp1_epi<2>(c, two, l, uvt, acc);
  ST(18);
  WAITV_BAR(0);
  ST(13);
  const int rot4 = (c.t03 + 1) >> 1;
  auto h_tick = [&](int m) {
    const int sg = 4 * m + w;
    int k = rot4 + sg; if (k >= 43) k -= 43;
    dma<4>(c.rsv, l * 16, OFF_H1 + (g * 43 + k) * 4096, c.Lb + (sg >= 38 ? sg - 38 : sg) * 4096);
  };
  h_tick(0);
  const v4u b2v = load_b2(c, tid);
  WAITV_BAR(0);
  USE(b2v);
  if (w == 3) poll8(c.gfl + 64 * 32, c.tag, l);
  SBAR();
  ST(14);
  h_tick(1); h_tick(2); h_tick(3);
#pragma unroll
  for (int m = 1; m < 10; ++m) {
    if (m == 1 || m == 7) WAITV_BAR(8);
    else if (m < 7) WAITV_BAR(12);
    else if (m == 8) WAITV_BAR(4);
    else WAITV_BAR(0);
    if (m == 1) SL(26);
    if (m == 5) SL(27);
    if (m == 9) SL(28);
    if (m == 1) h_tick(4);
    if (m + 4 < 10 || (m + 4 == 10 && w < 3)) h_tick(m + 4);
  }
  epi4(c, tid, b2v, x1);
}

extern "C" __global__ void __launch_bounds__(512, 2)
fused_block(const u16* __restrict__ x_, const u16* __restrict__ wqkv_, const u16* __restrict__ wo_, const u16* __restrict__ w1_,
            const u16* __restrict__ b1_, const u16* __restrict__ w2_, const u16* __restrict__ b2_, const u16* __restrict__ c_,
            const u16* __restrict__ wmod_, u16* __restrict__ out_, unsigned char* __restrict__ ws_) {
  extern __shared__ __attribute__((aligned(16))) char smem[];
  asm volatile("" ::"s"(x_), "s"(wqkv_), "s"(wo_), "s"(w1_), "s"(b1_), "s"(w2_), "s"(b2_), "s"(c_), "s"(wmod_), "s"(out_), "s"(ws_));
  unsigned long long pc;
  asm volatile("s_getpc_b64 %0" : "=s"(pc));
  LAS char* L = (LAS char*)smem;
  const unsigned Lb = (unsigned)(uintptr_t)L;
  const int tid0 = threadIdx.x, w = __builtin_amdgcn_readfirstlane(tid0 >> 6);
  const int b = blockIdx.x, xcd = b & 7, idx = b >> 3, g = xcd * 4 + (idx >> 3), j = idx & 7;
  GAS unsigned char* ws = (GAS unsigned char*)ws_;
  const rsrc_t rs = __builtin_amdgcn_make_buffer_rsrc((void*)ws_, 0, WS_USED, 0x00020000);
  const v4u rsv = {(unsigned)(uintptr_t)ws_, (unsigned)((uintptr_t)ws_ >> 32) & 0xffffu, WS_USED, 0x00020000u};
  SRT(32);
  ST(0);
  unsigned epoch = *(const __attribute__((address_space(4))) unsigned*)(uintptr_t)(ws_ + OFF_E);
#define GET_TAG asm volatile("" : "+s"(epoch) : : "memory"); const unsigned tag = epoch + 1u;
  const int rq = xcd >> 1;
  const int nt3 = j < 6 ? 11 : 10, t03 = j < 6 ? 11 * j : 66 + 10 * (j - 6);
  gu32* gfl = (gu32*)(ws + OFF_GFLAG) + g * 32;
  const v4u rsq = {(unsigned)(uintptr_t)wqkv_, (unsigned)((uintptr_t)wqkv_ >> 32) & 0xffffu, 512u * 1536u * 2u, 0x00020000u};
#define LANE_IDS const int l = lane_id(), tid = w * 64 + l, i = l & 15, q = l >> 4; \
  const unsigned w1v = (8 * (w & 3) + (l >> 3)) * 3072 + 128 * j + 16 * ((l & 7) ^ ((((l >> 4) & 1) | ((w & 1) << 1)) << 1));
  if (w < 4) {
    asm volatile("; matrix waves" ::: "memory");
    LANE_IDS
    unsigned dummy;
    asm volatile("s_load_dword %0, %1, 0x0\n\t"
                 "s_load_dword %0, %1, 0x40\n\t"
                 "s_load_dword %0, %1, 0x80\n\t"
                 "s_load_dword %0, %1, 0xc0\n\t"
                 "s_load_dword %0, %1, 0x100\n\t"
                 "s_load_dword %0, %1, 0x140" : "=&s"(dummy) : "s"((pc & ~255ull) + idx * CODE_STEP + w * 384));
    if (b < 64) {
      v2u cw; v4u wm[4];
      mod_loads(c_, wmod_, b & 15, b >> 4, w, l, cw, wm);
      asm volatile("" : "+s"(epoch) : : "memory");
      mod_job(rs, L, L_MRED, b & 15, b >> 4, w, l, epoch + 1u, cw, wm);
    }
    GET_TAG
    asm volatile("s_waitcnt lgkmcnt(0)" : "+s"(dummy));
    ST(29);
    SBAR();
    ST(30);
#pragma unroll
    for (int s0 = 0; s0 < 11; ++s0) {
      int s = s0 + (idx >> 3) + 4 * (xcd & 1); if (s >= 11) s -= 11;
      dma3(rsq, w1v, ((s + 4 * rq) & 15) * (32 * 3072), Lb + W1POS(s) + w * 3072);
    }
    const int u = b * 4 + w;
    const bool retile = u < 824, isw1 = u >= 128 && u < 480, isw2 = u >= 480;
    const int rcb = isw2 ? (int)((unsigned)(u - 480) / 43u) : ((u - (isw1 ? 128 : 0)) >> 4), rs_ = isw2 ? u - 480 - 43 * rcb : (u & 15);
    const int rnt = (isw1 && rcb == 21) ? 2 : 4;
    int rdst[4];
    const int rcbv = fresh(rcb), rsv_ = fresh(rs_);
#pragma unroll
    for (int t = 0; t < 4; ++t) {
      const int rcb = rcbv, rs_ = rsv_;
      const int tl = 4 * rcb + t;
      const int j3 = tl < 66 ? (int)((unsigned)tl / 11u) : (tl < 76 ? 6 : 7);
      const int s2 = (rs_ - 2 * (isw1 ? j3 : rcb)) & 15;
      int s4 = rs_ - (rcb < 6 ? (11 * rcb + 1) >> 1 : 3 + 5 * rcb); if (s4 < 0) s4 += 43;
      rdst[t] = isw1 ? OFF_W3T + (tl * 16 + s2) * 1024
              : isw2 ? OFF_W4T + ((((rcb * 2 + (t >> 1)) * 2 + (s4 & 1)) * 22 + (s4 >> 1)) * 2 + (t & 1)) * 1024
                     : OFF_W2T + ((((rcb * 2 + (t >> 1)) * 2 + (s2 >> 3)) * 8 + (s2 & 7)) * 2 + (t & 1)) * 1024;
    }
    if (retile) {
      const GAS u16* src = (const GAS u16*)(isw2 ? w2_ : isw1 ? w1_ : wo_);
      const int n = isw1 ? 1376 : 512, ch = rnt == 2 ? (l & 3) : (l & 7);
      const GAS u16* p = src + (32 * rs_ + (l >> 3)) * n + 64 * rcb + 8 * ch;
#pragma unroll
      for (int e = 0; e < 4; ++e) gdma(p + 8 * e * n, Lb + L_RT + w * 4096 + e * 1024);
      ST(1);
      WAITV(4);
    } else WAITV(0);
    ST(2);
    meet4(L, L_SYNC, w, tag, l);
    ST(3);
    const int tb_ = q * 3072 + (i >> 2) * 128 + ((w ^ ((i >> 3) | ((q & 1) << 1))) * 32) + (i & 3) * 8;
    const int tb1 = fresh(tb_ + L_R1);
    v4u wf[16][3];
#pragma unroll
    for (int s = 0; s < 11; ++s)
#pragma unroll
      for (int sg = 0; sg < 3; ++sg) wf[s][sg] = trfrag(L + (s < 5 ? tb1 + s * 12288 : tb_ + W1POS(s)) + sg * 1024);
    if (retile) {
      WAITV(0);
#pragma unroll
      for (int t = 0; t < 4; ++t)
        if (t < rnt) stf(rs, trfrag(L + L_RT + w * 4096 + (8 * q + (i >> 2)) * 128 + t * 32 + (i & 3) * 8), l * 16, rdst[t]);
    }
    BAR();
    ST(4);
    dma3(rsq, w1v, ((11 + 4 * rq) & 15) * (32 * 3072), Lb + L_R1 + w * 3072);
    f4 aq[4], ak[5], av[5];
    const int a0 = l * 16 ^ (q << 4), a1 = l * 16 ^ ((q | 4) << 4), a0h = fresh(a0 + 61440), a1h = fresh(a1 + 61440);
    v4u a[2][5];
    SBAR();
    ST(5);
#pragma unroll
    for (int r = 0; r < 5; ++r) a[0][r] = *(LAS v4u*)(L + r * 1024 + a0);
#pragma unroll
    for (int s = 0; s < 16; ++s) {
      if (s == 3) {
        WAITV(0); SBAR();
        if (tid == 0) __hip_atomic_store((gu32*)(ws + OFF_RFLAG) + b, tag, RLX_AGENT);
      }
      if (s == 7 || s == 10 || s == 13) SBAR();
      if (s == 10) SL(31);
      SB();
#pragma unroll
      for (int r = 0; r < 5; ++r) {
        MAC(s == 0, ak[r], wf[s][1], a[s & 1][r]);
        SB();
        if (s < 11) a[(s + 1) & 1][r] = *(LAS v4u*)(L + ((s + 1) * 5 + r) * 1024 + (((s + 1) & 1) ? a1 : a0));
        else if (s < 15) a[(s + 1) & 1][r] = *(LAS v4u*)(L + ((s - 11) * 5 + r) * 1024 + (((s + 1) & 1) ? a1h : a0h));
        SB();
        MAC(s == 0, av[r], a[s & 1][r], wf[s][2]);
        SB();
        if (s >= 10 && s < 15 && r < 3) { wf[s + 1][r] = trfrag(L + tb1 + (s - 10) * 12288 + r * 1024); SB(); }
        if (r) { MAC(s == 0, aq[r - 1], wf[s][0], a[s & 1][r]); SB(); }
      }
    }
    MFMA_DONE();
    ST(6);
    {
      v2u qb[4], kb[5], vb[5];
#pragma unroll
      for (int r = 0; r < 5; ++r) {
        kb[r] = v2u{pk(ak[r].x, ak[r].y), pk(ak[r].z, ak[r].w)};
        vb[r] = v2u{pk(av[r].x, av[r].y), pk(av[r].z, av[r].w)};
        if (r) qb[r - 1] = v2u{pk(aq[r - 1].x, aq[r - 1].y), pk(aq[r - 1].z, aq[r - 1].w)};
      }
      const bool odd = i & 1;
      const bool cur0 = 4 * q + (i & 1) <= i, cur1 = 4 * q + 2 + (i & 1) <= i;
      const float sc = 0.25f * 1.4426950409f;
      const int hd = 4 * j + w;
      unsigned h[4][2];
#pragma unroll
      for (int r = 0; r < 4; ++r) {
        const f4 z = {0.f, 0.f, 0.f, 0.f};
        f4 sp = mfma16(kb[r], qb[r], z), scu = mfma16(kb[r + 1], qb[r], z);
        float p0 = odd ? sp.y : sp.x, p1 = odd ? sp.w : sp.z, c0 = odd ? scu.y : scu.x, c1 = odd ? scu.w : scu.z;
        if (r == 0 && g == 0) { p0 = -4e9f; p1 = -4e9f; }
        float v0 = cur0 ? c0 : p0, v1 = cur1 ? c1 : p1;
        const float mx = swap32_max(swap16_max(fmaxf(v0, v1)));
        v0 = __builtin_amdgcn_exp2f((v0 - mx) * sc); v1 = __builtin_amdgcn_exp2f((v1 - mx) * sc);
        const float rs_ = __builtin_amdgcn_rcpf(swap32_sum(swap16_sum(v0 + v1)));
        const unsigned pp = pk(v0 * rs_, v1 * rs_);
        const unsigned d0 = odd ? pp << 16 : pp & 0xffffu, d1 = odd ? pp & 0xffff0000u : pp >> 16;
        const v4u pf = {cur0 ? 0u : d0, cur1 ? 0u : d1, cur0 ? d0 : 0u, cur1 ? d1 : 0u};
        const f4 o = mfma32(v4u{vb[r].x, vb[r].y, vb[r + 1].x, vb[r + 1].y}, pf, z);
        h[r][0] = pk(o.x, o.y); h[r][1] = pk(o.z, o.w);
      }
#pragma unroll
      for (int pp = 0; pp < 2; ++pp) {
        auto s0 = __builtin_amdgcn_permlane16_swap(h[2 * pp][0], h[2 * pp + 1][0], false, false);
        auto s1 = __builtin_amdgcn_permlane16_swap(h[2 * pp][1], h[2 * pp + 1][1], false, false);
        stf(rs, v4u{s0[0], s1[0], s0[1], s1[1]}, (q & 1) * 1024 + ((q >> 1) * 16 + i) * 16,
            OFF_O + ((g * 16 + (hd >> 1)) * 4 + 2 * pp) * 1024 + (hd & 1) * 512);
      }
    }
    ST(19);
    const bool two = w < nt3 - 8;
    const Ctx c = {rs, rsv, L, Lb, tag, g, j, w, t03, t03 + (two ? 2 * w : nt3 - 8 + w), gfl, x_, b1_, b2_, out_};
    tail_c(c, two, tid STA);
    ST(16);
    SLF();
    __builtin_amdgcn_endpgm();
  } else {
    asm volatile("; helper waves" ::: "memory");
    LANE_IDS
    const int v = w - 4;
    const int mcb = 16 + ((b - 64) & 31), mks = (b - 64) >> 5;
    const int row0 = 20 * ((v + idx) & 3) + q;
    v4u xr[5][4];
#pragma unroll
    for (int p = 0; p < 5; ++p) {
      int tok = 64 * g - 16 + row0 + 4 * p;
      tok = tok < 0 ? 0 : tok;
#pragma unroll
      for (int n = 0; n < 4; ++n) xr[p][n] = *(const GAS v4u*)((const GAS u16*)x_ + (size_t)tok * 512 + 128 * ((n + rq) & 3) + 8 * i);
    }
    ST(29);
    SBAR();
    GET_TAG
    ST(1);
    f2 xn[5][16];
#pragma unroll
    for (int p = 0; p < 5; ++p) {
      f2 s2 = {0.f, 0.f}, q2 = {0.f, 0.f};
#pragma unroll
      for (int e = 0; e < 16; ++e) {
        xn[p][e] = unpk(xr[p][e >> 2][e & 3]);
        s2 += xn[p][e];
        q2 = fma2(xn[p][e], xn[p][e], q2);
      }
      const float m = sum16(s2.x + s2.y) * (1.0f / 512.0f);
      const float r = __builtin_amdgcn_rsqf(fmaxf(sum16(q2.x + q2.y) * (1.0f / 512.0f) - m * m, 0.f) + 1e-5f);
      const f2 r2 = {r, r}, mr2 = {-m * r, -m * r};
#pragma unroll
      for (int e = 0; e < 16; ++e) { xn[p][e] = fma2(xn[p][e], r2, mr2); asm volatile("" : "+v"(xn[p][e])); }
    }
    int ha[5];
#pragma unroll
    for (int p = 0; p < 5; ++p) {
      const int r = row0 + 4 * p, qq = i & 3;
      ha[p] = ((i >> 2) * 5 + (r >> 4)) * 1024 + (qq * 16 + ((r & 15) ^ (qq | (i & 4)))) * 16;
      asm volatile("" : "+v"(ha[p]));
    }
    ST(2);
    { const int cb[4] = {4 * v, 4 * v + 1, 4 * v + 2, 4 * v + 3}; mod_fetch<4, true>(rs, L, cb, tag, l); }
    ST(3);
    const bool job2 = b >= 64 && b < 192;
    v2u cw2; v4u wm2[4];
    BAR();
    ST(4);
#pragma unroll
    for (int n = 0; n < 4; ++n) {
      const int ch = L_MODF + (16 * ((n + rq) & 3) + i) * 32;
      const f4 sa0 = *(LAS f4*)(L + ch), sa1 = *(LAS f4*)(L + ch + 16), ca0 = *(LAS f4*)(L + ch + 2048), ca1 = *(LAS f4*)(L + ch + 2064);
      const f2 sa2[4] = {{sa0.x, sa0.y}, {sa0.z, sa0.w}, {sa1.x, sa1.y}, {sa1.z, sa1.w}}, ca2[4] = {{ca0.x, ca0.y}, {ca0.z, ca0.w}, {ca1.x, ca1.y}, {ca1.z, ca1.w}};
      v4u o[5];
#pragma unroll
      for (int p = 0; p < 5; ++p)
#pragma unroll
        for (int e = 0; e < 4; ++e) {
          if (n == 0) o[p][e] = pk2(fma2(xn[p][4 * n + e], ca2[e], sa2[e]));
          else o[p][e] = pk(fma1(xn[p][4 * n + e].x, ca2[e].x, sa2[e].x), fma1(xn[p][4 * n + e].y, ca2[e].y, sa2[e].y));
        }
#pragma unroll
      for (int p = 0; p < 5; ++p) *(LAS v4u*)(L + ha[p] + n * 20480) = o[p];
      if (n == 2) dma3(rsq, w1v, ((13 + 4 * rq) & 15) * (32 * 3072), Lb + L_R1 + 2 * 12288 + v * 3072);
      if (n < 3) BAR();
      if (n == 0) ST(5);
      if (n == 1) {
        dma3(rsq, w1v, ((12 + 4 * rq) & 15) * (32 * 3072), Lb + L_R1 + 12288 + v * 3072);
        if (job2) mod_loads(c_, wmod_, mcb, mks, v, fresh(l), cw2, wm2);
        asm volatile("" ::: "memory");
      }
    }
    WAITV_BAR(0);
    ST(6);
    dma3(rsq, w1v, ((14 + 4 * rq) & 15) * (32 * 3072), Lb + L_R1 + 3 * 12288 + v * 3072);
    dma3(rsq, w1v, ((15 + 4 * rq) & 15) * (32 * 3072), Lb + L_R1 + 4 * 12288 + v * 3072);
    v4u rf;
    if (w == 7) rf = ldf(rs, l * 16, OFF_RFLAG);
    WAITV(0);
    SBAR();
    if (job2) mod_job(rs, L, L_MODF + 2048, mcb, mks, v, fresh(l), tag, cw2, wm2);
    if (w == 7) {
      _Pragma("nounroll") for (unsigned s = 0; s < SPIN_CAP; ++s) {
        if (__all(rf.x == tag && rf.y == tag && rf.z == tag && rf.w == tag)) break;
        __builtin_amdgcn_s_sleep(8);
        asm volatile("" ::: "memory");
        rf = ldf(rs, l * 16, OFF_RFLAG);
      }
      *(volatile LAS unsigned*)(L + L_OK) = tag;
      if (b == 0 && l == 0) *(GAS unsigned*)(ws + OFF_E) = tag;
    } else lds_wait(L, L_OK, tag);
    __builtin_amdgcn_fence(__ATOMIC_ACQUIRE, "wavefront");
    asm volatile("" ::: "memory");
    ST(31);
    const Ctx c = {rs, rsv, L, Lb, tag, g, j, w, t03, t03 + nt3 - 8 + w, gfl, x_, b1_, b2_, out_};
    const int nh = v & 1, kh = v >> 1;
    const unsigned w2off = OFF_W2T + ((j * 2 + nh) * 2 + kh) * 16384, w3off = OFF_W3T + c.tile0 * 16384;
    v4u w2[16], w3a[16], w3b[16], w4[44];
#pragma unroll
    for (int n = 0; n < 16; ++n) w2[n] = LDW(n, w2off);
    const v4u xres = load_xres(c, tid);
    const float b1a = bflo(((const GAS u16*)b1_)[c.tile0 * 16 + i]);
#pragma unroll
    for (int n = 0; n < 16; ++n) w3a[n] = LDW(n, w3off);
    SB();
    ST(19);
    if (w == 7) poll8(gfl, tag, l);
    SBAR();
    ST(8);
    f2 x1[4];
    {
      f4 acc[4][2];
      v4u a2[2][4];
      const int ab = kh * 32768 + l * 16;
      SBAR(); SL(20);
#pragma unroll
      for (int r = 0; r < 4; ++r) a2[0][r] = *(LAS v4u*)(L + ab + r * 1024);
#pragma unroll
      for (int n = 0; n < 8; ++n) {
        if (n == 1 || n == 3 || n == 5) SBAR();
        SB();
#pragma unroll
        for (int r = 0; r < 4; ++r) {
          MAC(n == 0, acc[r][0], w2[2 * n], a2[n & 1][r]);
          SB();
          if (n < 7) a2[(n + 1) & 1][r] = *(LAS v4u*)(L + ab + ((n + 1) * 4 + r) * 1024);
          SB();
          MAC(n == 0, acc[r][1], w2[2 * n + 1], a2[n & 1][r]);
          SB();
        }
      }
      MFMA_DONE();
      SL(21);
      const int lf = fresh(l), rb = L_RED + kh * 17408 + (lf & 15) * 272 + nh * 128 + (lf >> 4) * 16;
#pragma unroll
      for (int r = 0; r < 4; ++r)
#pragma unroll
        for (int t = 0; t < 2; ++t) *(LAS f4*)(L + rb + r * 16 * 272 + t * 64) = acc[r][t];
    }
    epi2(c, tid, xres, x1);
    ST(17);
    const unsigned w4off = OFF_W4T + ((j * 2 + nh) * 2 + kh) * 44 * 1024;
    asm volatile("" ::: "memory");
#pragma unroll
    for (int n = 0; n < 8; ++n) w4[n] = LDW(n, w4off);
    SB();
    float uvt[1];
    col_terms<1>(c, false, l, w3a, w3b, b1a, b1a, uvt);
    WAITV_BAR(8);
    ST(9);
    SBAR();
    ST(10);
    {
      f4 acc[4][1];
      const float S[5] = {0.f, 0.f, 0.f, 0.f, 0.f};
      mlp1_loop<1, 1>(c, l, w3a, w3b, acc, w4, w4off, false, S STA);
      BAR();
      ST(12);
      mlp1_epi<1>(c, false, l, uvt, acc);
    }
    ST(18);
    WAITV_BAR(0);
    if (tid == 256) __hip_atomic_store(gfl + 64 * 32 + j, tag, RLX_AGENT);
    ST(13);
#pragma unroll
    for (int n = 32; n < 44; ++n) w4[n] = LDW(n, w4off);
    const v4u b2v = load_b2(c, tid);
    {
      f4 acc[4][2];
      v4u a4[2][4];
      const int ab = kh * 4096 + l * 16;
      SBAR();
#pragma unroll
      for (int r = 0; r < 4; ++r) a4[0][r] = *(LAS v4u*)(L + ab + r * 1024);
#pragma unroll
      for (int n = 0; n < 22; ++n) {
        if (n == 1) { SBAR(); ST(14); }
        if ((n & 1) && n < 19) SBAR();
        if (n == 1) SL(26);
        if (n == 9) SL(27);
        if (n == 17) SL(28);
        SB();
        const int sp = 2 * (n + 1) >= 38 ? 2 * (n + 1) - 38 : 2 * (n + 1);
        if (n < 21 || kh == 0) {
#pragma unroll
          for (int r = 0; r < 4; ++r) {
            MAC(n == 0, acc[r][0], w4[2 * n], a4[n & 1][r]);
            SB();
            if (n < 21) a4[(n + 1) & 1][r] = *(LAS v4u*)(L + ab + sp * 4096 + r * 1024);
            SB();
            MAC(n == 0, acc[r][1], w4[2 * n + 1], a4[n & 1][r]);
            SB();
          }
        }
      }
      MFMA_DONE();
      ST(15);
      const int lf = fresh(l), rb = L_RED + kh * 17408 + (lf & 15) * 272 + nh * 128 + (lf >> 4) * 16;
#pragma unroll
      for (int r = 0; r < 4; ++r)
#pragma unroll
        for (int t = 0; t < 2; ++t) *(LAS f4*)(L + rb + r * 16 * 272 + t * 64) = acc[r][t];
    }
    epi4(c, tid, b2v, x1);
    ST(16);
    SLF();
    __builtin_amdgcn_endpgm();
  }
}

extern "C" void kernel_launch(void* const* d_in, const int* in_sizes, int n_in, void* d_out, int out_size, void* d_ws, size_t ws_size,
                              hipStream_t stream) {
  (void)hipFuncSetAttribute((const void*)fused_block, hipFuncAttributeMaxDynamicSharedMemorySize, LDS_BYTES);
  fused_block<<<256, 512, LDS_BYTES, stream>>>((const u16*)d_in[0], (const u16*)d_in[1], (const u16*)d_in[2], (const u16*)d_in[4],
                                               (const u16*)d_in[5], (const u16*)d_in[6], (const u16*)d_in[7], (const u16*)d_in[8],
                                               (const u16*)d_in[9], (u16*)d_out, (unsigned char*)d_ws);
}
```

```cpp
#include <hip/hip_runtime.h>
#include <stdint.h>

#define GAS __attribute__((address_space(1)))
#define LAS __attribute__((address_space(3)))
#define DI __device__ __forceinline__
typedef unsigned short u16;
typedef unsigned v4u __attribute__((ext_vector_type(4)));
typedef unsigned v2u __attribute__((ext_vector_type(2)));
typedef float f4 __attribute__((ext_vector_type(4)));
typedef float f2 __attribute__((ext_vector_type(2)));
typedef __bf16 bf2 __attribute__((ext_vector_type(2)));
typedef __bf16 bf8 __attribute__((ext_vector_type(8)));
typedef short s4 __attribute__((ext_vector_type(4)));
typedef GAS unsigned gu32;
typedef GAS unsigned long long gu64;
#define RLX_AGENT __ATOMIC_RELAXED, __HIP_MEMORY_SCOPE_AGENT

constexpr unsigned OFF_E = 0;
constexpr unsigned OFF_RFLAG = 768u << 10;
constexpr unsigned OFF_GFLAG = 4096;
constexpr unsigned OFF_MODP = 16384;
constexpr unsigned OFF_STAT = 65536;
constexpr unsigned OFF_W2T = 1u << 20;
constexpr unsigned OFF_W3T = OFF_W2T + 512 * 1024;
constexpr unsigned OFF_W4T = OFF_W3T + 1376 * 1024;
constexpr unsigned OFF_O = OFF_W4T + 1408 * 1024;
constexpr unsigned OFF_XA = OFF_O + 2048 * 1024;
constexpr unsigned OFF_H1 = OFF_XA + 2048 * 1024;
constexpr unsigned WS_USED = OFF_H1 + 32 * 43 * 4096;
constexpr unsigned OFF_STAMP = 64u << 20;

constexpr int L_RT = 49152;
constexpr int L_R1 = 81920;
constexpr int L_RED = 65536;
constexpr int L_STAT = 102400;
constexpr int L_UV = 118784;
constexpr int L_MODF = 155648;
constexpr int L_MODB = L_MODF + 4096 - 2048;
constexpr int L_MRED = 77824;
constexpr int L_SYNC = 79872;
constexpr int L_OK = L_MODF + 3584;
constexpr int L_COEF = L_MODF;
constexpr int L_RR = L_COEF + 1024;
constexpr int LDS_BYTES = 163840;

constexpr int CODE_STEP = 1216;
constexpr unsigned SPIN_CAP = 1u << 18;

#define ST(n)
#define SL(n)
#define SRT(n)
#define SLF()

DI float bflo(unsigned u) { return __uint_as_float(u << 16); }
DI float bfhi(unsigned u) { return __uint_as_float(u & 0xffff0000u); }
DI unsigned pk(float a, float b) { f2 v = {a, b}; return __builtin_bit_cast(unsigned, __builtin_convertvector(v, bf2)); }
DI f2 unpk(unsigned u) { return f2{bflo(u), bfhi(u)}; }
DI f2 fma2(f2 a, f2 b, f2 c) { return __builtin_elementwise_fma(a, b, c); }
DI unsigned pk2(f2 v) { return __builtin_bit_cast(unsigned, __builtin_convertvector(v, bf2)); }
DI f4 mfma32(v4u a, v4u b, f4 c) { return __builtin_amdgcn_mfma_f32_16x16x32_bf16(__builtin_bit_cast(bf8, a), __builtin_bit_cast(bf8, b), c, 0, 0, 0); }
template <bool FIRST> DI void mac(f4& c, v4u a, v4u b) {
  if constexpr (FIRST) asm volatile("v_mfma_f32_16x16x32_bf16 %0, %1, %2, 0" : "=&v"(c) : "v"(a), "v"(b));
  else asm volatile("v_mfma_f32_16x16x32_bf16 %0, %1, %2, %0" : "+v"(c) : "v"(a), "v"(b));
}
#define MAC(first, c, a, b) do { if (first) mac<true>(c, a, b); else mac<false>(c, a, b); } while (0)
#define MFMA_DONE() do { asm volatile("s_nop 15" ::: "memory"); __builtin_amdgcn_sched_barrier(0); } while (0)
DI f4 mfma16(v2u a, v2u b, f4 c) { return __builtin_amdgcn_mfma_f32_16x16x16bf16_1k(__builtin_bit_cast(s4, a), __builtin_bit_cast(s4, b), c, 0, 0, 0); }
template <int CTRL> DI float dpp(float x) { return __builtin_bit_cast(float, __builtin_amdgcn_update_dpp(0, __builtin_bit_cast(int, x), CTRL, 0xf, 0xf, true)); }
DI float swap16_sum(float x) { auto r = __builtin_amdgcn_permlane16_swap(__float_as_uint(x), __float_as_uint(x), false, false); return __uint_as_float(r[0]) + __uint_as_float(r[1]); }
DI float swap32_sum(float x) { auto r = __builtin_amdgcn_permlane32_swap(__float_as_uint(x), __float_as_uint(x), false, false); return __uint_as_float(r[0]) + __uint_as_float(r[1]); }
DI float swap16_max(float x) { auto r = __builtin_amdgcn_permlane16_swap(__float_as_uint(x), __float_as_uint(x), false, false); return fmaxf(__uint_as_float(r[0]), __uint_as_float(r[1])); }
DI float swap32_max(float x) { auto r = __builtin_amdgcn_permlane32_swap(__float_as_uint(x), __float_as_uint(x), false, false); return fmaxf(__uint_as_float(r[0]), __uint_as_float(r[1])); }
DI float sum8(float x) { x += dpp<0xB1>(x); x += dpp<0x4E>(x); x += dpp<0x141>(x); return x; }
DI float sum16(float x) { x = sum8(x); x += dpp<0x140>(x); return x; }

#define BAR() asm volatile("s_waitcnt lgkmcnt(0)\n\ts_barrier" ::: "memory")
#define WAITV(n) do { asm volatile("s_waitcnt vmcnt(%0)" ::"i"(n) : "memory"); if ((n) == 0) __builtin_amdgcn_s_waitcnt(0x0F70); } while (0)
#define WAITV_BAR(n) do { asm volatile("s_waitcnt vmcnt(%0) lgkmcnt(0)\n\ts_barrier" ::"i"(n) : "memory"); if ((n) == 0) __builtin_amdgcn_s_waitcnt(0x0F70); } while (0)
#define SBAR() asm volatile("s_barrier" ::: "memory")
#define USE(x) asm volatile("" ::"v"(x))

template <int N> DI void dma(v4u rs, unsigned voff, unsigned soff, unsigned lds) {
  unsigned keep;
  if constexpr (N == 1)
    asm volatile("s_mov_b32 %0, m0\n\ts_mov_b32 m0, %2\n\ts_nop 0\n\t"
                 "buffer_load_dwordx4 %1, %3, %4 offen sc1 lds\n\t"
                 "s_mov_b32 m0, %0" : "=&s"(keep) : "v"(voff), "s"(lds), "s"(rs), "s"(soff) : "memory");
  else if constexpr (N == 2)
    asm volatile("s_mov_b32 %0, m0\n\ts_mov_b32 m0, %2\n\ts_nop 0\n\t"
                 "buffer_load_dwordx4 %1, %3, %4 offen sc1 lds\n\t"
                 "buffer_load_dwordx4 %1, %3, %4 offen offset:1024 sc1 lds\n\t"
                 "s_mov_b32 m0, %0" : "=&s"(keep) : "v"(voff), "s"(lds), "s"(rs), "s"(soff) : "memory");
  else
    asm volatile("s_mov_b32 %0, m0\n\ts_mov_b32 m0, %2\n\ts_nop 0\n\t"
                 "buffer_load_dwordx4 %1, %3, %4 offen sc1 lds\n\t"
                 "buffer_load_dwordx4 %1, %3, %4 offen offset:1024 sc1 lds\n\t"
                 "buffer_load_dwordx4 %1, %3, %4 offen offset:2048 sc1 lds\n\t"
                 "buffer_load_dwordx4 %1, %3, %4 offen offset:3072 sc1 lds\n\t"
                 "s_mov_b32 m0, %0" : "=&s"(keep) : "v"(voff), "s"(lds), "s"(rs), "s"(soff) : "memory");
}
DI void dma3(v4u rs, unsigned voff, unsigned soff, unsigned lds) {
  unsigned keep;
  asm volatile("s_mov_b32 %0, m0\n\ts_mov_b32 m0, %2\n\ts_nop 0\n\t"
               "buffer_load_dwordx4 %1, %3, %4 offen lds\n\t"
               "buffer_load_dwordx4 %1, %3, %4 offen offset:1024 lds\n\t"
               "buffer_load_dwordx4 %1, %3, %4 offen offset:2048 lds\n\t"
               "s_mov_b32 m0, %0" : "=&s"(keep) : "v"(voff), "s"(lds), "s"(rs), "s"(soff) : "memory");
}
DI v4u trfrag(LAS char* p) {
  v2u lo = __builtin_bit_cast(v2u, __builtin_amdgcn_ds_read_tr16_b64_v4i16((LAS s4*)p));
  v2u hi = __builtin_bit_cast(v2u, __builtin_amdgcn_ds_read_tr16_b64_v4i16((LAS s4*)(p + 512)));
  return v4u{lo.x, lo.y, hi.x, hi.y};
}
DI int lane_id() { int r; asm volatile("v_mbcnt_lo_u32_b32 %0, -1, 0\n\tv_mbcnt_hi_u32_b32 %0, -1, %0" : "=v"(r)); return r; }
DI int fresh(int x) { asm volatile("" : "+v"(x)); return x; }
#define SB() __builtin_amdgcn_sched_barrier(0)
DI void gdma(const GAS void* p, unsigned lds) {
  unsigned keep;
  asm volatile("s_mov_b32 %0, m0\n\ts_mov_b32 m0, %2\n\ts_nop 0\n\tglobal_load_lds_dwordx4 %1, off nt\n\ts_mov_b32 m0, %0"
               : "=&s"(keep) : "v"(p), "s"(lds) : "memory");
}
typedef __amdgpu_buffer_rsrc_t rsrc_t;
DI v4u ldf(rsrc_t rs, unsigned voff, unsigned soff) { return __builtin_amdgcn_raw_buffer_load_b128(rs, voff, soff, 16); }
#define LDW(n, base) ldw_rt(c.rs, l * 16, base, n)
DI v4u ldw_rt(rsrc_t rs, unsigned voff, unsigned base, int n) { return __builtin_amdgcn_raw_buffer_load_b128(rs, voff + (n & 3) * 1024, base + (n >> 2) * 4096, 16); }
DI v4u ld_rflags(rsrc_t rs, int l) {
  v4u r;
#pragma unroll
  for (int k = 0; k < 4; ++k) r[k] = __builtin_amdgcn_raw_buffer_load_b32(rs, l * 128, OFF_RFLAG + k * 8192, 16);
  return r;
}
DI void stf(rsrc_t rs, v4u v, unsigned voff, unsigned soff) { __builtin_amdgcn_raw_buffer_store_b128(v, rs, voff + soff, 0, 16); }

DI void meet4(LAS char* L, int at, int me, unsigned tag, int l) {
  volatile LAS unsigned* f = (volatile LAS unsigned*)(L + at);
  f[me] = tag;
  _Pragma("nounroll") for (unsigned s = 0; s < 16 * SPIN_CAP; ++s)
    if (__all(f[l & 3] == tag)) break;
  asm volatile("" ::: "memory");
}
DI void lds_wait(LAS char* L, int at, unsigned tag) {
  volatile LAS unsigned* f = (volatile LAS unsigned*)(L + at);
  _Pragma("nounroll") for (unsigned s = 0; s < 16 * SPIN_CAP; ++s)
    if (*f == tag) break;
  asm volatile("" ::: "memory");
}
DI void poll8(gu32* fl, unsigned tag, int l) {
  _Pragma("nounroll") for (unsigned s = 0; s < SPIN_CAP; ++s) {
    unsigned f = __hip_atomic_load(fl + (l & 7), RLX_AGENT);
    if (__all(f == tag)) break;
    __builtin_amdgcn_s_sleep(1);
  }
  __builtin_amdgcn_fence(__ATOMIC_ACQUIRE, "wavefront");
}

DI unsigned gelu2(f2 a, f2 k1, f2 k2, f2 ir) {
  const f2 t = a * fma2(a * a, k2, k1);
  const f2 d = fma2(f2{__builtin_amdgcn_exp2f(t.x), __builtin_amdgcn_exp2f(t.y)}, ir, ir);
  return pk2(a * f2{__builtin_amdgcn_rcpf(d.x), __builtin_amdgcn_rcpf(d.y)});
}

template <int N, bool F32> DI void mod_finish(rsrc_t rs, LAS char* L, const int (&cb)[N], v4u (&v)[N], unsigned tb, int l) {
  _Pragma("nounroll") for (unsigned s = 0; s < SPIN_CAP; ++s) {
    unsigned d = 0;
#pragma unroll
    for (int n = 0; n < N; ++n) d |= (v[n].x ^ tb) | (v[n].y ^ tb) | (v[n].z ^ tb) | (v[n].w ^ tb);
    if (__all(d << 24 == 0u)) break;
    __builtin_amdgcn_s_sleep(1);
    asm volatile("" ::: "memory");
#pragma unroll
    for (int n = 0; n < N; ++n) v[n] = ldf(rs, l * 16, OFF_MODP + cb[n] * 1024);
  }
#pragma unroll
  for (int n = 0; n < N; ++n) {
    float a[2];
#pragma unroll
    for (int k = 0; k < 2; ++k) {
      auto r = __builtin_amdgcn_permlane32_swap(v[n][k] & 0xffffff00u, v[n][k + 2] & 0xffffff00u, false, false);
      a[k] = swap16_sum(__uint_as_float(r[0]) + __uint_as_float(r[1]));
    }
    f2 m = unpk(pk(a[0], a[1]));
    const float one = ((cb[n] >> 3) == 1 || (cb[n] >> 3) == 4) ? 1.0f : 0.0f;
    const unsigned p = pk(m.x + one, m.y + one);
    if (!(l & 16)) {
      if (F32) *(LAS f2*)(L + L_MODF + cb[n] * 256 + (l & 15) * 16 + (l >> 5) * 8) = unpk(p);
      else {
        const int o = (l & 15) * 8 + (l >> 5) * 4;
        *(LAS unsigned*)(L + L_MODB + cb[n] * 128 + o) = p;
        if (n < 4) {
          const int u = L_UV + (cb[n] < 32 ? 2048 : 0) + (cb[n] & 7) * 128 + o;
          *(LAS unsigned*)(L + u) = p; *(LAS unsigned*)(L + u + 1024) = p;
        }
      }
    }
  }
}
template <int N, bool F32> DI void mod_fetch(rsrc_t rs, LAS char* L, const int (&cb)[N], unsigned tb, int l) {
  v4u v[N];
#pragma unroll
  for (int n = 0; n < N; ++n) v[n] = ldf(rs, l * 16, OFF_MODP + cb[n] * 1024);
  mod_finish<N, F32>(rs, L, cb, v, tb, l);
}

#define STP
#define STA
DI float fma1(float a, float b, float c) { float r; asm("v_fma_f32 %0, %1, %2, %3" : "=v"(r) : "v"(a), "v"(b), "v"(c)); return r; }
#define W1POS(s) ((s) < 6 ? L_R1 + (s) * 12288 : (s) < 10 ? ((s) - 6) * 12288 : 65536)

DI void mod_loads(const u16* c_, const u16* wmod_, int mcb, int mks, int v, int l, v2u& cw, v4u (&wm)[4]) {
  const int r0 = 128 * mks + 32 * v + 4 * (l >> 3);
  asm volatile("global_load_dwordx2 %0, %1, %2" : "=v"(cw) : "v"(r0 * 2), "s"(c_));
#pragma unroll
  for (int e = 0; e < 4; ++e) asm volatile("global_load_dwordx4 %0, %1, %2 nt" : "=v"(wm[e]) : "v"((r0 + e) * 6144 + 128 * mcb + 16 * (l & 7)), "s"(wmod_));
}
#define MOD_WAIT(n) asm volatile("s_waitcnt vmcnt(%5)" : "+v"(cw), "+v"(wm[0]), "+v"(wm[1]), "+v"(wm[2]), "+v"(wm[3]) : "i"(n) : "memory")
DI v4u mod_job(LAS char* L, int red, int v, int l, unsigned tag, v2u cw, const v4u (&wm)[4]) {
  v4u res = {0u, 0u, 0u, 0u};
  f2 a2[4];
#pragma unroll
  for (int e = 0; e < 4; ++e) {
    const float cv = (e & 1) ? bfhi(cw[e >> 1]) : bflo(cw[e >> 1]);
    float sv = cv * __builtin_amdgcn_rcpf(1.0f + __builtin_amdgcn_exp2f(-1.4426950409f * cv));
    sv = bflo(pk(sv, sv));
    const f2 s2 = {sv, sv};
#pragma unroll
    for (int k = 0; k < 4; ++k) a2[k] = e ? fma2(s2, unpk(wm[e][k]), a2[k]) : s2 * unpk(wm[e][k]);
  }
  float a[4];
#pragma unroll
  for (int k = 0; k < 4; ++k) {
    auto r = __builtin_amdgcn_permlane32_swap(__float_as_uint(a2[k >> 1][k & 1]), __float_as_uint(a2[2 + (k >> 1)][k & 1]), false, false);
    a[k] = __uint_as_float(r[0]) + __uint_as_float(r[1]);
  }
#pragma unroll
  for (int k = 0; k < 2; ++k) {
    auto r = __builtin_amdgcn_permlane16_swap(__float_as_uint(a[k]), __float_as_uint(a[k + 2]), false, false);
    a[k] = __uint_as_float(r[0]) + __uint_as_float(r[1]);
    a[k] += dpp<0x128>(a[k]);
  }
  if (!(l & 8)) *(LAS f2*)(L + red + v * 256 + (8 * (l & 7) + 2 * ((l >> 4) & 1) + 4 * (l >> 5)) * 4) = f2{a[0], a[1]};
  asm volatile("s_waitcnt lgkmcnt(0)" ::: "memory");
  volatile LAS unsigned* mf = (volatile LAS unsigned*)(L + red + 1024);
  if (v < 3) mf[v] = tag;
  else {
    _Pragma("nounroll") for (unsigned s = 0; s < SPIN_CAP; ++s)
      if (__all(mf[l < 3 ? l : 0] == tag)) break;
    asm volatile("" ::: "memory");
    f4 s = *(LAS f4*)(L + red + (l & 15) * 16);
#pragma unroll
    for (int ww = 1; ww < 4; ++ww) s += *(LAS f4*)(L + red + ww * 256 + (l & 15) * 16);
    res = (__builtin_bit_cast(v4u, s) & 0xffffff00u) | (tag & 0xffu);
  }
  return res;
}
DI void mod_publish(rsrc_t rs, int mcb, int mks, int v, int l, v4u res) {
  if (v == 3 && l < 16) stf(rs, res, l * 16, OFF_MODP + mcb * 1024 + mks * 256);
}

struct Ctx {
  rsrc_t rs; v4u rsv; LAS char* L; unsigned Lb, tag; int g, j, w, t03, tile0; gu32* gfl;
  const u16 *x_, *b1_, *b2_; u16* out_;
};

template <int T> DI void col_terms(const Ctx& c, bool two, int l, const v4u (&w3a)[16], const v4u (&w3b)[16], float b1a, float b1b, float (&uvt)[T]) {
  l = fresh(l);
  const int i = l & 15, q = l >> 4, ty = i >> 2;
  const int base = L_UV + (ty < 2 ? ty * 2048 + c.j * 128 : 2048 + ty * 1024) + q * 16;
  f4 a[2][T];
  v4u am[16];
#pragma unroll
  for (int s = 0; s < 16; ++s) am[s] = *(LAS v4u*)(c.L + base + s * 64);
  SB();
#pragma unroll
  for (int s = 0; s < 16; ++s) MAC(s < 2, a[s & 1][0], am[s], w3a[s]);
  if (T == 2 && two) {
#pragma unroll
    for (int s = 0; s < 16; ++s) MAC(s < 2, a[s & 1][T - 1], am[s], w3b[s]);
  }
  MFMA_DONE();
  uvt[0] = a[0][0].x + a[1][0].x + (q == 3 ? b1a : 0.f);
  if (T == 2) uvt[T - 1] = a[0][T - 1].x + a[1][T - 1].x + (q == 3 ? b1b : 0.f);
}

DI void epi2(const Ctx& c, int tid, v4u xres, f2 (&x1)[4]) {
  LAS char* L = c.L;
  tid = fresh(tid);
  const int etok = tid >> 3, ef = 64 * c.j + 8 * (tid & 7);
  const v4u gav = *(LAS v4u*)(L + L_MODB + 2048 + ef * 2), sbv = *(LAS v4u*)(L + L_MODB + 3072 + ef * 2), a2v = *(LAS v4u*)(L + L_MODB + 4096 + ef * 2);
  BAR();
  const int ra = L_RED + etok * 272 + (tid & 7) * 32;
  const f4 y0 = *(LAS f4*)(L + ra) + *(LAS f4*)(L + ra + 17408), y1 = *(LAS f4*)(L + ra + 16) + *(LAS f4*)(L + ra + 17408 + 16);
  f2 p[4], a2[4];
#pragma unroll
  for (int e = 0; e < 4; ++e) {
    const f2 y = e == 0 ? f2{y0.x, y0.y} : e == 1 ? f2{y0.z, y0.w} : e == 2 ? f2{y1.x, y1.y} : f2{y1.z, y1.w};
    a2[e] = unpk(a2v[e]);
    x1[e] = fma2(unpk(gav[e]), y, unpk(xres[e]));
    p[e] = x1[e] * a2[e];
  }
  stf(c.rs, v4u{pk2(p[0]), pk2(p[1]), pk2(p[2]), pk2(p[3])},
      (((tid & 7) >> 2) * 4 + (etok >> 4)) * 1024 + ((tid & 3) * 16 + (etok & 15)) * 16, OFF_XA + (c.g * 16 + 2 * c.j) * 4096);
  SB();
  f2 vx = x1[0], vxx = x1[0] * x1[0], vp = p[0], vpp = p[0] * p[0], vpa = p[0] * a2[0], vps = p[0] * unpk(sbv[0]);
#pragma unroll
  for (int e = 1; e < 4; ++e) {
    vx += x1[e]; vxx = fma2(x1[e], x1[e], vxx); vp += p[e]; vpp = fma2(p[e], p[e], vpp); vpa = fma2(p[e], a2[e], vpa); vps = fma2(p[e], unpk(sbv[e]), vps);
  }
  const float sx = sum8(vx.x + vx.y), sxx = sum8(vxx.x + vxx.y), sp = sum8(vp.x + vp.y), spp = sum8(vpp.x + vpp.y), spa = sum8(vpa.x + vpa.y), sps = sum8(vps.x + vps.y);
  const bool first = (tid & 7) == 0;
  if ((tid & 7) < 2) stf(c.rs, __builtin_bit_cast(v4u, first ? f4{sx, sxx, sp, spp} : f4{spa, sps, spa, sps}), etok * 16 + (first ? 0 : 1024), OFF_STAT + (c.g * 8 + c.j) * 2048);
}

template <int T, int KIND>
DI void mlp1_loop(const Ctx& c, int l, const v4u (&w3a)[16], const v4u (&w3b)[16], f4 (&acc)[4][T], v4u (&w4)[44], unsigned w4off, bool two,
                  const float (&S)[5] STP) {
  LAS char* L = c.L;
  const int tid = c.w * 64 + l;
  auto xa_slot = [&](int slot) { dma<4>(c.rsv, l * 16, OFF_XA + (c.g * 16 + ((slot + 2 * c.j) & 15)) * 4096, c.Lb + slot * 4096); };
  v4u a[2][4];
#pragma unroll
  for (int r = 0; r < 4; ++r) a[0][r] = *(LAS v4u*)(L + r * 1024 + l * 16);
#pragma unroll
  for (int s = 0; s < 16; ++s) {
    if (s == 1 || s == 5 || s == 9 || s == 13) {
      if (KIND == 2 && c.w == 3) {
        if (s == 1) WAITV(16);
        if (s == 5) WAITV(40);
        if (s == 9) WAITV(24);
        if (s == 13) WAITV(0);
      }
      SBAR();
      SL(22 + s / 4);
      if (KIND == 2 && c.w == 3) {
        if (s == 1) {
#pragma unroll
          for (int k = 10; k < 16; ++k) xa_slot(k);
#pragma unroll
          for (int p = 0; p < 4; ++p) dma<4>(c.rsv, l * 16, OFF_STAT + c.g * 16384 + p * 4096, c.Lb + L_STAT + p * 4096);
        }
        if (s == 13) {
          float sx = 0, sxx = 0, sp = 0, spp = 0, spa = 0, sps = 0;
#pragma unroll
          for (int p = 0; p < 8; ++p) {
            const f4 a4 = *(LAS f4*)(L + L_STAT + p * 2048 + l * 16);
            const f2 b = *(LAS f2*)(L + L_STAT + p * 2048 + 1024 + l * 16);
            sx += a4.x; sxx += a4.y; sp += a4.z; spp += a4.w; spa += b.x; sps += b.y;
          }
          const float inv = 1.0f / 512.0f;
          const float m = sx * inv, var = fmaxf(sxx * inv - m * m, 0.f);
          const float sd = __builtin_amdgcn_sqrtf(var + 1e-5f), r = __builtin_amdgcn_rcpf(sd);
          const float m2 = (r * (sp - m * S[0]) + S[1]) * inv;
          const float e2 = (r * r * (spp - 2.f * m * spa + m * m * S[2]) + 2.f * r * (sps - m * S[3]) + S[4]) * inv;
          const float sd2 = __builtin_amdgcn_sqrtf(fmaxf(e2 - m2 * m2, 0.f) + 1e-5f);
          const float R = r * __builtin_amdgcn_rcpf(sd2), k1 = -2.302208198f * R;
          *(LAS f4*)(L + L_COEF + l * 16) = f4{-m, sd, -m2 * sd, sd * sd2};
          *(LAS f4*)(L + L_RR + l * 16) = f4{k1, 0.044715f * k1 * R * R, sd * sd2, 0.f};
        }
      }
    }
    SB();
#pragma unroll
    for (int r = 0; r < 4; ++r) {
      MAC(s == 0, acc[r][0], w3a[s], a[s & 1][r]);
      SB();
      if (s < 15) a[(s + 1) & 1][r] = *(LAS v4u*)(L + ((s + 1) * 4 + r) * 1024 + l * 16);
      SB();
    }
    if (T == 2 && two) {
#pragma unroll
      for (int r = 0; r < 4; ++r) MAC(s == 0, acc[r][T - 1], w3b[s], a[s & 1][r]);
      SB();
    }
    if (KIND == 1 && s >= 2 && s < 14) {
      w4[2 * s + 4] = LDW(2 * s + 4, w4off);
      w4[2 * s + 5] = LDW(2 * s + 5, w4off);
      SB();
    }
  }
  MFMA_DONE();
}

template <int T> DI void mlp1_epi(const Ctx& c, bool two, int l, const float (&uvt)[T], f4 (&acc)[4][T]) {
  l = fresh(l);
  const int i = l & 15, q = l >> 4;
  float cf[4]; f4 rr[4];
#pragma unroll
  for (int r = 0; r < 4; ++r) {
    cf[r] = *(LAS float*)(c.L + L_COEF + (r * 16 + i) * 16 + q * 4);
    rr[r] = *(LAS f4*)(c.L + L_RR + (r * 16 + i) * 16);
  }
#pragma unroll
  for (int t = 0; t < T; ++t) {
    if (t && !two) break;
#pragma unroll
    for (int r = 0; r < 4; ++r) acc[r][t] = __builtin_amdgcn_mfma_f32_16x16x4f32(uvt[t], cf[r], acc[r][t], 0, 0, 0);
  }
  SB();
#pragma unroll
  for (int t = 0; t < T; ++t) {
    if (t && !two) break;
    const int tl = c.tile0 + t;
#pragma unroll
    for (int pp = 0; pp < 2; ++pp) {
      unsigned h[2][2];
#pragma unroll
      for (int r2 = 0; r2 < 2; ++r2) {
        const int r = 2 * pp + r2;
        const f2 k1 = {rr[r].x, rr[r].x}, k2 = {rr[r].y, rr[r].y}, ir = {rr[r].z, rr[r].z};
        h[r2][0] = gelu2(f2{acc[r][t].x, acc[r][t].y}, k1, k2, ir);
        h[r2][1] = gelu2(f2{acc[r][t].z, acc[r][t].w}, k1, k2, ir);
      }
      auto s0 = __builtin_amdgcn_permlane16_swap(h[0][0], h[1][0], false, false);
      auto s1 = __builtin_amdgcn_permlane16_swap(h[0][1], h[1][1], false, false);
      stf(c.rs, v4u{s0[0], s1[0], s0[1], s1[1]}, (q & 1) * 1024 + ((q >> 1) * 16 + i) * 16,
          OFF_H1 + ((c.g * 43 + (tl >> 1)) * 4 + 2 * pp) * 1024 + (tl & 1) * 512);
      SB();
    }
  }
}

DI void epi4(const Ctx& c, int tid, v4u b2v, const f2 (&x1)[4]) {
  LAS char* L = c.L;
  tid = fresh(tid);
  const int etok = tid >> 3, ef = 64 * c.j + 8 * (tid & 7);
  const v4u gbv = *(LAS v4u*)(L + L_MODB + 5120 + ef * 2);
  BAR();
  const int ra = L_RED + etok * 272 + (tid & 7) * 32;
  const f4 y0 = *(LAS f4*)(L + ra) + *(LAS f4*)(L + ra + 17408), y1 = *(LAS f4*)(L + ra + 16) + *(LAS f4*)(L + ra + 17408 + 16);
  v4u o;
#pragma unroll
  for (int e = 0; e < 4; ++e) {
    const f2 y = e == 0 ? f2{y0.x, y0.y} : e == 1 ? f2{y0.z, y0.w} : e == 2 ? f2{y1.x, y1.y} : f2{y1.z, y1.w};
    o[e] = pk2(fma2(unpk(gbv[e]), y + unpk(b2v[e]), x1[e]));
  }
  const rsrc_t ro = __builtin_amdgcn_make_buffer_rsrc((void*)c.out_, 0, 2048 * 512 * 2, 0x00020000);
  __builtin_amdgcn_raw_buffer_store_b128(o, ro, etok * 1024 + (tid & 7) * 16 + c.g * 65536 + c.j * 128, 0, 16);
}
DI v4u load_xres(const Ctx& c, int tid) {
  const rsrc_t rx = __builtin_amdgcn_make_buffer_rsrc((void*)c.x_, 0, 2048 * 512 * 2, 0x00020000);
  return __builtin_amdgcn_raw_buffer_load_b128(rx, (tid >> 3) * 1024 + (tid & 7) * 16, c.g * 65536 + c.j * 128, 0);
}
DI v4u load_b2(const Ctx& c, int tid) {
  const rsrc_t rb2 = __builtin_amdgcn_make_buffer_rsrc((void*)c.b2_, 0, 1024, 0x00020000);
  return __builtin_amdgcn_raw_buffer_load_b128(rb2, (tid & 7) * 16, c.j * 128, 0);
}

DI void tail_c(const Ctx& c, bool two, int tid STP) {
  tid = fresh(tid);
  const int l = tid & 63, i = l & 15, w = c.w, g = c.g, j = c.j;
  LAS char* L = c.L;
  const unsigned w3off = OFF_W3T + c.tile0 * 16384;
  v4u w3a[16], w3b[16], w4[44];
  WAITV(0);
  meet4(L, L_OK + 16, w, c.tag, l);
  if (tid == 0) __hip_atomic_store(c.gfl + j, c.tag, RLX_AGENT);
  ST(11);
  auto o_tick = [&](int m) {
    dma<4>(c.rsv, l * 16, OFF_O + (g * 16 + ((4 * m + w + 2 * j) & 15)) * 4096, c.Lb + (4 * m + w) * 4096);
  };
  if (w < 2) o_tick(0);
  lds_wait(L, L_OK, c.tag);
  const int c0 = 24 + 4 * w, cb5[5] = {c0, c0 + 1, c0 + 2, c0 + 3, ((w & 1) ? 40 : 16) + j};
  v4u mv[5];
#pragma unroll
  for (int k = 0; k < 5; ++k) mv[k] = ldf(c.rs, l * 16, OFF_MODP + cb5[k] * 1024);
  const float b1a = bflo(((const GAS u16*)c.b1_)[c.tile0 * 16 + i]), b1b = bflo(((const GAS u16*)c.b1_)[(c.tile0 + (two ? 1 : 0)) * 16 + i]);
  const v4u xres = load_xres(c, tid);
#pragma unroll
  for (int n = 0; n < 16; ++n) w3a[n] = LDW(n, w3off);
  SB();
  mod_finish<5, false>(c.rs, L, cb5, mv, c.tag, l);
  if (w < 2) { const unsigned f = w ? 0u : 0x3f803f80u; *(LAS v4u*)(L + L_UV + 4096 + w * 1024 + l * 16) = v4u{f, f, f, f}; }
  if (w == 3) poll8(c.gfl, c.tag, l);
  BAR();
  ST(8);
  if (w >= 2) o_tick(0);
  o_tick(1);
  WAITV_BAR(4); SL(20);
  o_tick(2);
  WAITV_BAR(4);
  o_tick(3);
  WAITV_BAR(4);
  WAITV_BAR(0); SL(21);
  USE(xres); USE(b1a); USE(b1b);
  float S[5] = {0.f, 0.f, 0.f, 0.f, 0.f};
  if (two) {
#pragma unroll
    for (int n = 0; n < 16; ++n) w3b[n] = LDW(16 + n, w3off);
  }
  f2 x1[4];
  epi2(c, tid, xres, x1);
  ST(17);
  if (w == 3) {
    const v4u av = *(LAS v4u*)(L + L_MODB + 4096 + l * 16), sv = *(LAS v4u*)(L + L_MODB + 3072 + l * 16);
#pragma unroll
    for (int e = 0; e < 4; ++e) {
      const float a0 = bflo(av[e]), a1 = bfhi(av[e]), s0 = bflo(sv[e]), s1 = bfhi(sv[e]);
      S[0] += a0 + a1; S[1] += s0 + s1; S[2] += a0 * a0 + a1 * a1; S[3] += a0 * s0 + a1 * s1; S[4] += s0 * s0 + s1 * s1;
    }
#pragma unroll
    for (int e = 0; e < 5; ++e) S[e] = swap32_sum(swap16_sum(sum16(S[e])));
  }
  WAITV_BAR(0);
  if (tid == 128) __hip_atomic_store(c.gfl + 32 * 32 + j, c.tag, RLX_AGENT);
  ST(9);
#pragma unroll
  for (int n = 0; n < 16; ++n) { USE(w3a[n]); USE(w3b[n]); }
  if (w < 2) dma<4>(c.rsv, l * 16, OFF_XA + (g * 16 + 2 * j + w) * 4096, c.Lb + w * 4096);
  float uvt[2];
  col_terms<2>(c, two, l, w3a, w3b, b1a, b1b, uvt);
  USE(uvt[0]); USE(uvt[1]);
  if (w < 2) WAITV(0);
  if (w == 3) {
    poll8(c.gfl + 32 * 32, c.tag, l);
    ST(15);
#pragma unroll
    for (int k = 2; k < 10; ++k) dma<4>(c.rsv, l * 16, OFF_XA + (g * 16 + ((k + 2 * j) & 15)) * 4096, c.Lb + k * 4096);
  }
  SBAR();
  ST(10);
  f4 acc[4][2];
  mlp1_loop<2, 2>(c, l, w3a, w3b, acc, w4, 0, two, S STA);
  BAR();
  ST(12);
  mlp1_epi<2>(c, two, l, uvt, acc);
  ST(18);
  WAITV_BAR(0);
  ST(13);
  const int rot4 = (c.t03 + 1) >> 1;
  auto h_tick = [&](int m) {
    const int sg = 4 * m + w;
    int k = rot4 + sg; if (k >= 43) k -= 43;
    dma<4>(c.rsv, l * 16, OFF_H1 + (g * 43 + k) * 4096, c.Lb + (sg >= 38 ? sg - 38 : sg) * 4096);
  };
  h_tick(0);
  const v4u b2v = load_b2(c, tid);
  if (w == 3) poll8(c.gfl + 64 * 32, c.tag, l);
  WAITV_BAR(0);
  USE(b2v);
  ST(14);
  h_tick(1); h_tick(2); h_tick(3);
#pragma unroll
  for (int m = 1; m < 10; ++m) {
    if (m == 1 || m == 7) WAITV_BAR(8);
    else if (m < 7) WAITV_BAR(12);
    else if (m == 8) WAITV_BAR(4);
    else WAITV_BAR(0);
    if (m == 1) SL(26);
    if (m == 5) SL(27);
    if (m == 9) SL(28);
    if (m == 1) h_tick(4);
    if (m + 4 < 10 || (m + 4 == 10 && w < 3)) h_tick(m + 4);
  }
  epi4(c, tid, b2v, x1);
}

extern "C" __global__ void __launch_bounds__(512, 2)
fused_block(const u16* __restrict__ x_, const u16* __restrict__ wqkv_, const u16* __restrict__ wo_, const u16* __restrict__ w1_,
            const u16* __restrict__ b1_, const u16* __restrict__ w2_, const u16* __restrict__ b2_, const u16* __restrict__ c_,
            const u16* __restrict__ wmod_, u16* __restrict__ out_, unsigned char* __restrict__ ws_) {
  extern __shared__ __attribute__((aligned(16))) char smem[];
  asm volatile("" ::"s"(x_), "s"(wqkv_), "s"(wo_), "s"(w1_), "s"(b1_), "s"(w2_), "s"(b2_), "s"(c_), "s"(wmod_), "s"(out_), "s"(ws_));
  unsigned long long pc;
  asm volatile("s_getpc_b64 %0" : "=s"(pc));
  LAS char* L = (LAS char*)smem;
  const unsigned Lb = (unsigned)(uintptr_t)L;
  const int tid0 = threadIdx.x, w = __builtin_amdgcn_readfirstlane(tid0 >> 6);
  const int b = blockIdx.x, xcd = b & 7, idx = b >> 3, g = xcd * 4 + (idx >> 3), j = idx & 7;
  GAS unsigned char* ws = (GAS unsigned char*)ws_;
  const rsrc_t rs = __builtin_amdgcn_make_buffer_rsrc((void*)ws_, 0, WS_USED, 0x00020000);
  const v4u rsv = {(unsigned)(uintptr_t)ws_, (unsigned)((uintptr_t)ws_ >> 32) & 0xffffu, WS_USED, 0x00020000u};
  SRT(32);
  ST(0);
  unsigned epoch = *(const __attribute__((address_space(4))) unsigned*)(uintptr_t)(ws_ + OFF_E);
#define GET_TAG asm volatile("" : "+s"(epoch) : : "memory"); const unsigned tag = epoch + 1u;
  const int rq = xcd >> 1;
  const int nt3 = j < 6 ? 11 : 10, t03 = j < 6 ? 11 * j : 66 + 10 * (j - 6);
  gu32* gfl = (gu32*)(ws + OFF_GFLAG) + g * 32;
  const v4u rsq = {(unsigned)(uintptr_t)wqkv_, (unsigned)((uintptr_t)wqkv_ >> 32) & 0xffffu, 512u * 1536u * 2u, 0x00020000u};
#define UNIT ((b - 50) * 4 + w)
#define W1V ((8 * (w & 3) + (l >> 3)) * 3072 + 128 * j + 16 * ((l & 7) ^ ((((l >> 4) & 1) | ((w & 1) << 1)) << 1)))
#define LANE_IDS const int l = lane_id(), tid = w * 64 + l, i = l & 15, q = l >> 4; const unsigned w1v = W1V;
  v4u xr[5][4];
  const bool job1 = b < 64;
#define XLOADS(p) do { int tok = 64 * g - 16 + 20 * (w & 3) + (l >> 4) + 4 * (p); tok = tok < 0 ? 0 : tok; \
    _Pragma("unroll") for (int n = 0; n < 4; ++n) \
      asm volatile("global_load_dwordx4 %0, %1, %2" : "=v"(xr[p][n]) : "v"(tok * 1024 + 256 * ((n + rq) & 3) + 16 * (l & 15)), "s"(x_)); } while (0)
  if (w < 4) {
    const int l = lane_id(), i = l & 15, q = l >> 4;
    const int mcb = job1 ? (b & 15) : 16 + ((b - 64) & 31), mks = job1 ? (b >> 4) : ((b - 64) >> 5);
    v2u cw; v4u wm[4];
    if (b < 192) mod_loads(c_, wmod_, mcb, mks, w, l, cw, wm);
    unsigned dummy;
    asm volatile("s_load_dword %0, %1, 0x0\n\t"
                 "s_load_dword %0, %1, 0x40\n\t"
                 "s_load_dword %0, %1, 0x80\n\t"
                 "s_load_dword %0, %1, 0xc0\n\t"
                 "s_load_dword %0, %1, 0x100\n\t"
                 "s_load_dword %0, %1, 0x140" : "=&s"(dummy) : "s"((pc & ~255ull) + idx * CODE_STEP + w * 384));
    if (job1) {
      asm volatile("" : "+s"(epoch) : : "memory");
      MOD_WAIT(0);
      mod_publish(rs, mcb, mks, w, l, mod_job(L, L_MRED, w, l, epoch + 1u, cw, wm));
      SBAR();
    }
    const int u = UNIT;
    const bool retile = b >= 50, isw1 = u >= 128 && u < 480, isw2 = u >= 480;
    const int rcb = isw2 ? (int)((unsigned)(u - 480) / 43u) : ((u - (isw1 ? 128 : 0)) >> 4), rs_ = isw2 ? u - 480 - 43 * rcb : (u & 15);
    const int rnt = (isw1 && rcb == 21) ? 2 : 4;
    if (retile) {
      const GAS u16* src = (const GAS u16*)(isw2 ? w2_ : isw1 ? w1_ : wo_);
      const int n = isw1 ? 1376 : 512, ch = rnt == 2 ? (l & 3) : (l & 7);
      const GAS u16* p = src + (32 * rs_ + (l >> 3)) * n + 64 * rcb + 8 * ch;
#pragma unroll
      for (int e = 0; e < 4; ++e) gdma(p + 8 * e * n, Lb + L_RT + w * 4096 + e * 1024);
    }
    ST(29);
    const unsigned w1v = W1V;
#pragma unroll
    for (int s = 0; s < 11; ++s) dma3(rsq, w1v, ((s + 4 * rq) & 15) * (32 * 3072), Lb + W1POS(s) + w * 3072);
    ST(30);
    int rdst[4];
    const int rcbv = fresh(rcb), rsv_ = fresh(rs_);
#pragma unroll
    for (int t = 0; t < 4; ++t) {
      const int rcb = rcbv, rs_ = rsv_;
      const int tl = 4 * rcb + t;
      const int j3 = tl < 66 ? (int)((unsigned)tl / 11u) : (tl < 76 ? 6 : 7);
      const int s2 = (rs_ - 2 * (isw1 ? j3 : rcb)) & 15;
      int s4 = rs_ - (rcb < 6 ? (11 * rcb + 1) >> 1 : 3 + 5 * rcb); if (s4 < 0) s4 += 43;
      rdst[t] = isw1 ? OFF_W3T + (tl * 16 + s2) * 1024
              : isw2 ? OFF_W4T + ((((rcb * 2 + (t >> 1)) * 2 + (s4 & 1)) * 22 + (s4 >> 1)) * 2 + (t & 1)) * 1024
                     : OFF_W2T + ((((rcb * 2 + (t >> 1)) * 2 + (s2 & 1)) * 8 + (s2 >> 1)) * 2 + (t & 1)) * 1024;
    }
    asm volatile("s_waitcnt lgkmcnt(0)" : "+s"(dummy), "+s"(epoch) : : "memory");
    v4u res;
    if (!job1 && b < 192) { MOD_WAIT(33); res = mod_job(L, L_MRED, w, l, epoch + 1u, cw, wm); }
    ST(1);
    if (retile) {
      WAITV(33);
#pragma unroll
      for (int t = 0; t < 4; ++t)
        if (t < rnt) stf(rs, trfrag(L + L_RT + w * 4096 + (8 * q + (i >> 2)) * 128 + t * 32 + (i & 3) * 8), l * 16, rdst[t]);
      if (rnt == 4) WAITV(4); else WAITV(2);
    } else WAITV(0);
    if (!job1 && b < 192) mod_publish(rs, mcb, mks, w, l, res);
  } else {
    const int l = lane_id();
    __builtin_amdgcn_s_setprio(3);
    if (job1) SBAR();
    XLOADS(0); XLOADS(1); XLOADS(2);
    ST(29);
    SB();
  }
  if (w < 4) {
    asm volatile("; matrix waves" ::: "memory");
    LANE_IDS
    GET_TAG
    ST(2);
    meet4(L, L_SYNC, w, tag, l);
    ST(3);
    const int tb_ = q * 3072 + (i >> 2) * 128 + ((w ^ ((i >> 3) | ((q & 1) << 1))) * 32) + (i & 3) * 8;
    const int tb1 = fresh(tb_ + L_R1);
    v4u wf[16][3];
#pragma unroll
    for (int s = 0; s < 11; ++s)
#pragma unroll
      for (int sg = 0; sg < 3; ++sg) wf[s][sg] = trfrag(L + (s < 5 ? tb1 + s * 12288 : tb_ + W1POS(s)) + sg * 1024);
    ST(7);
    BAR();
    ST(4);
    dma3(rsq, w1v, ((11 + 4 * rq) & 15) * (32 * 3072), Lb + L_R1 + w * 3072);
    f4 aq[4], ak[5], av[5];
    const int a0 = l * 16 ^ (q << 4), a1 = l * 16 ^ ((q | 4) << 4), a0h = fresh(a0 + 61440), a1h = fresh(a1 + 61440);
    v4u a[2][5];
    SBAR();
    ST(5);
#pragma unroll
    for (int r = 0; r < 5; ++r) a[0][r] = *(LAS v4u*)(L + r * 1024 + a0);
#pragma unroll
    for (int s = 0; s < 16; ++s) {
      if (s == 3) WAITV(0);
      if (s == 3 || s == 7 || s == 10 || s == 13) SBAR();
      if (s == 3 && tid == 0) __hip_atomic_store((gu32*)(ws + OFF_RFLAG) + 32 * b, tag, RLX_AGENT);
      if (s == 10) SL(31);
      SB();
#pragma unroll
      for (int r = 0; r < 5; ++r) {
        MAC(s == 0, ak[r], wf[s][1], a[s & 1][r]);
        SB();
        if (s < 11) a[(s + 1) & 1][r] = *(LAS v4u*)(L + ((s + 1) * 5 + r) * 1024 + (((s + 1) & 1) ? a1 : a0));
        else if (s < 15) a[(s + 1) & 1][r] = *(LAS v4u*)(L + ((s - 11) * 5 + r) * 1024 + (((s + 1) & 1) ? a1h : a0h));
        SB();
        MAC(s == 0, av[r], a[s & 1][r], wf[s][2]);
        SB();
        if (s >= 10 && s < 15 && r < 3) { wf[s + 1][r] = trfrag(L + tb1 + (s - 10) * 12288 + r * 1024); SB(); }
        if (r) { MAC(s == 0, aq[r - 1], wf[s][0], a[s & 1][r]); SB(); }
      }
    }
    MFMA_DONE();
    ST(6);
    {
      v2u qb[4], kb[5], vb[5];
#pragma unroll
      for (int r = 0; r < 5; ++r) {
        kb[r] = v2u{pk(ak[r].x, ak[r].y), pk(ak[r].z, ak[r].w)};
        vb[r] = v2u{pk(av[r].x, av[r].y), pk(av[r].z, av[r].w)};
        if (r) qb[r - 1] = v2u{pk(aq[r - 1].x, aq[r - 1].y), pk(aq[r - 1].z, aq[r - 1].w)};
      }
      const bool odd = i & 1;
      const bool cur0 = 4 * q + (i & 1) <= i, cur1 = 4 * q + 2 + (i & 1) <= i;
      const float sc = 0.25f * 1.4426950409f;
      const int hd = 4 * j + w;
      unsigned h[4][2];
#pragma unroll
      for (int r = 0; r < 4; ++r) {
        const f4 z = {0.f, 0.f, 0.f, 0.f};
        f4 sp = mfma16(kb[r], qb[r], z), scu = mfma16(kb[r + 1], qb[r], z);
        float p0 = odd ? sp.y : sp.x, p1 = odd ? sp.w : sp.z, c0 = odd ? scu.y : scu.x, c1 = odd ? scu.w : scu.z;
        if (r == 0 && g == 0) { p0 = -4e9f; p1 = -4e9f; }
        float v0 = cur0 ? c0 : p0, v1 = cur1 ? c1 : p1;
        const float mx = swap32_max(swap16_max(fmaxf(v0, v1)));
        v0 = __builtin_amdgcn_exp2f((v0 - mx) * sc); v1 = __builtin_amdgcn_exp2f((v1 - mx) * sc);
        const float rs_ = __builtin_amdgcn_rcpf(swap32_sum(swap16_sum(v0 + v1)));
        const unsigned pp = pk(v0 * rs_, v1 * rs_);
        const unsigned d0 = odd ? pp << 16 : pp & 0xffffu, d1 = odd ? pp & 0xffff0000u : pp >> 16;
        const v4u pf = {cur0 ? 0u : d0, cur1 ? 0u : d1, cur0 ? d0 : 0u, cur1 ? d1 : 0u};
        const f4 o = mfma32(v4u{vb[r].x, vb[r].y, vb[r + 1].x, vb[r + 1].y}, pf, z);
        h[r][0] = pk(o.x, o.y); h[r][1] = pk(o.z, o.w);
      }
#pragma unroll
      for (int pp = 0; pp < 2; ++pp) {
        auto s0 = __builtin_amdgcn_permlane16_swap(h[2 * pp][0], h[2 * pp + 1][0], false, false);
        auto s1 = __builtin_amdgcn_permlane16_swap(h[2 * pp][1], h[2 * pp + 1][1], false, false);
        stf(rs, v4u{s0[0], s1[0], s0[1], s1[1]}, (q & 1) * 1024 + ((q >> 1) * 16 + i) * 16,
            OFF_O + ((g * 16 + (hd >> 1)) * 4 + 2 * pp) * 1024 + (hd & 1) * 512);
      }
    }
    ST(19);
    const bool two = w < nt3 - 8;
    const Ctx c = {rs, rsv, L, Lb, tag, g, j, w, t03, t03 + (two ? 2 * w : nt3 - 8 + w), gfl, x_, b1_, b2_, out_};
    tail_c(c, two, tid STA);
    ST(16);
    SLF();
    __builtin_amdgcn_endpgm();
  } else {
    asm volatile("; helper waves" ::: "memory");
    LANE_IDS
    const int v = w - 4;
    const int row0 = 20 * v + q;
    GET_TAG
    ST(1);
    f2 xn[5][16];
    const int cb4[4] = {4 * v, 4 * v + 1, 4 * v + 2, 4 * v + 3};
    v4u mv[4];
#pragma unroll
    for (int p = 0; p < 5; ++p) {
      f2 s2 = {0.f, 0.f}, q2 = {0.f, 0.f};
      asm volatile("s_waitcnt vmcnt(%4)" : "+v"(xr[p][0]), "+v"(xr[p][1]), "+v"(xr[p][2]), "+v"(xr[p][3]) : "i"(p < 3 ? 8 : 16 - 4 * p) : "memory");
      if (p == 0) { XLOADS(3); SB(); }
      if (p == 1) { XLOADS(4); SB(); }
      if (p == 4) {
#pragma unroll
        for (int n = 0; n < 4; ++n) mv[n] = ldf(rs, l * 16, OFF_MODP + cb4[n] * 1024);
        SB();
      }
#pragma unroll
      for (int e = 0; e < 16; ++e) {
        xn[p][e] = unpk(xr[p][e >> 2][e & 3]);
        s2 += xn[p][e];
        q2 = fma2(xn[p][e], xn[p][e], q2);
      }
      const float m = sum16(s2.x + s2.y) * (1.0f / 512.0f);
      const float r = __builtin_amdgcn_rsqf(fmaxf(sum16(q2.x + q2.y) * (1.0f / 512.0f) - m * m, 0.f) + 1e-5f);
      const f2 r2 = {r, r}, mr2 = {-m * r, -m * r};
#pragma unroll
      for (int e = 0; e < 16; ++e) { xn[p][e] = fma2(xn[p][e], r2, mr2); asm volatile("" : "+v"(xn[p][e])); }
    }
    int ha[5];
#pragma unroll
    for (int p = 0; p < 5; ++p) {
      const int r = row0 + 4 * p, qq = i & 3;
      ha[p] = ((i >> 2) * 5 + (r >> 4)) * 1024 + (qq * 16 + ((r & 15) ^ (qq | (i & 4)))) * 16;
      asm volatile("" : "+v"(ha[p]));
    }
    ST(2);
    mod_finish<4, true>(rs, L, cb4, mv, tag, l);
    WAITV(0);
    __builtin_amdgcn_s_setprio(0);
    ST(3);
    BAR();
    ST(4);
#pragma unroll
    for (int n = 0; n < 4; ++n) {
      const int ch = L_MODF + (16 * ((n + rq) & 3) + i) * 32;
      const f4 sa0 = *(LAS f4*)(L + ch), sa1 = *(LAS f4*)(L + ch + 16), ca0 = *(LAS f4*)(L + ch + 2048), ca1 = *(LAS f4*)(L + ch + 2064);
      const f2 sa2[4] = {{sa0.x, sa0.y}, {sa0.z, sa0.w}, {sa1.x, sa1.y}, {sa1.z, sa1.w}}, ca2[4] = {{ca0.x, ca0.y}, {ca0.z, ca0.w}, {ca1.x, ca1.y}, {ca1.z, ca1.w}};
      v4u o[5];
#pragma unroll
      for (int p = 0; p < 5; ++p)
#pragma unroll
        for (int e = 0; e < 4; ++e) {
          if (n == 0) o[p][e] = pk2(fma2(xn[p][4 * n + e], ca2[e], sa2[e]));
          else o[p][e] = pk(fma1(xn[p][4 * n + e].x, ca2[e].x, sa2[e].x), fma1(xn[p][4 * n + e].y, ca2[e].y, sa2[e].y));
        }
#pragma unroll
      for (int p = 0; p < 5; ++p) *(LAS v4u*)(L + ha[p] + n * 20480) = o[p];
      if (n == 2) dma3(rsq, w1v, ((13 + 4 * rq) & 15) * (32 * 3072), Lb + L_R1 + 2 * 12288 + v * 3072);
      if (n < 3) BAR();
      if (n == 0) ST(5);
      if (n == 1) {
        dma3(rsq, w1v, ((12 + 4 * rq) & 15) * (32 * 3072), Lb + L_R1 + 12288 + v * 3072);
      }
    }
    WAITV_BAR(0);
    ST(6);
    dma3(rsq, w1v, ((14 + 4 * rq) & 15) * (32 * 3072), Lb + L_R1 + 3 * 12288 + v * 3072);
    dma3(rsq, w1v, ((15 + 4 * rq) & 15) * (32 * 3072), Lb + L_R1 + 4 * 12288 + v * 3072);
    v4u rf;
    if (w == 4) rf = ld_rflags(rs, l);
    WAITV(0);
    SBAR();
    {
      if (w == 4) {
        _Pragma("nounroll") for (unsigned s = 0; s < SPIN_CAP; ++s) {
          if (__all(rf.x == tag && rf.y == tag && rf.z == tag && rf.w == tag)) break;
          __builtin_amdgcn_s_sleep(8);
          asm volatile("" ::: "memory");
          rf = ld_rflags(rs, l);
        }
        *(volatile LAS unsigned*)(L + L_OK) = tag;
      } else lds_wait(L, L_OK, tag);
      __builtin_amdgcn_fence(__ATOMIC_ACQUIRE, "wavefront");
      asm volatile("" ::: "memory");
    }
    ST(31);
    const Ctx c = {rs, rsv, L, Lb, tag, g, j, w, t03, t03 + nt3 - 8 + w, gfl, x_, b1_, b2_, out_};
    const int nh = v & 1, kh = v >> 1;
    const unsigned w2off = OFF_W2T + ((j * 2 + nh) * 2 + kh) * 16384, w3off = OFF_W3T + c.tile0 * 16384;
    v4u w2[16], w3a[16], w3b[16], w4[44];
#pragma unroll
    for (int n = 0; n < 16; ++n) w2[n] = LDW(n, w2off);
    SB();
    ST(7);
    if (b == 0 && tid == 448) *(GAS unsigned*)(ws + OFF_E) = tag;
    const v4u xres = load_xres(c, tid);
    const float b1a = bflo(((const GAS u16*)b1_)[c.tile0 * 16 + i]);
    ST(19);
    SBAR();
    ST(8);
    f2 x1[4];
    {
      f4 acc[4][2];
      v4u a2[2][4];
      const int ab = kh * 4096 + l * 16;
      SBAR(); SL(20);
#pragma unroll
      for (int r = 0; r < 4; ++r) a2[0][r] = *(LAS v4u*)(L + ab + r * 1024);
#pragma unroll
      for (int n = 0; n < 8; ++n) {
        if (n == 1 || n == 3 || n == 5) SBAR();
        SB();
#pragma unroll
        for (int r = 0; r < 4; ++r) {
          MAC(n == 0, acc[r][0], w2[2 * n], a2[n & 1][r]);
          SB();
          if (n < 7) a2[(n + 1) & 1][r] = *(LAS v4u*)(L + ab + ((n + 1) * 8 + r) * 1024);
          SB();
          MAC(n == 0, acc[r][1], w2[2 * n + 1], a2[n & 1][r]);
          SB();
        }
      }
      MFMA_DONE();
      SL(21);
      const int lf = fresh(l), rb = L_RED + kh * 17408 + (lf & 15) * 272 + nh * 128 + (lf >> 4) * 16;
#pragma unroll
      for (int r = 0; r < 4; ++r)
#pragma unroll
        for (int t = 0; t < 2; ++t) *(LAS f4*)(L + rb + r * 16 * 272 + t * 64) = acc[r][t];
    }
    epi2(c, tid, xres, x1);
    ST(17);
    const unsigned w4off = OFF_W4T + ((j * 2 + nh) * 2 + kh) * 44 * 1024;
    asm volatile("" ::: "memory");
#pragma unroll
    for (int n = 0; n < 8; ++n) w3a[n] = LDW(n, w3off);
    WAITV_BAR(8);
    ST(9);
#pragma unroll
    for (int n = 8; n < 16; ++n) w3a[n] = LDW(n, w3off);
#pragma unroll
    for (int n = 0; n < 8; ++n) w4[n] = LDW(n, w4off);
    SB();
    float uvt[1];
    col_terms<1>(c, false, l, w3a, w3b, b1a, b1a, uvt);
    USE(uvt[0]);
    SBAR();
    ST(10);
    {
      f4 acc[4][1];
      const float S[5] = {0.f, 0.f, 0.f, 0.f, 0.f};
      mlp1_loop<1, 1>(c, l, w3a, w3b, acc, w4, w4off, false, S STA);
      BAR();
      ST(12);
      mlp1_epi<1>(c, false, l, uvt, acc);
    }
    ST(18);
    WAITV_BAR(0);
    if (tid == 256) __hip_atomic_store(gfl + 64 * 32 + j, tag, RLX_AGENT);
    ST(13);
#pragma unroll
    for (int n = 32; n < 44; ++n) w4[n] = LDW(n, w4off);
    const v4u b2v = load_b2(c, tid);
    {
      f4 acc[4][2];
      v4u a4[2][4];
      const int ab = kh * 4096 + l * 16;
      SBAR();
      ST(14);
#pragma unroll
      for (int r = 0; r < 4; ++r) a4[0][r] = *(LAS v4u*)(L + ab + r * 1024);
#pragma unroll
      for (int n = 0; n < 22; ++n) {
        if ((n & 1) && n < 19) SBAR();
        if (n == 1) SL(26);
        if (n == 9) SL(27);
        if (n == 17) SL(28);
        SB();
        const int sp = 2 * (n + 1) >= 38 ? 2 * (n + 1) - 38 : 2 * (n + 1);
        if (n < 21 || kh == 0) {
#pragma unroll
          for (int r = 0; r < 4; ++r) {
            MAC(n == 0, acc[r][0], w4[2 * n], a4[n & 1][r]);
            SB();
            if (n < 21) a4[(n + 1) & 1][r] = *(LAS v4u*)(L + ab + sp * 4096 + r * 1024);
            SB();
            MAC(n == 0, acc[r][1], w4[2 * n + 1], a4[n & 1][r]);
            SB();
          }
        }
      }
      MFMA_DONE();
      ST(15);
      const int lf = fresh(l), rb = L_RED + kh * 17408 + (lf & 15) * 272 + nh * 128 + (lf >> 4) * 16;
#pragma unroll
      for (int r = 0; r < 4; ++r)
#pragma unroll
        for (int t = 0; t < 2; ++t) *(LAS f4*)(L + rb + r * 16 * 272 + t * 64) = acc[r][t];
    }
    epi4(c, tid, b2v, x1);
    ST(16);
    SLF();
    __builtin_amdgcn_endpgm();
  }
}

extern "C" void kernel_launch(void* const* d_in, const int* in_sizes, int n_in, void* d_out, int out_size, void* d_ws, size_t ws_size,
                              hipStream_t stream) {
  (void)hipFuncSetAttribute((const void*)fused_block, hipFuncAttributeMaxDynamicSharedMemorySize, LDS_BYTES);
  fused_block<<<256, 512, LDS_BYTES, stream>>>((const u16*)d_in[0], (const u16*)d_in[1], (const u16*)d_in[2], (const u16*)d_in[4],
                                               (const u16*)d_in[5], (const u16*)d_in[6], (const u16*)d_in[7], (const u16*)d_in[8],
                                               (const u16*)d_in[9], (u16*)d_out, (unsigned char*)d_ws);
}
```

```cpp
#include <hip/hip_runtime.h>
#include <stdint.h>

#define GAS __attribute__((address_space(1)))
#define LAS __attribute__((address_space(3)))
#define DI __device__ __forceinline__
typedef unsigned short u16;
typedef unsigned v4u __attribute__((ext_vector_type(4)));
typedef unsigned v2u __attribute__((ext_vector_type(2)));
typedef float f4 __attribute__((ext_vector_type(4)));
typedef float f2 __attribute__((ext_vector_type(2)));
typedef __bf16 bf2 __attribute__((ext_vector_type(2)));
typedef __bf16 bf8 __attribute__((ext_vector_type(8)));
typedef short s4 __attribute__((ext_vector_type(4)));
typedef GAS unsigned gu32;
typedef GAS unsigned long long gu64;
#define RLX_AGENT __ATOMIC_RELAXED, __HIP_MEMORY_SCOPE_AGENT

constexpr unsigned OFF_E = 0;
constexpr unsigned OFF_RFLAG = 768u << 10;
constexpr unsigned OFF_GFLAG = 4096;
constexpr unsigned OFF_MODP = 16384;
constexpr unsigned OFF_STAT = 65536;
constexpr unsigned OFF_W2T = 1u << 20;
constexpr unsigned OFF_W3T = OFF_W2T + 512 * 1024;
constexpr unsigned OFF_W4T = OFF_W3T + 1376 * 1024;
constexpr unsigned OFF_O = OFF_W4T + 1408 * 1024;
constexpr unsigned OFF_XA = OFF_O + 2048 * 1024;
constexpr unsigned OFF_H1 = OFF_XA + 2048 * 1024;
constexpr unsigned WS_USED = OFF_H1 + 32 * 43 * 4096;
constexpr unsigned OFF_STAMP = 64u << 20;

constexpr int L_RT = 49152;
constexpr int L_R1 = 81920;
constexpr int L_RED = 65536;
constexpr int L_STAT = 102400;
constexpr int L_UV = 118784;
constexpr int L_MODF = 155648;
constexpr int L_MODB = L_MODF + 4096 - 2048;
constexpr int L_MRED = 77824;
constexpr int L_SYNC = 79872;
constexpr int L_OK = L_MODF + 3584;
constexpr int L_COEF = L_MODF;
constexpr int L_RR = L_COEF + 1024;
constexpr int LDS_BYTES = 163840;

constexpr int CODE_STEP = 1216;
constexpr unsigned SPIN_CAP = 1u << 18;

#define ST(n)
#define SL(n)
#define SRT(n)
#define SLF()

DI float bflo(unsigned u) { return __uint_as_float(u << 16); }
DI float bfhi(unsigned u) { return __uint_as_float(u & 0xffff0000u); }
DI unsigned pk(float a, float b) { f2 v = {a, b}; return __builtin_bit_cast(unsigned, __builtin_convertvector(v, bf2)); }
DI f2 unpk(unsigned u) { return f2{bflo(u), bfhi(u)}; }
DI f2 fma2(f2 a, f2 b, f2 c) { return __builtin_elementwise_fma(a, b, c); }
DI unsigned pk2(f2 v) { return __builtin_bit_cast(unsigned, __builtin_convertvector(v, bf2)); }
DI f4 mfma32(v4u a, v4u b, f4 c) { return __builtin_amdgcn_mfma_f32_16x16x32_bf16(__builtin_bit_cast(bf8, a), __builtin_bit_cast(bf8, b), c, 0, 0, 0); }
template <bool FIRST> DI void mac(f4& c, v4u a, v4u b) {
  if constexpr (FIRST) asm volatile("v_mfma_f32_16x16x32_bf16 %0, %1, %2, 0" : "=&v"(c) : "v"(a), "v"(b));
  else asm volatile("v_mfma_f32_16x16x32_bf16 %0, %1, %2, %0" : "+v"(c) : "v"(a), "v"(b));
}
#define MAC(first, c, a, b) do { if (first) mac<true>(c, a, b); else mac<false>(c, a, b); } while (0)
#define MFMA_DONE() do { asm volatile("s_nop 15" ::: "memory"); __builtin_amdgcn_sched_barrier(0); } while (0)
DI f4 mfma16(v2u a, v2u b, f4 c) { return __builtin_amdgcn_mfma_f32_16x16x16bf16_1k(__builtin_bit_cast(s4, a), __builtin_bit_cast(s4, b), c, 0, 0, 0); }
template <int CTRL> DI float dpp(float x) { return __builtin_bit_cast(float, __builtin_amdgcn_update_dpp(0, __builtin_bit_cast(int, x), CTRL, 0xf, 0xf, true)); }
DI float swap16_sum(float x) { auto r = __builtin_amdgcn_permlane16_swap(__float_as_uint(x), __float_as_uint(x), false, false); return __uint_as_float(r[0]) + __uint_as_float(r[1]); }
DI float swap32_sum(float x) { auto r = __builtin_amdgcn_permlane32_swap(__float_as_uint(x), __float_as_uint(x), false, false); return __uint_as_float(r[0]) + __uint_as_float(r[1]); }
DI float swap16_max(float x) { auto r = __builtin_amdgcn_permlane16_swap(__float_as_uint(x), __float_as_uint(x), false, false); return fmaxf(__uint_as_float(r[0]), __uint_as_float(r[1])); }
DI float swap32_max(float x) { auto r = __builtin_amdgcn_permlane32_swap(__float_as_uint(x), __float_as_uint(x), false, false); return fmaxf(__uint_as_float(r[0]), __uint_as_float(r[1])); }
DI float sum8(float x) { x += dpp<0xB1>(x); x += dpp<0x4E>(x); x += dpp<0x141>(x); return x; }
DI float sum16(float x) { x = sum8(x); x += dpp<0x140>(x); return x; }

#define BAR() asm volatile("s_waitcnt lgkmcnt(0)\n\ts_barrier" ::: "memory")
#define WAITV(n) do { asm volatile("s_waitcnt vmcnt(%0)" ::"i"(n) : "memory"); if ((n) == 0) __builtin_amdgcn_s_waitcnt(0x0F70); } while (0)
#define WAITV_BAR(n) do { asm volatile("s_waitcnt vmcnt(%0) lgkmcnt(0)\n\ts_barrier" ::"i"(n) : "memory"); if ((n) == 0) __builtin_amdgcn_s_waitcnt(0x0F70); } while (0)
#define SBAR() asm volatile("s_barrier" ::: "memory")
#define USE(x) asm volatile("" ::"v"(x))

template <int N> DI void dma(v4u rs, unsigned voff, unsigned soff, unsigned lds) {
  unsigned keep;
  if constexpr (N == 1)
    asm volatile("s_mov_b32 %0, m0\n\ts_mov_b32 m0, %2\n\ts_nop 0\n\t"
                 "buffer_load_dwordx4 %1, %3, %4 offen sc1 lds\n\t"
                 "s_mov_b32 m0, %0" : "=&s"(keep) : "v"(voff), "s"(lds), "s"(rs), "s"(soff) : "memory");
  else if constexpr (N == 2)
    asm volatile("s_mov_b32 %0, m0\n\ts_mov_b32 m0, %2\n\ts_nop 0\n\t"
                 "buffer_load_dwordx4 %1, %3, %4 offen sc1 lds\n\t"
                 "buffer_load_dwordx4 %1, %3, %4 offen offset:1024 sc1 lds\n\t"
                 "s_mov_b32 m0, %0" : "=&s"(keep) : "v"(voff), "s"(lds), "s"(rs), "s"(soff) : "memory");
  else
    asm volatile("s_mov_b32 %0, m0\n\ts_mov_b32 m0, %2\n\ts_nop 0\n\t"
                 "buffer_load_dwordx4 %1, %3, %4 offen sc1 lds\n\t"
                 "buffer_load_dwordx4 %1, %3, %4 offen offset:1024 sc1 lds\n\t"
                 "buffer_load_dwordx4 %1, %3, %4 offen offset:2048 sc1 lds\n\t"
                 "buffer_load_dwordx4 %1, %3, %4 offen offset:3072 sc1 lds\n\t"
                 "s_mov_b32 m0, %0" : "=&s"(keep) : "v"(voff), "s"(lds), "s"(rs), "s"(soff) : "memory");
}
DI void dma3(v4u rs, unsigned voff, unsigned soff, unsigned lds) {
  unsigned keep;
  asm volatile("s_mov_b32 %0, m0\n\ts_mov_b32 m0, %2\n\ts_nop 0\n\t"
               "buffer_load_dwordx4 %1, %3, %4 offen lds\n\t"
               "buffer_load_dwordx4 %1, %3, %4 offen offset:1024 lds\n\t"
               "buffer_load_dwordx4 %1, %3, %4 offen offset:2048 lds\n\t"
               "s_mov_b32 m0, %0" : "=&s"(keep) : "v"(voff), "s"(lds), "s"(rs), "s"(soff) : "memory");
}
DI v4u trfrag(LAS char* p) {
  v2u lo = __builtin_bit_cast(v2u, __builtin_amdgcn_ds_read_tr16_b64_v4i16((LAS s4*)p));
  v2u hi = __builtin_bit_cast(v2u, __builtin_amdgcn_ds_read_tr16_b64_v4i16((LAS s4*)(p + 512)));
  return v4u{lo.x, lo.y, hi.x, hi.y};
}
DI int lane_id() { int r; asm volatile("v_mbcnt_lo_u32_b32 %0, -1, 0\n\tv_mbcnt_hi_u32_b32 %0, -1, %0" : "=v"(r)); return r; }
DI int fresh(int x) { asm volatile("" : "+v"(x)); return x; }
#define SB() __builtin_amdgcn_sched_barrier(0)
DI void gdma(const GAS void* p, unsigned lds) {
  unsigned keep;
  asm volatile("s_mov_b32 %0, m0\n\ts_mov_b32 m0, %2\n\ts_nop 0\n\tglobal_load_lds_dwordx4 %1, off nt\n\ts_mov_b32 m0, %0"
               : "=&s"(keep) : "v"(p), "s"(lds) : "memory");
}
typedef __amdgpu_buffer_rsrc_t rsrc_t;
DI v4u ldf(rsrc_t rs, unsigned voff, unsigned soff) { return __builtin_amdgcn_raw_buffer_load_b128(rs, voff, soff, 16); }
#define LDW(n, base) ldw_rt(c.rs, l * 16, base, n)
DI v4u ldw_rt(rsrc_t rs, unsigned voff, unsigned base, int n) { return __builtin_amdgcn_raw_buffer_load_b128(rs, voff + (n & 3) * 1024, base + (n >> 2) * 4096, 16); }
DI v4u ld_rflags(rsrc_t rs, int l) {
  v4u r;
#pragma unroll
  for (int k = 0; k < 4; ++k) r[k] = __builtin_amdgcn_raw_buffer_load_b32(rs, l * 128, OFF_RFLAG + k * 8192, 16);
  return r;
}
DI void stf(rsrc_t rs, v4u v, unsigned voff, unsigned soff) { __builtin_amdgcn_raw_buffer_store_b128(v, rs, voff + soff, 0, 16); }

DI void meet4(LAS char* L, int at, int me, unsigned tag, int l) {
  volatile LAS unsigned* f = (volatile LAS unsigned*)(L + at);
  f[me] = tag;
  _Pragma("nounroll") for (unsigned s = 0; s < 16 * SPIN_CAP; ++s)
    if (__all(f[l & 3] == tag)) break;
  asm volatile("" ::: "memory");
}
DI void lds_wait(LAS char* L, int at, unsigned tag) {
  volatile LAS unsigned* f = (volatile LAS unsigned*)(L + at);
  _Pragma("nounroll") for (unsigned s = 0; s < 16 * SPIN_CAP; ++s)
    if (*f == tag) break;
  asm volatile("" ::: "memory");
}
DI void poll8(gu32* fl, unsigned tag, int l) {
  _Pragma("nounroll") for (unsigned s = 0; s < SPIN_CAP; ++s) {
    unsigned f = __hip_atomic_load(fl + (l & 7), RLX_AGENT);
    if (__all(f == tag)) break;
    __builtin_amdgcn_s_sleep(1);
  }
  __builtin_amdgcn_fence(__ATOMIC_ACQUIRE, "wavefront");
}

DI unsigned gelu2(f2 a, f2 k1, f2 k2, f2 ir) {
  const f2 t = a * fma2(a * a, k2, k1);
  const f2 d = fma2(f2{__builtin_amdgcn_exp2f(t.x), __builtin_amdgcn_exp2f(t.y)}, ir, ir);
  return pk2(a * f2{__builtin_amdgcn_rcpf(d.x), __builtin_amdgcn_rcpf(d.y)});
}

template <int N, bool F32> DI void mod_finish(rsrc_t rs, LAS char* L, const int (&cb)[N], v4u (&v)[N], unsigned tb, int l) {
  _Pragma("nounroll") for (unsigned s = 0; s < SPIN_CAP; ++s) {
    unsigned d = 0;
#pragma unroll
    for (int n = 0; n < N; ++n) d |= (v[n].x ^ tb) | (v[n].y ^ tb) | (v[n].z ^ tb) | (v[n].w ^ tb);
    if (__all(d << 24 == 0u)) break;
    __builtin_amdgcn_s_sleep(1);
    asm volatile("" ::: "memory");
#pragma unroll
    for (int n = 0; n < N; ++n) v[n] = ldf(rs, l * 16, OFF_MODP + cb[n] * 1024);
  }
#pragma unroll
  for (int n = 0; n < N; ++n) {
    float a[2];
#pragma unroll
    for (int k = 0; k < 2; ++k) {
      auto r = __builtin_amdgcn_permlane32_swap(v[n][k] & 0xffffff00u, v[n][k + 2] & 0xffffff00u, false, false);
      a[k] = swap16_sum(__uint_as_float(r[0]) + __uint_as_float(r[1]));
    }
    f2 m = unpk(pk(a[0], a[1]));
    const float one = ((cb[n] >> 3) == 1 || (cb[n] >> 3) == 4) ? 1.0f : 0.0f;
    const unsigned p = pk(m.x + one, m.y + one);
    if (!(l & 16)) {
      if (F32) *(LAS f2*)(L + L_MODF + cb[n] * 256 + (l & 15) * 16 + (l >> 5) * 8) = unpk(p);
      else {
        const int o = (l & 15) * 8 + (l >> 5) * 4;
        *(LAS unsigned*)(L + L_MODB + cb[n] * 128 + o) = p;
        if (n < 4) {
          const int u = L_UV + (cb[n] < 32 ? 2048 : 0) + (cb[n] & 7) * 128 + o;
          *(LAS unsigned*)(L + u) = p; *(LAS unsigned*)(L + u + 1024) = p;
        }
      }
    }
  }
}
template <int N, bool F32> DI void mod_fetch(rsrc_t rs, LAS char* L, const int (&cb)[N], unsigned tb, int l) {
  v4u v[N];
#pragma unroll
  for (int n = 0; n < N; ++n) v[n] = ldf(rs, l * 16, OFF_MODP + cb[n] * 1024);
  mod_finish<N, F32>(rs, L, cb, v, tb, l);
}

#define STP
#define STA
DI float fma1(float a, float b, float c) { float r; asm("v_fma_f32 %0, %1, %2, %3" : "=v"(r) : "v"(a), "v"(b), "v"(c)); return r; }
#define W1POS(s) ((s) < 6 ? L_R1 + (s) * 12288 : (s) < 10 ? ((s) - 6) * 12288 : 65536)

DI void mod_loads(const u16* c_, const u16* wmod_, int mcb, int mks, int v, int l, v2u& cw, v4u (&wm)[4]) {
  const int r0 = 128 * mks + 32 * v + 4 * (l >> 3);
  asm volatile("global_load_dwordx2 %0, %1, %2" : "=v"(cw) : "v"(r0 * 2), "s"(c_));
#pragma unroll
  for (int e = 0; e < 4; ++e) asm volatile("global_load_dwordx4 %0, %1, %2 nt" : "=v"(wm[e]) : "v"((r0 + e) * 6144 + 128 * mcb + 16 * (l & 7)), "s"(wmod_));
}
#define MOD_WAIT(n) asm volatile("s_waitcnt vmcnt(%5)" : "+v"(cw), "+v"(wm[0]), "+v"(wm[1]), "+v"(wm[2]), "+v"(wm[3]) : "i"(n) : "memory")
DI v4u mod_job(LAS char* L, int red, int v, int l, unsigned tag, v2u cw, const v4u (&wm)[4]) {
  v4u res = {0u, 0u, 0u, 0u};
  f2 a2[4];
#pragma unroll
  for (int e = 0; e < 4; ++e) {
    const float cv = (e & 1) ? bfhi(cw[e >> 1]) : bflo(cw[e >> 1]);
    float sv = cv * __builtin_amdgcn_rcpf(1.0f + __builtin_amdgcn_exp2f(-1.4426950409f * cv));
    sv = bflo(pk(sv, sv));
    const f2 s2 = {sv, sv};
#pragma unroll
    for (int k = 0; k < 4; ++k) a2[k] = e ? fma2(s2, unpk(wm[e][k]), a2[k]) : s2 * unpk(wm[e][k]);
  }
  float a[4];
#pragma unroll
  for (int k = 0; k < 4; ++k) {
    auto r = __builtin_amdgcn_permlane32_swap(__float_as_uint(a2[k >> 1][k & 1]), __float_as_uint(a2[2 + (k >> 1)][k & 1]), false, false);
    a[k] = __uint_as_float(r[0]) + __uint_as_float(r[1]);
  }
#pragma unroll
  for (int k = 0; k < 2; ++k) {
    auto r = __builtin_amdgcn_permlane16_swap(__float_as_uint(a[k]), __float_as_uint(a[k + 2]), false, false);
    a[k] = __uint_as_float(r[0]) + __uint_as_float(r[1]);
    a[k] += dpp<0x128>(a[k]);
  }
  if (!(l & 8)) *(LAS f2*)(L + red + v * 256 + (8 * (l & 7) + 2 * ((l >> 4) & 1) + 4 * (l >> 5)) * 4) = f2{a[0], a[1]};
  asm volatile("s_waitcnt lgkmcnt(0)" ::: "memory");
  volatile LAS unsigned* mf = (volatile LAS unsigned*)(L + red + 1024);
  if (v < 3) mf[v] = tag;
  else {
    _Pragma("nounroll") for (unsigned s = 0; s < SPIN_CAP; ++s)
      if (__all(mf[l < 3 ? l : 0] == tag)) break;
    asm volatile("" ::: "memory");
    f4 s = *(LAS f4*)(L + red + (l & 15) * 16);
#pragma unroll
    for (int ww = 1; ww < 4; ++ww) s += *(LAS f4*)(L + red + ww * 256 + (l & 15) * 16);
    res = (__builtin_bit_cast(v4u, s) & 0xffffff00u) | (tag & 0xffu);
  }
  return res;
}
DI void mod_publish(rsrc_t rs, int mcb, int mks, int v, int l, v4u res) {
  if (v == 3 && l < 16) stf(rs, res, l * 16, OFF_MODP + mcb * 1024 + mks * 256);
}

struct Ctx {
  rsrc_t rs; v4u rsv; LAS char* L; unsigned Lb, tag; int g, j, w, t03, tile0; gu32* gfl;
  const u16 *x_, *b1_, *b2_; u16* out_;
};

template <int T> DI void col_terms(const Ctx& c, bool two, int l, const v4u (&w3a)[16], const v4u (&w3b)[16], float b1a, float b1b, float (&uvt)[T]) {
  l = fresh(l);
  const int i = l & 15, q = l >> 4, ty = i >> 2;
  const int base = L_UV + (ty < 2 ? ty * 2048 + c.j * 128 : 2048 + ty * 1024) + q * 16;
  f4 a[2][T];
  v4u am[16];
#pragma unroll
  for (int s = 0; s < 16; ++s) am[s] = *(LAS v4u*)(c.L + base + s * 64);
  SB();
#pragma unroll
  for (int s = 0; s < 16; ++s) MAC(s < 2, a[s & 1][0], am[s], w3a[s]);
  if (T == 2 && two) {
#pragma unroll
    for (int s = 0; s < 16; ++s) MAC(s < 2, a[s & 1][T - 1], am[s], w3b[s]);
  }
  MFMA_DONE();
  uvt[0] = a[0][0].x + a[1][0].x + (q == 3 ? b1a : 0.f);
  if (T == 2) uvt[T - 1] = a[0][T - 1].x + a[1][T - 1].x + (q == 3 ? b1b : 0.f);
}

DI void epi2(const Ctx& c, int tid, v4u xres, f2 (&x1)[4]) {
  LAS char* L = c.L;
  tid = fresh(tid);
  const int etok = tid >> 3, ef = 64 * c.j + 8 * (tid & 7);
  const v4u gav = *(LAS v4u*)(L + L_MODB + 2048 + ef * 2), sbv = *(LAS v4u*)(L + L_MODB + 3072 + ef * 2), a2v = *(LAS v4u*)(L + L_MODB + 4096 + ef * 2);
  BAR();
  const int ra = L_RED + etok * 272 + (tid & 7) * 32;
  const f4 y0 = *(LAS f4*)(L + ra) + *(LAS f4*)(L + ra + 17408), y1 = *(LAS f4*)(L + ra + 16) + *(LAS f4*)(L + ra + 17408 + 16);
  f2 p[4], a2[4];
#pragma unroll
  for (int e = 0; e < 4; ++e) {
    const f2 y = e == 0 ? f2{y0.x, y0.y} : e == 1 ? f2{y0.z, y0.w} : e == 2 ? f2{y1.x, y1.y} : f2{y1.z, y1.w};
    a2[e] = unpk(a2v[e]);
    x1[e] = fma2(unpk(gav[e]), y, unpk(xres[e]));
    p[e] = x1[e] * a2[e];
  }
  stf(c.rs, v4u{pk2(p[0]), pk2(p[1]), pk2(p[2]), pk2(p[3])},
      (((tid & 7) >> 2) * 4 + (etok >> 4)) * 1024 + ((tid & 3) * 16 + (etok & 15)) * 16, OFF_XA + (c.g * 16 + 2 * c.j) * 4096);
  SB();
  f2 vx = x1[0], vxx = x1[0] * x1[0], vp = p[0], vpp = p[0] * p[0], vpa = p[0] * a2[0], vps = p[0] * unpk(sbv[0]);
#pragma unroll
  for (int e = 1; e < 4; ++e) {
    vx += x1[e]; vxx = fma2(x1[e], x1[e], vxx); vp += p[e]; vpp = fma2(p[e], p[e], vpp); vpa = fma2(p[e], a2[e], vpa); vps = fma2(p[e], unpk(sbv[e]), vps);
  }
  const float sx = sum8(vx.x + vx.y), sxx = sum8(vxx.x + vxx.y), sp = sum8(vp.x + vp.y), spp = sum8(vpp.x + vpp.y), spa = sum8(vpa.x + vpa.y), sps = sum8(vps.x + vps.y);
  const bool first = (tid & 7) == 0;
  if ((tid & 7) < 2) stf(c.rs, __builtin_bit_cast(v4u, first ? f4{sx, sxx, sp, spp} : f4{spa, sps, spa, sps}), etok * 16 + (first ? 0 : 1024), OFF_STAT + (c.g * 8 + c.j) * 2048);
}

template <int T, int KIND>
DI void mlp1_loop(const Ctx& c, int l, const v4u (&w3a)[16], const v4u (&w3b)[16], f4 (&acc)[4][T], v4u (&w4)[44], unsigned w4off, bool two,
                  const float (&S)[5] STP) {
  LAS char* L = c.L;
  const int tid = c.w * 64 + l;
  auto xa_slot = [&](int slot) { dma<4>(c.rsv, l * 16, OFF_XA + (c.g * 16 + ((slot + 2 * c.j) & 15)) * 4096, c.Lb + slot * 4096); };
  v4u a[2][4];
#pragma unroll
  for (int r = 0; r < 4; ++r) a[0][r] = *(LAS v4u*)(L + r * 1024 + l * 16);
#pragma unroll
  for (int s = 0; s < 16; ++s) {
    if (s == 1 || s == 5 || s == 9 || s == 13) {
      if (KIND == 2 && c.w == 3) {
        if (s == 1) WAITV(16);
        if (s == 5) WAITV(40);
        if (s == 9) WAITV(24);
        if (s == 13) WAITV(0);
      }
      SBAR();
      SL(22 + s / 4);
      if (KIND == 2 && c.w == 3) {
        if (s == 1) {
#pragma unroll
          for (int k = 10; k < 16; ++k) xa_slot(k);
#pragma unroll
          for (int p = 0; p < 4; ++p) dma<4>(c.rsv, l * 16, OFF_STAT + c.g * 16384 + p * 4096, c.Lb + L_STAT + p * 4096);
        }
        if (s == 13) {
          float sx = 0, sxx = 0, sp = 0, spp = 0, spa = 0, sps = 0;
#pragma unroll
          for (int p = 0; p < 8; ++p) {
            const f4 a4 = *(LAS f4*)(L + L_STAT + p * 2048 + l * 16);
            const f2 b = *(LAS f2*)(L + L_STAT + p * 2048 + 1024 + l * 16);
            sx += a4.x; sxx += a4.y; sp += a4.z; spp += a4.w; spa += b.x; sps += b.y;
          }
          const float inv = 1.0f / 512.0f;
          const float m = sx * inv, var = fmaxf(sxx * inv - m * m, 0.f);
          const float sd = __builtin_amdgcn_sqrtf(var + 1e-5f), r = __builtin_amdgcn_rcpf(sd);
          const float m2 = (r * (sp - m * S[0]) + S[1]) * inv;
          const float e2 = (r * r * (spp - 2.f * m * spa + m * m * S[2]) + 2.f * r * (sps - m * S[3]) + S[4]) * inv;
          const float sd2 = __builtin_amdgcn_sqrtf(fmaxf(e2 - m2 * m2, 0.f) + 1e-5f);
          const float R = r * __builtin_amdgcn_rcpf(sd2), k1 = -2.302208198f * R;
          *(LAS f4*)(L + L_COEF + l * 16) = f4{-m, sd, -m2 * sd, sd * sd2};
          *(LAS f4*)(L + L_RR + l * 16) = f4{k1, 0.044715f * k1 * R * R, sd * sd2, 0.f};
        }
      }
    }
    SB();
#pragma unroll
    for (int r = 0; r < 4; ++r) {
      MAC(s == 0, acc[r][0], w3a[s], a[s & 1][r]);
      SB();
      if (s < 15) a[(s + 1) & 1][r] = *(LAS v4u*)(L + ((s + 1) * 4 + r) * 1024 + l * 16);
      SB();
    }
    if (T == 2 && two) {
#pragma unroll
      for (int r = 0; r < 4; ++r) MAC(s == 0, acc[r][T - 1], w3b[s], a[s & 1][r]);
      SB();
    }
    if (KIND == 1 && s >= 2 && s < 14) {
      w4[2 * s + 4] = LDW(2 * s + 4, w4off);
      w4[2 * s + 5] = LDW(2 * s + 5, w4off);
      SB();
    }
  }
  MFMA_DONE();
}

template <int T> DI void mlp1_epi(const Ctx& c, bool two, int l, const float (&uvt)[T], f4 (&acc)[4][T]) {
  l = fresh(l);
  const int i = l & 15, q = l >> 4;
  float cf[4]; f4 rr[4];
#pragma unroll
  for (int r = 0; r < 4; ++r) {
    cf[r] = *(LAS float*)(c.L + L_COEF + (r * 16 + i) * 16 + q * 4);
    rr[r] = *(LAS f4*)(c.L + L_RR + (r * 16 + i) * 16);
  }
#pragma unroll
  for (int t = 0; t < T; ++t) {
    if (t && !two) break;
#pragma unroll
    for (int r = 0; r < 4; ++r) acc[r][t] = __builtin_amdgcn_mfma_f32_16x16x4f32(uvt[t], cf[r], acc[r][t], 0, 0, 0);
  }
  SB();
#pragma unroll
  for (int t = 0; t < T; ++t) {
    if (t && !two) break;
    const int tl = c.tile0 + t;
#pragma unroll
    for (int pp = 0; pp < 2; ++pp) {
      unsigned h[2][2];
#pragma unroll
      for (int r2 = 0; r2 < 2; ++r2) {
        const int r = 2 * pp + r2;
        const f2 k1 = {rr[r].x, rr[r].x}, k2 = {rr[r].y, rr[r].y}, ir = {rr[r].z, rr[r].z};
        h[r2][0] = gelu2(f2{acc[r][t].x, acc[r][t].y}, k1, k2, ir);
        h[r2][1] = gelu2(f2{acc[r][t].z, acc[r][t].w}, k1, k2, ir);
      }
      auto s0 = __builtin_amdgcn_permlane16_swap(h[0][0], h[1][0], false, false);
      auto s1 = __builtin_amdgcn_permlane16_swap(h[0][1], h[1][1], false, false);
      stf(c.rs, v4u{s0[0], s1[0], s0[1], s1[1]}, (q & 1) * 1024 + ((q >> 1) * 16 + i) * 16,
          OFF_H1 + ((c.g * 43 + (tl >> 1)) * 4 + 2 * pp) * 1024 + (tl & 1) * 512);
      SB();
    }
  }
}

DI void epi4(const Ctx& c, int tid, v4u b2v, const f2 (&x1)[4]) {
  LAS char* L = c.L;
  tid = fresh(tid);
  const int etok = tid >> 3, ef = 64 * c.j + 8 * (tid & 7);
  const v4u gbv = *(LAS v4u*)(L + L_MODB + 5120 + ef * 2);
  BAR();
  const int ra = L_RED + etok * 272 + (tid & 7) * 32;
  const f4 y0 = *(LAS f4*)(L + ra) + *(LAS f4*)(L + ra + 17408), y1 = *(LAS f4*)(L + ra + 16) + *(LAS f4*)(L + ra + 17408 + 16);
  v4u o;
#pragma unroll
  for (int e = 0; e < 4; ++e) {
    const f2 y = e == 0 ? f2{y0.x, y0.y} : e == 1 ? f2{y0.z, y0.w} : e == 2 ? f2{y1.x, y1.y} : f2{y1.z, y1.w};
    o[e] = pk2(fma2(unpk(gbv[e]), y + unpk(b2v[e]), x1[e]));
  }
  const rsrc_t ro = __builtin_amdgcn_make_buffer_rsrc((void*)c.out_, 0, 2048 * 512 * 2, 0x00020000);
  __builtin_amdgcn_raw_buffer_store_b128(o, ro, etok * 1024 + (tid & 7) * 16 + c.g * 65536 + c.j * 128, 0, 0);
}
DI v4u load_xres(const Ctx& c, int tid) {
  const rsrc_t rx = __builtin_amdgcn_make_buffer_rsrc((void*)c.x_, 0, 2048 * 512 * 2, 0x00020000);
  return __builtin_amdgcn_raw_buffer_load_b128(rx, (tid >> 3) * 1024 + (tid & 7) * 16, c.g * 65536 + c.j * 128, 0);
}
DI v4u load_b2(const Ctx& c, int tid) {
  const rsrc_t rb2 = __builtin_amdgcn_make_buffer_rsrc((void*)c.b2_, 0, 1024, 0x00020000);
  return __builtin_amdgcn_raw_buffer_load_b128(rb2, (tid & 7) * 16, c.j * 128, 0);
}

DI void tail_c(const Ctx& c, bool two, int tid STP) {
  tid = fresh(tid);
  const int l = tid & 63, i = l & 15, w = c.w, g = c.g, j = c.j;
  LAS char* L = c.L;
  const unsigned w3off = OFF_W3T + c.tile0 * 16384;
  v4u w3a[16], w3b[16], w4[44];
  WAITV(0);
  meet4(L, L_OK + 16, w, c.tag, l);
  if (tid == 0) __hip_atomic_store(c.gfl + j, c.tag, RLX_AGENT);
  ST(11);
  auto o_tick = [&](int m) {
    dma<4>(c.rsv, l * 16, OFF_O + (g * 16 + ((4 * m + w + 2 * j) & 15)) * 4096, c.Lb + (4 * m + w) * 4096);
  };
  if (w < 2) o_tick(0);
  lds_wait(L, L_OK, c.tag);
  const int c0 = 24 + 4 * w, cb5[5] = {c0, c0 + 1, c0 + 2, c0 + 3, ((w & 1) ? 40 : 16) + j};
  v4u mv[5];
#pragma unroll
  for (int k = 0; k < 5; ++k) mv[k] = ldf(c.rs, l * 16, OFF_MODP + cb5[k] * 1024);
  const float b1a = bflo(((const GAS u16*)c.b1_)[c.tile0 * 16 + i]), b1b = bflo(((const GAS u16*)c.b1_)[(c.tile0 + (two ? 1 : 0)) * 16 + i]);
  const v4u xres = load_xres(c, tid);
#pragma unroll
  for (int n = 0; n < 16; ++n) w3a[n] = LDW(n, w3off);
  SB();
  mod_finish<5, false>(c.rs, L, cb5, mv, c.tag, l);
  if (w < 2) { const unsigned f = w ? 0u : 0x3f803f80u; *(LAS v4u*)(L + L_UV + 4096 + w * 1024 + l * 16) = v4u{f, f, f, f}; }
  if (w == 3) poll8(c.gfl, c.tag, l);
  BAR();
  ST(8);
  if (w >= 2) o_tick(0);
  o_tick(1);
  WAITV_BAR(4); SL(20);
  o_tick(2);
  WAITV_BAR(4);
  o_tick(3);
  WAITV_BAR(4);
  WAITV_BAR(0); SL(21);
  USE(xres); USE(b1a); USE(b1b);
  float S[5] = {0.f, 0.f, 0.f, 0.f, 0.f};
  if (two) {
#pragma unroll
    for (int n = 0; n < 16; ++n) w3b[n] = LDW(16 + n, w3off);
  }
  f2 x1[4];
  epi2(c, tid, xres, x1);
  ST(17);
  if (w == 3) {
    const v4u av = *(LAS v4u*)(L + L_MODB + 4096 + l * 16), sv = *(LAS v4u*)(L + L_MODB + 3072 + l * 16);
#pragma unroll
    for (int e = 0; e < 4; ++e) {
      const float a0 = bflo(av[e]), a1 = bfhi(av[e]), s0 = bflo(sv[e]), s1 = bfhi(sv[e]);
      S[0] += a0 + a1; S[1] += s0 + s1; S[2] += a0 * a0 + a1 * a1; S[3] += a0 * s0 + a1 * s1; S[4] += s0 * s0 + s1 * s1;
    }
#pragma unroll
    for (int e = 0; e < 5; ++e) S[e] = swap32_sum(swap16_sum(sum16(S[e])));
  }
  WAITV_BAR(0);
  if (tid == 128) __hip_atomic_store(c.gfl + 32 * 32 + j, c.tag, RLX_AGENT);
  ST(9);
#pragma unroll
  for (int n = 0; n < 16; ++n) { USE(w3a[n]); USE(w3b[n]); }
  if (w < 2) dma<4>(c.rsv, l * 16, OFF_XA + (g * 16 + 2 * j + w) * 4096, c.Lb + w * 4096);
  float uvt[2];
  col_terms<2>(c, two, l, w3a, w3b, b1a, b1b, uvt);
  USE(uvt[0]); USE(uvt[1]);
  if (w < 2) WAITV(0);
  if (w == 3) {
    poll8(c.gfl + 32 * 32, c.tag, l);
    ST(15);
#pragma unroll
    for (int k = 2; k < 10; ++k) dma<4>(c.rsv, l * 16, OFF_XA + (g * 16 + ((k + 2 * j) & 15)) * 4096, c.Lb + k * 4096);
  }
  SBAR();
  ST(10);
  f4 acc[4][2];
  mlp1_loop<2, 2>(c, l, w3a, w3b, acc, w4, 0, two, S STA);
  BAR();
  ST(12);
  mlp1_epi<2>(c, two, l, uvt, acc);
  ST(18);
  WAITV_BAR(0);
  ST(13);
  const int rot4 = (c.t03 + 1) >> 1;
  auto h_tick = [&](int m) {
    const int sg = 4 * m + w;
    int k = rot4 + sg; if (k >= 43) k -= 43;
    dma<4>(c.rsv, l * 16, OFF_H1 + (g * 43 + k) * 4096, c.Lb + (sg >= 38 ? sg - 38 : sg) * 4096);
  };
  h_tick(0);
  const v4u b2v = load_b2(c, tid);
  if (w == 3) poll8(c.gfl + 64 * 32, c.tag, l);
  WAITV_BAR(0);
  USE(b2v);
  ST(14);
  h_tick(1); h_tick(2); h_tick(3);
#pragma unroll
  for (int m = 1; m < 10; ++m) {
    if (m == 1 || m == 7) WAITV_BAR(8);
    else if (m < 7) WAITV_BAR(12);
    else if (m == 8) WAITV_BAR(4);
    else WAITV_BAR(0);
    if (m == 1) SL(26);
    if (m == 5) SL(27);
    if (m == 9) SL(28);
    if (m == 1) h_tick(4);
    if (m + 4 < 10 || (m + 4 == 10 && w < 3)) h_tick(m + 4);
  }
  epi4(c, tid, b2v, x1);
}

extern "C" __global__ void __launch_bounds__(512, 2)
fused_block(const u16* __restrict__ x_, const u16* __restrict__ wqkv_, const u16* __restrict__ wo_, const u16* __restrict__ w1_,
            const u16* __restrict__ b1_, const u16* __restrict__ w2_, const u16* __restrict__ b2_, const u16* __restrict__ c_,
            const u16* __restrict__ wmod_, u16* __restrict__ out_, unsigned char* __restrict__ ws_) {
  extern __shared__ __attribute__((aligned(16))) char smem[];
  unsigned long long tstart_;
  asm volatile("s_memrealtime %0" : "=s"(tstart_));
  __builtin_amdgcn_sched_barrier(0);
  asm volatile("" ::"s"(x_), "s"(wqkv_), "s"(wo_), "s"(w1_), "s"(b1_), "s"(w2_), "s"(b2_), "s"(c_), "s"(wmod_), "s"(out_), "s"(ws_));
  asm volatile("s_waitcnt lgkmcnt(0)" : "+s"(tstart_));
  unsigned long long pc;
  asm volatile("s_getpc_b64 %0" : "=s"(pc));
  LAS char* L = (LAS char*)smem;
  const unsigned Lb = (unsigned)(uintptr_t)L;
  const int tid0 = threadIdx.x, w = __builtin_amdgcn_readfirstlane(tid0 >> 6);
  const int b = blockIdx.x, xcd = b & 7, idx = b >> 3, g = xcd * 4 + (idx >> 3), j = idx & 7;
  GAS unsigned char* ws = (GAS unsigned char*)ws_;
  const rsrc_t rs = __builtin_amdgcn_make_buffer_rsrc((void*)ws_, 0, WS_USED, 0x00020000);
  const v4u rsv = {(unsigned)(uintptr_t)ws_, (unsigned)((uintptr_t)ws_ >> 32) & 0xffffu, WS_USED, 0x00020000u};
  SRT(32);
  ST(0);
  unsigned epoch = *(const __attribute__((address_space(4))) unsigned*)(uintptr_t)(ws_ + OFF_E);
#define GET_TAG asm volatile("" : "+s"(epoch) : : "memory"); const unsigned tag = epoch + 1u;
  const int rq = xcd >> 1;
  const int nt3 = j < 6 ? 11 : 10, t03 = j < 6 ? 11 * j : 66 + 10 * (j - 6);
  gu32* gfl = (gu32*)(ws + OFF_GFLAG) + g * 32;
  const v4u rsq = {(unsigned)(uintptr_t)wqkv_, (unsigned)((uintptr_t)wqkv_ >> 32) & 0xffffu, 512u * 1536u * 2u, 0x00020000u};
#define UNIT ((b - 50) * 4 + w)
#define W1V ((8 * (w & 3) + (l >> 3)) * 3072 + 128 * j + 16 * ((l & 7) ^ ((((l >> 4) & 1) | ((w & 1) << 1)) << 1)))
#define LANE_IDS const int l = lane_id(), tid = w * 64 + l, i = l & 15, q = l >> 4; const unsigned w1v = W1V;
  v4u xr[5][4];
  const bool job1 = b < 64;
#define XLOADS(p) do { int tok = 64 * g - 16 + 20 * (w & 3) + (l >> 4) + 4 * (p); tok = tok < 0 ? 0 : tok; \
    _Pragma("unroll") for (int n = 0; n < 4; ++n) \
      asm volatile("global_load_dwordx4 %0, %1, %2" : "=v"(xr[p][n]) : "v"(tok * 1024 + 256 * ((n + rq) & 3) + 16 * (l & 15)), "s"(x_)); } while (0)
  if (w < 4) {
    const int l = lane_id(), i = l & 15, q = l >> 4;
    const int mcb = job1 ? (b & 15) : 16 + ((b - 64) & 31), mks = job1 ? (b >> 4) : ((b - 64) >> 5);
    v2u cw; v4u wm[4];
    if (b < 192) mod_loads(c_, wmod_, mcb, mks, w, l, cw, wm);
    unsigned dummy;
    asm volatile("s_load_dword %0, %1, 0x0\n\t"
                 "s_load_dword %0, %1, 0x40\n\t"
                 "s_load_dword %0, %1, 0x80\n\t"
                 "s_load_dword %0, %1, 0xc0\n\t"
                 "s_load_dword %0, %1, 0x100\n\t"
                 "s_load_dword %0, %1, 0x140" : "=&s"(dummy) : "s"((pc & ~255ull) + idx * CODE_STEP + w * 384));
    if (job1) {
      asm volatile("" : "+s"(epoch) : : "memory");
      MOD_WAIT(0);
      mod_publish(rs, mcb, mks, w, l, mod_job(L, L_MRED, w, l, epoch + 1u, cw, wm));
      SBAR();
    }
    const int u = UNIT;
    const bool retile = b >= 50, isw1 = u >= 128 && u < 480, isw2 = u >= 480;
    const int rcb = isw2 ? (int)((unsigned)(u - 480) / 43u) : ((u - (isw1 ? 128 : 0)) >> 4), rs_ = isw2 ? u - 480 - 43 * rcb : (u & 15);
    const int rnt = (isw1 && rcb == 21) ? 2 : 4;
    if (retile) {
      const GAS u16* src = (const GAS u16*)(isw2 ? w2_ : isw1 ? w1_ : wo_);
      const int n = isw1 ? 1376 : 512, ch = rnt == 2 ? (l & 3) : (l & 7);
      const GAS u16* p = src + (32 * rs_ + (l >> 3)) * n + 64 * rcb + 8 * ch;
#pragma unroll
      for (int e = 0; e < 4; ++e) gdma(p + 8 * e * n, Lb + L_RT + w * 4096 + e * 1024);
    }
    ST(29);
    const unsigned w1v = W1V;
#pragma unroll
    for (int s = 0; s < 11; ++s) dma3(rsq, w1v, ((s + 4 * rq) & 15) * (32 * 3072), Lb + W1POS(s) + w * 3072);
    ST(30);
    int rdst[4];
    const int rcbv = fresh(rcb), rsv_ = fresh(rs_);
#pragma unroll
    for (int t = 0; t < 4; ++t) {
      const int rcb = rcbv, rs_ = rsv_;
      const int tl = 4 * rcb + t;
      const int j3 = tl < 66 ? (int)((unsigned)tl / 11u) : (tl < 76 ? 6 : 7);
      const int s2 = (rs_ - 2 * (isw1 ? j3 : rcb)) & 15;
      int s4 = rs_ - (rcb < 6 ? (11 * rcb + 1) >> 1 : 3 + 5 * rcb); if (s4 < 0) s4 += 43;
      rdst[t] = isw1 ? OFF_W3T + (tl * 16 + s2) * 1024
              : isw2 ? OFF_W4T + ((((rcb * 2 + (t >> 1)) * 2 + (s4 & 1)) * 22 + (s4 >> 1)) * 2 + (t & 1)) * 1024
                     : OFF_W2T + ((((rcb * 2 + (t >> 1)) * 2 + (s2 & 1)) * 8 + (s2 >> 1)) * 2 + (t & 1)) * 1024;
    }
    asm volatile("s_waitcnt lgkmcnt(0)" : "+s"(dummy), "+s"(epoch) : : "memory");
    v4u res;
    if (!job1 && b < 192) { MOD_WAIT(33); res = mod_job(L, L_MRED, w, l, epoch + 1u, cw, wm); }
    ST(1);
    if (retile) {
      WAITV(33);
#pragma unroll
      for (int t = 0; t < 4; ++t)
        if (t < rnt) stf(rs, trfrag(L + L_RT + w * 4096 + (8 * q + (i >> 2)) * 128 + t * 32 + (i & 3) * 8), l * 16, rdst[t]);
      if (rnt == 4) WAITV(4); else WAITV(2);
    } else WAITV(0);
    if (!job1 && b < 192) mod_publish(rs, mcb, mks, w, l, res);
  } else {
    const int l = lane_id();
    __builtin_amdgcn_s_setprio(3);
    if (job1) SBAR();
    XLOADS(0); XLOADS(1); XLOADS(2);
    ST(29);
    SB();
  }
  if (w < 4) {
    asm volatile("; matrix waves" ::: "memory");
    LANE_IDS
    GET_TAG
    ST(2);
    meet4(L, L_SYNC, w, tag, l);
    ST(3);
    const int tb_ = q * 3072 + (i >> 2) * 128 + ((w ^ ((i >> 3) | ((q & 1) << 1))) * 32) + (i & 3) * 8;
    const int tb1 = fresh(tb_ + L_R1);
    v4u wf[16][3];
#pragma unroll
    for (int s = 0; s < 11; ++s)
#pragma unroll
      for (int sg = 0; sg < 3; ++sg) wf[s][sg] = trfrag(L + (s < 5 ? tb1 + s * 12288 : tb_ + W1POS(s)) + sg * 1024);
    ST(7);
    BAR();
    ST(4);
    dma3(rsq, w1v, ((11 + 4 * rq) & 15) * (32 * 3072), Lb + L_R1 + w * 3072);
    f4 aq[4], ak[5], av[5];
    const int a0 = l * 16 ^ (q << 4), a1 = l * 16 ^ ((q | 4) << 4), a0h = fresh(a0 + 61440), a1h = fresh(a1 + 61440);
    v4u a[2][5];
    SBAR();
    ST(5);
#pragma unroll
    for (int r = 0; r < 5; ++r) a[0][r] = *(LAS v4u*)(L + r * 1024 + a0);
#pragma unroll
    for (int s = 0; s < 16; ++s) {
      if (s == 3) WAITV(0);
      if (s == 3 || s == 7 || s == 10 || s == 13) SBAR();
      if (s == 3 && tid == 0) __hip_atomic_store((gu32*)(ws + OFF_RFLAG) + 32 * b, tag, RLX_AGENT);
      if (s == 10) SL(31);
      SB();
#pragma unroll
      for (int r = 0; r < 5; ++r) {
        MAC(s == 0, ak[r], wf[s][1], a[s & 1][r]);
        SB();
        if (s < 11) a[(s + 1) & 1][r] = *(LAS v4u*)(L + ((s + 1) * 5 + r) * 1024 + (((s + 1) & 1) ? a1 : a0));
        else if (s < 15) a[(s + 1) & 1][r] = *(LAS v4u*)(L + ((s - 11) * 5 + r) * 1024 + (((s + 1) & 1) ? a1h : a0h));
        SB();
        MAC(s == 0, av[r], a[s & 1][r], wf[s][2]);
        SB();
        if (s >= 10 && s < 15 && r < 3) { wf[s + 1][r] = trfrag(L + tb1 + (s - 10) * 12288 + r * 1024); SB(); }
        if (r) { MAC(s == 0, aq[r - 1], wf[s][0], a[s & 1][r]); SB(); }
      }
    }
    MFMA_DONE();
    ST(6);
    {
      v2u qb[4], kb[5], vb[5];
#pragma unroll
      for (int r = 0; r < 5; ++r) {
        kb[r] = v2u{pk(ak[r].x, ak[r].y), pk(ak[r].z, ak[r].w)};
        vb[r] = v2u{pk(av[r].x, av[r].y), pk(av[r].z, av[r].w)};
        if (r) qb[r - 1] = v2u{pk(aq[r - 1].x, aq[r - 1].y), pk(aq[r - 1].z, aq[r - 1].w)};
      }
      const bool odd = i & 1;
      const bool cur0 = 4 * q + (i & 1) <= i, cur1 = 4 * q + 2 + (i & 1) <= i;
      const float sc = 0.25f * 1.4426950409f;
      const int hd = 4 * j + w;
      unsigned h[4][2];
#pragma unroll
      for (int r = 0; r < 4; ++r) {
        const f4 z = {0.f, 0.f, 0.f, 0.f};
        f4 sp = mfma16(kb[r], qb[r], z), scu = mfma16(kb[r + 1], qb[r], z);
        float p0 = odd ? sp.y : sp.x, p1 = odd ? sp.w : sp.z, c0 = odd ? scu.y : scu.x, c1 = odd ? scu.w : scu.z;
        if (r == 0 && g == 0) { p0 = -4e9f; p1 = -4e9f; }
        float v0 = cur0 ? c0 : p0, v1 = cur1 ? c1 : p1;
        const float mx = swap32_max(swap16_max(fmaxf(v0, v1)));
        v0 = __builtin_amdgcn_exp2f((v0 - mx) * sc); v1 = __builtin_amdgcn_exp2f((v1 - mx) * sc);
        const float rs_ = __builtin_amdgcn_rcpf(swap32_sum(swap16_sum(v0 + v1)));
        const unsigned pp = pk(v0 * rs_, v1 * rs_);
        const unsigned d0 = odd ? pp << 16 : pp & 0xffffu, d1 = odd ? pp & 0xffff0000u : pp >> 16;
        const v4u pf = {cur0 ? 0u : d0, cur1 ? 0u : d1, cur0 ? d0 : 0u, cur1 ? d1 : 0u};
        const f4 o = mfma32(v4u{vb[r].x, vb[r].y, vb[r + 1].x, vb[r + 1].y}, pf, z);
        h[r][0] = pk(o.x, o.y); h[r][1] = pk(o.z, o.w);
      }
#pragma unroll
      for (int pp = 0; pp < 2; ++pp) {
        auto s0 = __builtin_amdgcn_permlane16_swap(h[2 * pp][0], h[2 * pp + 1][0], false, false);
        auto s1 = __builtin_amdgcn_permlane16_swap(h[2 * pp][1], h[2 * pp + 1][1], false, false);
        stf(rs, v4u{s0[0], s1[0], s0[1], s1[1]}, (q & 1) * 1024 + ((q >> 1) * 16 + i) * 16,
            OFF_O + ((g * 16 + (hd >> 1)) * 4 + 2 * pp) * 1024 + (hd & 1) * 512);
      }
    }
    ST(19);
    const bool two = w < nt3 - 8;
    const Ctx c = {rs, rsv, L, Lb, tag, g, j, w, t03, t03 + (two ? 2 * w : nt3 - 8 + w), gfl, x_, b1_, b2_, out_};
    tail_c(c, two, tid STA);
    ST(16);
    SLF();
    while (__builtin_amdgcn_s_memrealtime() - tstart_ < 3000ull) __builtin_amdgcn_s_sleep(1);
    __builtin_amdgcn_endpgm();
  } else {
    asm volatile("; helper waves" ::: "memory");
    LANE_IDS
    const int v = w - 4;
    const int row0 = 20 * v + q;
    GET_TAG
    ST(1);
    f2 xn[5][16];
    const int cb4[4] = {4 * v, 4 * v + 1, 4 * v + 2, 4 * v + 3};
    v4u mv[4];
#pragma unroll
    for (int p = 0; p < 5; ++p) {
      f2 s2 = {0.f, 0.f}, q2 = {0.f, 0.f};
      asm volatile("s_waitcnt vmcnt(%4)" : "+v"(xr[p][0]), "+v"(xr[p][1]), "+v"(xr[p][2]), "+v"(xr[p][3]) : "i"(p < 3 ? 8 : 16 - 4 * p) : "memory");
      if (p == 0) { XLOADS(3); SB(); }
      if (p == 1) { XLOADS(4); SB(); }
      if (p == 4) {
#pragma unroll
        for (int n = 0; n < 4; ++n) mv[n] = ldf(rs, l * 16, OFF_MODP + cb4[n] * 1024);
        SB();
      }
#pragma unroll
      for (int e = 0; e < 16; ++e) {
        xn[p][e] = unpk(xr[p][e >> 2][e & 3]);
        s2 += xn[p][e];
        q2 = fma2(xn[p][e], xn[p][e], q2);
      }
      const float m = sum16(s2.x + s2.y) * (1.0f / 512.0f);
      const float r = __builtin_amdgcn_rsqf(fmaxf(sum16(q2.x + q2.y) * (1.0f / 512.0f) - m * m, 0.f) + 1e-5f);
      const f2 r2 = {r, r}, mr2 = {-m * r, -m * r};
#pragma unroll
      for (int e = 0; e < 16; ++e) { xn[p][e] = fma2(xn[p][e], r2, mr2); asm volatile("" : "+v"(xn[p][e])); }
    }
    int ha[5];
#pragma unroll
    for (int p = 0; p < 5; ++p) {
      const int r = row0 + 4 * p, qq = i & 3;
      ha[p] = ((i >> 2) * 5 + (r >> 4)) * 1024 + (qq * 16 + ((r & 15) ^ (qq | (i & 4)))) * 16;
      asm volatile("" : "+v"(ha[p]));
    }
    ST(2);
    mod_finish<4, true>(rs, L, cb4, mv, tag, l);
    WAITV(0);
    __builtin_amdgcn_s_setprio(0);
    ST(3);
    BAR();
    ST(4);
#pragma unroll
    for (int n = 0; n < 4; ++n) {
      const int ch = L_MODF + (16 * ((n + rq) & 3) + i) * 32;
      const f4 sa0 = *(LAS f4*)(L + ch), sa1 = *(LAS f4*)(L + ch + 16), ca0 = *(LAS f4*)(L + ch + 2048), ca1 = *(LAS f4*)(L + ch + 2064);
      const f2 sa2[4] = {{sa0.x, sa0.y}, {sa0.z, sa0.w}, {sa1.x, sa1.y}, {sa1.z, sa1.w}}, ca2[4] = {{ca0.x, ca0.y}, {ca0.z, ca0.w}, {ca1.x, ca1.y}, {ca1.z, ca1.w}};
      v4u o[5];
#pragma unroll
      for (int p = 0; p < 5; ++p)
#pragma unroll
        for (int e = 0; e < 4; ++e) {
          if (n == 0) o[p][e] = pk2(fma2(xn[p][4 * n + e], ca2[e], sa2[e]));
          else o[p][e] = pk(fma1(xn[p][4 * n + e].x, ca2[e].x, sa2[e].x), fma1(xn[p][4 * n + e].y, ca2[e].y, sa2[e].y));
        }
#pragma unroll
      for (int p = 0; p < 5; ++p) *(LAS v4u*)(L + ha[p] + n * 20480) = o[p];
      if (n == 2) dma3(rsq, w1v, ((13 + 4 * rq) & 15) * (32 * 3072), Lb + L_R1 + 2 * 12288 + v * 3072);
      if (n < 3) BAR();
      if (n == 0) ST(5);
      if (n == 1) {
        dma3(rsq, w1v, ((12 + 4 * rq) & 15) * (32 * 3072), Lb + L_R1 + 12288 + v * 3072);
      }
    }
    WAITV_BAR(0);
    ST(6);
    dma3(rsq, w1v, ((14 + 4 * rq) & 15) * (32 * 3072), Lb + L_R1 + 3 * 12288 + v * 3072);
    dma3(rsq, w1v, ((15 + 4 * rq) & 15) * (32 * 3072), Lb + L_R1 + 4 * 12288 + v * 3072);
    v4u rf;
    if (w == 4) rf = ld_rflags(rs, l);
    WAITV(0);
    SBAR();
    {
      if (w == 4) {
        _Pragma("nounroll") for (unsigned s = 0; s < SPIN_CAP; ++s) {
          if (__all(rf.x == tag && rf.y == tag && rf.z == tag && rf.w == tag)) break;
          __builtin_amdgcn_s_sleep(8);
          asm volatile("" ::: "memory");
          rf = ld_rflags(rs, l);
        }
        *(volatile LAS unsigned*)(L + L_OK) = tag;
      } else lds_wait(L, L_OK, tag);
      __builtin_amdgcn_fence(__ATOMIC_ACQUIRE, "wavefront");
      asm volatile("" ::: "memory");
    }
    ST(31);
    const Ctx c = {rs, rsv, L, Lb, tag, g, j, w, t03, t03 + nt3 - 8 + w, gfl, x_, b1_, b2_, out_};
    const int nh = v & 1, kh = v >> 1;
    const unsigned w2off = OFF_W2T + ((j * 2 + nh) * 2 + kh) * 16384, w3off = OFF_W3T + c.tile0 * 16384;
    v4u w2[16], w3a[16], w3b[16], w4[44];
#pragma unroll
    for (int n = 0; n < 16; ++n) w2[n] = LDW(n, w2off);
    SB();
    ST(7);
    if (b == 0 && tid == 448) *(GAS unsigned*)(ws + OFF_E) = tag;
    const v4u xres = load_xres(c, tid);
    const float b1a = bflo(((const GAS u16*)b1_)[c.tile0 * 16 + i]);
    ST(19);
    SBAR();
    ST(8);
    f2 x1[4];
    {
      f4 acc[4][2];
      v4u a2[2][4];
      const int ab = kh * 4096 + l * 16;
      SBAR(); SL(20);
#pragma unroll
      for (int r = 0; r < 4; ++r) a2[0][r] = *(LAS v4u*)(L + ab + r * 1024);
#pragma unroll
      for (int n = 0; n < 8; ++n) {
        if (n == 1 || n == 3 || n == 5) SBAR();
        SB();
#pragma unroll
        for (int r = 0; r < 4; ++r) {
          MAC(n == 0, acc[r][0], w2[2 * n], a2[n & 1][r]);
          SB();
          if (n < 7) a2[(n + 1) & 1][r] = *(LAS v4u*)(L + ab + ((n + 1) * 8 + r) * 1024);
          SB();
          MAC(n == 0, acc[r][1], w2[2 * n + 1], a2[n & 1][r]);
          SB();
        }
      }
      MFMA_DONE();
      SL(21);
      const int lf = fresh(l), rb = L_RED + kh * 17408 + (lf & 15) * 272 + nh * 128 + (lf >> 4) * 16;
#pragma unroll
      for (int r = 0; r < 4; ++r)
#pragma unroll
        for (int t = 0; t < 2; ++t) *(LAS f4*)(L + rb + r * 16 * 272 + t * 64) = acc[r][t];
    }
    epi2(c, tid, xres, x1);
    ST(17);
    const unsigned w4off = OFF_W4T + ((j * 2 + nh) * 2 + kh) * 44 * 1024;
    asm volatile("" ::: "memory");
#pragma unroll
    for (int n = 0; n < 8; ++n) w3a[n] = LDW(n, w3off);
    WAITV_BAR(8);
    ST(9);
#pragma unroll
    for (int n = 8; n < 16; ++n) w3a[n] = LDW(n, w3off);
#pragma unroll
    for (int n = 0; n < 8; ++n) w4[n] = LDW(n, w4off);
    SB();
    float uvt[1];
    col_terms<1>(c, false, l, w3a, w3b, b1a, b1a, uvt);
    USE(uvt[0]);
    SBAR();
    ST(10);
    {
      f4 acc[4][1];
      const float S[5] = {0.f, 0.f, 0.f, 0.f, 0.f};
      mlp1_loop<1, 1>(c, l, w3a, w3b, acc, w4, w4off, false, S STA);
      BAR();
      ST(12);
      mlp1_epi<1>(c, false, l, uvt, acc);
    }
    ST(18);
    WAITV_BAR(0);
    if (tid == 256) __hip_atomic_store(gfl + 64 * 32 + j, tag, RLX_AGENT);
    ST(13);
#pragma unroll
    for (int n = 32; n < 44; ++n) w4[n] = LDW(n, w4off);
    const v4u b2v = load_b2(c, tid);
    {
      f4 acc[4][2];
      v4u a4[2][4];
      const int ab = kh * 4096 + l * 16;
      SBAR();
      ST(14);
#pragma unroll
      for (int r = 0; r < 4; ++r) a4[0][r] = *(LAS v4u*)(L + ab + r * 1024);
#pragma unroll
      for (int n = 0; n < 22; ++n) {
        if ((n & 1) && n < 19) SBAR();
        if (n == 1) SL(26);
        if (n == 9) SL(27);
        if (n == 17) SL(28);
        SB();
        const int sp = 2 * (n + 1) >= 38 ? 2 * (n + 1) - 38 : 2 * (n + 1);
        if (n < 21 || kh == 0) {
#pragma unroll
          for (int r = 0; r < 4; ++r) {
            MAC(n == 0, acc[r][0], w4[2 * n], a4[n & 1][r]);
            SB();
            if (n < 21) a4[(n + 1) & 1][r] = *(LAS v4u*)(L + ab + sp * 4096 + r * 1024);
            SB();
            MAC(n == 0, acc[r][1], w4[2 * n + 1], a4[n & 1][r]);
            SB();
          }
        }
      }
      MFMA_DONE();
      ST(15);
      const int lf = fresh(l), rb = L_RED + kh * 17408 + (lf & 15) * 272 + nh * 128 + (lf >> 4) * 16;
#pragma unroll
      for (int r = 0; r < 4; ++r)
#pragma unroll
        for (int t = 0; t < 2; ++t) *(LAS f4*)(L + rb + r * 16 * 272 + t * 64) = acc[r][t];
    }
    epi4(c, tid, b2v, x1);
    ST(16);
    SLF();
    while (__builtin_amdgcn_s_memrealtime() - tstart_ < 3000ull) __builtin_amdgcn_s_sleep(1);
    __builtin_amdgcn_endpgm();
  }
}

extern "C" void kernel_launch(void* const* d_in, const int* in_sizes, int n_in, void* d_out, int out_size, void* d_ws, size_t ws_size,
                              hipStream_t stream) {
  (void)hipFuncSetAttribute((const void*)fused_block, hipFuncAttributeMaxDynamicSharedMemorySize, LDS_BYTES);
  fused_block<<<256, 512, LDS_BYTES, stream>>>((const u16*)d_in[0], (const u16*)d_in[1], (const u16*)d_in[2], (const u16*)d_in[4],
                                               (const u16*)d_in[5], (const u16*)d_in[6], (const u16*)d_in[7], (const u16*)d_in[8],
                                               (const u16*)d_in[9], (u16*)d_out, (unsigned char*)d_ws);
}
```

```cpp
#include <hip/hip_runtime.h>
#include <stdint.h>

#define GAS __attribute__((address_space(1)))
#define LAS __attribute__((address_space(3)))
#define DI __device__ __forceinline__
typedef unsigned short u16;
typedef unsigned v4u __attribute__((ext_vector_type(4)));
typedef unsigned v2u __attribute__((ext_vector_type(2)));
typedef float f4 __attribute__((ext_vector_type(4)));
typedef float f2 __attribute__((ext_vector_type(2)));
typedef __bf16 bf2 __attribute__((ext_vector_type(2)));
typedef __bf16 bf8 __attribute__((ext_vector_type(8)));
typedef short s4 __attribute__((ext_vector_type(4)));
typedef GAS unsigned gu32;
typedef GAS unsigned long long gu64;
#define RLX_AGENT __ATOMIC_RELAXED, __HIP_MEMORY_SCOPE_AGENT

constexpr unsigned OFF_E = 0;
constexpr unsigned OFF_RFLAG = 768u << 10;
constexpr unsigned OFF_GFLAG = 800u << 10;
constexpr unsigned OFF_MODP = 16384;
constexpr unsigned OFF_STAT = 65536;
constexpr unsigned OFF_W3T = 1u << 20;
constexpr unsigned OFF_W4T = OFF_W3T + 1376 * 1024;
constexpr unsigned OFF_O = OFF_W4T + 1408 * 1024;
constexpr unsigned OFF_XA = OFF_O + 2048 * 1024;
constexpr unsigned OFF_H1 = OFF_XA + 2048 * 1024;
constexpr unsigned WS_USED = OFF_H1 + 32 * 43 * 4096;
constexpr unsigned OFF_STAMP = 64u << 20;

constexpr int L_R1 = 81920;
constexpr int L_RED = 65536;
constexpr int L_STAT = 102400;
constexpr int L_UV = 147456;
constexpr int L_RT = 49152;
constexpr int L_MODF = 155648;
constexpr int L_EC = 159744;
constexpr int L_MRED = 77824;
constexpr int L_SYNC = 79872;
constexpr int L_MRED2 = L_MODF + 2048;
constexpr int L_COEF = L_MODF;
constexpr int L_RR = L_COEF + 1024;
constexpr int L_W = L_MODF + 3584;
constexpr int LDS_BYTES = 163840;

constexpr int CODE_STEP = 1216;
constexpr unsigned SPIN_CAP = 1u << 18;

#define ST(n)
#define SL(n)
#define SRT(n)
#define SLF()

DI float bflo(unsigned u) { return __uint_as_float(u << 16); }
DI float bfhi(unsigned u) { return __uint_as_float(u & 0xffff0000u); }
DI unsigned pk(float a, float b) { f2 v = {a, b}; return __builtin_bit_cast(unsigned, __builtin_convertvector(v, bf2)); }
DI f2 unpk(unsigned u) { return f2{bflo(u), bfhi(u)}; }
DI f2 fma2(f2 a, f2 b, f2 c) { return __builtin_elementwise_fma(a, b, c); }
DI unsigned pk2(f2 v) { return __builtin_bit_cast(unsigned, __builtin_convertvector(v, bf2)); }
DI f4 mfma32(v4u a, v4u b, f4 c) { return __builtin_amdgcn_mfma_f32_16x16x32_bf16(__builtin_bit_cast(bf8, a), __builtin_bit_cast(bf8, b), c, 0, 0, 0); }
template <bool FIRST> DI void mac(f4& c, v4u a, v4u b) {
  if constexpr (FIRST) asm volatile("v_mfma_f32_16x16x32_bf16 %0, %1, %2, 0" : "=&v"(c) : "v"(a), "v"(b));
  else asm volatile("v_mfma_f32_16x16x32_bf16 %0, %1, %2, %0" : "+v"(c) : "v"(a), "v"(b));
}
#define MAC(first, c, a, b) do { if (first) mac<true>(c, a, b); else mac<false>(c, a, b); } while (0)
#define MFMA_DONE() do { asm volatile("s_nop 15" ::: "memory"); __builtin_amdgcn_sched_barrier(0); } while (0)
DI f4 mfma16(v2u a, v2u b, f4 c) { return __builtin_amdgcn_mfma_f32_16x16x16bf16_1k(__builtin_bit_cast(s4, a), __builtin_bit_cast(s4, b), c, 0, 0, 0); }
template <int CTRL> DI float dpp(float x) { return __builtin_bit_cast(float, __builtin_amdgcn_update_dpp(0, __builtin_bit_cast(int, x), CTRL, 0xf, 0xf, true)); }
DI float swap16_sum(float x) { auto r = __builtin_amdgcn_permlane16_swap(__float_as_uint(x), __float_as_uint(x), false, false); return __uint_as_float(r[0]) + __uint_as_float(r[1]); }
DI float swap32_sum(float x) { auto r = __builtin_amdgcn_permlane32_swap(__float_as_uint(x), __float_as_uint(x), false, false); return __uint_as_float(r[0]) + __uint_as_float(r[1]); }
DI float swap16_max(float x) { auto r = __builtin_amdgcn_permlane16_swap(__float_as_uint(x), __float_as_uint(x), false, false); return fmaxf(__uint_as_float(r[0]), __uint_as_float(r[1])); }
DI float swap32_max(float x) { auto r = __builtin_amdgcn_permlane32_swap(__float_as_uint(x), __float_as_uint(x), false, false); return fmaxf(__uint_as_float(r[0]), __uint_as_float(r[1])); }
DI float sum8(float x) { x += dpp<0xB1>(x); x += dpp<0x4E>(x); x += dpp<0x141>(x); return x; }
DI float sum16(float x) { x = sum8(x); x += dpp<0x140>(x); return x; }

#define BAR() asm volatile("s_waitcnt lgkmcnt(0)\n\ts_barrier" ::: "memory")
#define WAITV(n) do { asm volatile("s_waitcnt vmcnt(%0)" ::"i"(n) : "memory"); if ((n) == 0) __builtin_amdgcn_s_waitcnt(0x0F70); } while (0)
#define WAITV_BAR(n) do { asm volatile("s_waitcnt vmcnt(%0) lgkmcnt(0)\n\ts_barrier" ::"i"(n) : "memory"); if ((n) == 0) __builtin_amdgcn_s_waitcnt(0x0F70); } while (0)
#define SBAR() asm volatile("s_barrier" ::: "memory")
#define USE(x) asm volatile("" ::"v"(x))

template <int N> DI void dma(v4u rs, unsigned voff, unsigned soff, unsigned lds) {
  unsigned keep;
  if constexpr (N == 1)
    asm volatile("s_mov_b32 %0, m0\n\ts_mov_b32 m0, %2\n\ts_nop 0\n\t"
                 "buffer_load_dwordx4 %1, %3, %4 offen sc1 lds\n\t"
                 "s_mov_b32 m0, %0" : "=&s"(keep) : "v"(voff), "s"(lds), "s"(rs), "s"(soff) : "memory");
  else if constexpr (N == 2)
    asm volatile("s_mov_b32 %0, m0\n\ts_mov_b32 m0, %2\n\ts_nop 0\n\t"
                 "buffer_load_dwordx4 %1, %3, %4 offen sc1 lds\n\t"
                 "buffer_load_dwordx4 %1, %3, %4 offen offset:1024 sc1 lds\n\t"
                 "s_mov_b32 m0, %0" : "=&s"(keep) : "v"(voff), "s"(lds), "s"(rs), "s"(soff) : "memory");
  else
    asm volatile("s_mov_b32 %0, m0\n\ts_mov_b32 m0, %2\n\ts_nop 0\n\t"
                 "buffer_load_dwordx4 %1, %3, %4 offen sc1 lds\n\t"
                 "buffer_load_dwordx4 %1, %3, %4 offen offset:1024 sc1 lds\n\t"
                 "buffer_load_dwordx4 %1, %3, %4 offen offset:2048 sc1 lds\n\t"
                 "buffer_load_dwordx4 %1, %3, %4 offen offset:3072 sc1 lds\n\t"
                 "s_mov_b32 m0, %0" : "=&s"(keep) : "v"(voff), "s"(lds), "s"(rs), "s"(soff) : "memory");
}
DI void dma3(v4u rs, unsigned voff, unsigned soff, unsigned lds) {
  unsigned keep;
  asm volatile("s_mov_b32 %0, m0\n\ts_mov_b32 m0, %2\n\ts_nop 0\n\t"
               "buffer_load_dwordx4 %1, %3, %4 offen lds\n\t"
               "buffer_load_dwordx4 %1, %3, %4 offen offset:1024 lds\n\t"
               "buffer_load_dwordx4 %1, %3, %4 offen offset:2048 lds\n\t"
               "s_mov_b32 m0, %0" : "=&s"(keep) : "v"(voff), "s"(lds), "s"(rs), "s"(soff) : "memory");
}
DI v4u trfrag(LAS char* p) {
  v2u lo = __builtin_bit_cast(v2u, __builtin_amdgcn_ds_read_tr16_b64_v4i16((LAS s4*)p));
  v2u hi = __builtin_bit_cast(v2u, __builtin_amdgcn_ds_read_tr16_b64_v4i16((LAS s4*)(p + 512)));
  return v4u{lo.x, lo.y, hi.x, hi.y};
}
DI int lane_id() { int r; asm volatile("v_mbcnt_lo_u32_b32 %0, -1, 0\n\tv_mbcnt_hi_u32_b32 %0, -1, %0" : "=v"(r)); return r; }
DI int fresh(int x) { asm volatile("" : "+v"(x)); return x; }
#define SB() __builtin_amdgcn_sched_barrier(0)
DI void dma_nt(v4u rs, unsigned voff, unsigned soff, unsigned lds) {
  unsigned keep;
  asm volatile("s_mov_b32 %0, m0\n\ts_mov_b32 m0, %2\n\ts_nop 0\n\tbuffer_load_dwordx4 %1, %3, %4 offen nt lds\n\ts_mov_b32 m0, %0"
               : "=&s"(keep) : "v"(voff), "s"(lds), "s"(rs), "s"(soff) : "memory");
}
DI void gdma4(const GAS void* p, unsigned lds) {
  unsigned keep;
  asm volatile("s_mov_b32 %0, m0\n\ts_mov_b32 m0, %2\n\ts_nop 0\n\tglobal_load_lds_dword %1, off\n\ts_mov_b32 m0, %0"
               : "=&s"(keep) : "v"(p), "s"(lds) : "memory");
}
typedef __amdgpu_buffer_rsrc_t rsrc_t;
DI v4u ldf(rsrc_t rs, unsigned voff, unsigned soff) { return __builtin_amdgcn_raw_buffer_load_b128(rs, voff, soff, 16); }
#define LDW(n, base) ldw_rt(c.rs, l * 16, base, n)
DI v4u ldw_rt(rsrc_t rs, unsigned voff, unsigned base, int n) { return __builtin_amdgcn_raw_buffer_load_b128(rs, voff + (n & 3) * 1024, base + (n >> 2) * 4096, 16); }
DI v4u ld_rflags(rsrc_t rs, int l) { return ldf(rs, (l >> 1) * 128 + (l & 1) * 16, OFF_RFLAG); }
DI void stf(rsrc_t rs, v4u v, unsigned voff, unsigned soff) { __builtin_amdgcn_raw_buffer_store_b128(v, rs, voff + soff, 0, 16); }

DI void meet4(LAS char* L, int at, int me, unsigned tag, int l) {
  volatile LAS unsigned* f = (volatile LAS unsigned*)(L + at);
  f[me] = tag;
  _Pragma("nounroll") for (unsigned s = 0; s < 16 * SPIN_CAP; ++s)
    if (__all(f[l & 3] == tag)) break;
  asm volatile("" ::: "memory");
}
DI void post(LAS char* L, int at, unsigned tag) { *(volatile LAS unsigned*)(L + at) = tag; }
DI void wait4(LAS char* L, int at, unsigned tag, int l) {
  volatile LAS unsigned* f = (volatile LAS unsigned*)(L + at);
  _Pragma("nounroll") for (unsigned s = 0; s < 16 * SPIN_CAP; ++s)
    if (__all(f[l & 3] == tag)) break;
  asm volatile("" ::: "memory");
}
DI void lds_wait(LAS char* L, int at, unsigned tag) {
  volatile LAS unsigned* f = (volatile LAS unsigned*)(L + at);
  _Pragma("nounroll") for (unsigned s = 0; s < 16 * SPIN_CAP; ++s)
    if (*f == tag) break;
  asm volatile("" ::: "memory");
}
DI void poll8(gu32* fl, unsigned tag, int l) {
  _Pragma("nounroll") for (unsigned s = 0; s < SPIN_CAP; ++s) {
    unsigned f = __hip_atomic_load(fl + (l & 7), RLX_AGENT);
    if (__all(f == tag)) break;
    __builtin_amdgcn_s_sleep(1);
  }
  __builtin_amdgcn_fence(__ATOMIC_ACQUIRE, "wavefront");
}

DI unsigned gelu2(f2 a, f2 k1, f2 k2, f2 ir) {
  const f2 t = a * fma2(a * a, k2, k1);
  const f2 d = fma2(f2{__builtin_amdgcn_exp2f(t.x), __builtin_amdgcn_exp2f(t.y)}, ir, ir);
  return pk2(a * f2{__builtin_amdgcn_rcpf(d.x), __builtin_amdgcn_rcpf(d.y)});
}

DI void mod_get(rsrc_t rs, unsigned voff, v4u (&v)[4]) {
#pragma unroll
  for (int k = 0; k < 4; ++k) v[k] = ldf(rs, voff, OFF_MODP + k * 12288);
}
DI v2u mod_sum(rsrc_t rs, unsigned voff, v4u (&v)[4], unsigned tag, float plus) {
  const unsigned tb = tag & 0xffu;
  _Pragma("nounroll") for (unsigned s = 0; s < SPIN_CAP; ++s) {
#pragma unroll
    for (int k = 0; k < 4; ++k) v[k] ^= tb;
    const v4u d = (v[0] | v[1]) | (v[2] | v[3]);
    if (__all(((d.x | d.y) | (d.z | d.w)) << 24 == 0u)) break;
    __builtin_amdgcn_s_sleep(1);
    asm volatile("" ::: "memory");
    mod_get(rs, voff, v);
  }
  const f4 s = (__builtin_bit_cast(f4, v[0]) + __builtin_bit_cast(f4, v[1])) + (__builtin_bit_cast(f4, v[2]) + __builtin_bit_cast(f4, v[3]));
  const f2 p2 = {plus, plus};
  return v2u{pk2(unpk(pk(s.x, s.y)) + p2), pk2(unpk(pk(s.z, s.w)) + p2)};
}
DI f4 widen(v2u p) { return f4{bflo(p.x), bfhi(p.x), bflo(p.y), bfhi(p.y)}; }

#define STP
#define STA
DI float fma1(float a, float b, float c) { float r; asm("v_fma_f32 %0, %1, %2, %3" : "=v"(r) : "v"(a), "v"(b), "v"(c)); return r; }
#define W1POS(s) ((s) < 5 ? L_R1 + (s) * 12288 : ((s) - 5) * 12288)

DI v4u mkrs(const void* p, unsigned bytes) { return v4u{(unsigned)(uintptr_t)p, (unsigned)((uintptr_t)p >> 32) & 0xffffu, bytes, 0x00020000u}; }
DI void mod_loads(const u16* c_, const u16* wmod_, bool on, int mcb, int mks, int v, int l, v2u& cw, v4u (&wm)[4]) {
  const int r0 = 128 * mks + 32 * v + 4 * (l >> 3);
  const v4u rc = mkrs(c_, on ? 1024u : 0u), rw = mkrs(wmod_, on ? 512u * 6144u : 0u);
  asm volatile("buffer_load_dwordx2 %0, %1, %2, 0 offen" : "=v"(cw) : "v"(r0 * 2), "s"(rc));
#pragma unroll
  for (int e = 0; e < 4; ++e) asm volatile("buffer_load_dwordx4 %0, %1, %2, %3 offen nt" : "=v"(wm[e]) : "v"(r0 * 6144 + 128 * mcb + 16 * (l & 7)), "s"(rw), "s"(e * 6144));
}
#define MOD_WAIT(n) asm volatile("s_waitcnt vmcnt(%5)" : "+v"(cw), "+v"(wm[0]), "+v"(wm[1]), "+v"(wm[2]), "+v"(wm[3]) : "i"(n) : "memory")
DI v4u mod_job(LAS char* L, int red, int v, int l, unsigned tag, v2u cw, const v4u (&wm)[4]) {
  v4u res = {0u, 0u, 0u, 0u};
  f2 a2[4];
#pragma unroll
  for (int e = 0; e < 4; ++e) {
    const float cv = (e & 1) ? bfhi(cw[e >> 1]) : bflo(cw[e >> 1]);
    float sv = cv * __builtin_amdgcn_rcpf(1.0f + __builtin_amdgcn_exp2f(-1.4426950409f * cv));
    sv = bflo(pk(sv, sv));
    const f2 s2 = {sv, sv};
#pragma unroll
    for (int k = 0; k < 4; ++k) a2[k] = e ? fma2(s2, unpk(wm[e][k]), a2[k]) : s2 * unpk(wm[e][k]);
  }
  float a[4];
#pragma unroll
  for (int k = 0; k < 4; ++k) {
    auto r = __builtin_amdgcn_permlane32_swap(__float_as_uint(a2[k >> 1][k & 1]), __float_as_uint(a2[2 + (k >> 1)][k & 1]), false, false);
    a[k] = __uint_as_float(r[0]) + __uint_as_float(r[1]);
  }
#pragma unroll
  for (int k = 0; k < 2; ++k) {
    auto r = __builtin_amdgcn_permlane16_swap(__float_as_uint(a[k]), __float_as_uint(a[k + 2]), false, false);
    a[k] = __uint_as_float(r[0]) + __uint_as_float(r[1]);
    a[k] += dpp<0x128>(a[k]);
  }
  if (!(l & 8)) *(LAS f2*)(L + red + v * 256 + (8 * (l & 7) + 2 * ((l >> 4) & 1) + 4 * (l >> 5)) * 4) = f2{a[0], a[1]};
  asm volatile("s_waitcnt lgkmcnt(0)" ::: "memory");
  volatile LAS unsigned* mf = (volatile LAS unsigned*)(L + red + 1024);
  if (v < 3) mf[v] = tag;
  else {
    _Pragma("nounroll") for (unsigned s = 0; s < SPIN_CAP; ++s)
      if (__all(mf[l < 3 ? l : 0] == tag)) break;
    asm volatile("" ::: "memory");
    f4 s = *(LAS f4*)(L + red + (l & 15) * 16);
#pragma unroll
    for (int ww = 1; ww < 4; ++ww) s += *(LAS f4*)(L + red + ww * 256 + (l & 15) * 16);
    res = (__builtin_bit_cast(v4u, s) & 0xffffff00u) | (tag & 0xffu);
  }
  return res;
}
DI void mod_publish(rsrc_t rs, int mcb, int mks, int v, int l, v4u res) {
  if (v == 3 && l < 16) stf(rs, res, l * 16, OFF_MODP + mks * 12288 + mcb * 256);
}

struct Ctx {
  rsrc_t rs; v4u rsv; LAS char* L; unsigned Lb, tag; int g, j, w, t03, tile0; gu32* gfl;
  const u16 *x_, *b1_, *b2_; u16* out_;
};

template <int T> DI void col_terms(const Ctx& c, bool two, int l, const v4u (&w3a)[16], const v4u (&w3b)[16], float b1a, float b1b, float (&uvt)[T]) {
  l = fresh(l);
  const int i = l & 15, q = l >> 4, ty = i >> 2;
  const int base = L_UV + (ty < 2 ? ty * 2048 + c.j * 128 : 2048 + ty * 1024) + q * 16;
  f4 a[2][T];
  v4u am[16];
#pragma unroll
  for (int s = 0; s < 16; ++s) am[s] = *(LAS v4u*)(c.L + base + s * 64);
  SB();
#pragma unroll
  for (int s = 0; s < 16; ++s) MAC(s < 2, a[s & 1][0], am[s], w3a[s]);
  if (T == 2 && two) {
#pragma unroll
    for (int s = 0; s < 16; ++s) MAC(s < 2, a[s & 1][T - 1], am[s], w3b[s]);
  }
  MFMA_DONE();
  uvt[0] = a[0][0].x + a[1][0].x + (q == 3 ? b1a : 0.f);
  if (T == 2) uvt[T - 1] = a[0][T - 1].x + a[1][T - 1].x + (q == 3 ? b1b : 0.f);
}

template <int T, int KIND>
DI void mlp1_loop(const Ctx& c, int l, const v4u (&w3a)[16], const v4u (&w3b)[16], f4 (&acc)[4][T], v4u (&w4)[44], unsigned w4off, bool two,
                  const float (&S)[5] STP) {
  LAS char* L = c.L;
  const int tid = c.w * 64 + l;
  auto xa_slot = [&](int slot) { dma<4>(c.rsv, l * 16, OFF_XA + (c.g * 16 + ((slot + 2 * c.j) & 15)) * 4096, c.Lb + slot * 4096); };
  v4u a[2][4];
#pragma unroll
  for (int r = 0; r < 4; ++r) a[0][r] = *(LAS v4u*)(L + r * 1024 + l * 16);
#pragma unroll
  for (int s = 0; s < 16; ++s) {
    if (s == 1 || s == 5 || s == 9 || s == 13) {
      if (KIND == 2 && c.w == 3) {
        if (s == 1) WAITV(16);
        if (s == 5) WAITV(24);
        if (s == 9) WAITV(16);
      }
      SBAR();
      SL(22 + s / 4);
      if (KIND == 2 && c.w == 3) {
        if (s == 1) {
#pragma unroll
          for (int k = 10; k < 16; ++k) xa_slot(k);
        }
        if (s == 5) {
#pragma unroll
          for (int p = 0; p < 4; ++p) dma<4>(c.rsv, l * 16, OFF_STAT + c.g * 16384 + p * 4096, c.Lb + L_STAT + p * 4096);
        }
        if (s == 9) {
          float sx, sxx, sp, spp, spa, sps;
          _Pragma("nounroll") for (unsigned it = 0; it < SPIN_CAP; ++it) {
            WAITV(0);
            v4u a4[8]; v2u b2[8];
            unsigned d = 0;
#pragma unroll
            for (int p = 0; p < 8; ++p) {
              a4[p] = *(LAS v4u*)(L + L_STAT + p * 2048 + l * 16) ^ (c.tag & 0xffu);
              b2[p] = *(LAS v2u*)(L + L_STAT + p * 2048 + 1024 + l * 16) ^ (c.tag & 0xffu);
              d |= (a4[p].x | a4[p].y) | (a4[p].z | a4[p].w) | (b2[p].x | b2[p].y);
            }
            f4 ta = __builtin_bit_cast(f4, a4[0]); f2 tb = __builtin_bit_cast(f2, b2[0]);
#pragma unroll
            for (int p = 1; p < 8; ++p) { ta += __builtin_bit_cast(f4, a4[p]); tb += __builtin_bit_cast(f2, b2[p]); }
            sx = ta.x; sxx = ta.y; sp = ta.z; spp = ta.w; spa = tb.x; sps = tb.y;
            if (__all(d << 24 == 0u)) break;
            asm volatile("s_waitcnt lgkmcnt(0)" ::: "memory");
#pragma unroll
            for (int p = 0; p < 4; ++p) dma<4>(c.rsv, l * 16, OFF_STAT + c.g * 16384 + p * 4096, c.Lb + L_STAT + p * 4096);
          }
          const float inv = 1.0f / 512.0f;
          const float m = sx * inv, var = fmaxf(sxx * inv - m * m, 0.f);
          const float sd = __builtin_amdgcn_sqrtf(var + 1e-5f), r = __builtin_amdgcn_rcpf(sd);
          const float m2 = (r * (sp - m * S[0]) + S[1]) * inv;
          const float e2 = (r * r * (spp - 2.f * m * spa + m * m * S[2]) + 2.f * r * (sps - m * S[3]) + S[4]) * inv;
          const float sd2 = __builtin_amdgcn_sqrtf(fmaxf(e2 - m2 * m2, 0.f) + 1e-5f);
          const float R = r * __builtin_amdgcn_rcpf(sd2), k1 = -2.302208198f * R;
          *(LAS f4*)(L + L_COEF + l * 16) = f4{-m, sd, -m2 * sd, sd * sd2};
          *(LAS f4*)(L + L_RR + l * 16) = f4{k1, 0.044715f * k1 * R * R, sd * sd2, 0.f};
        }
      }
    }
    SB();
#pragma unroll
    for (int r = 0; r < 4; ++r) {
      MAC(s == 0, acc[r][0], w3a[s], a[s & 1][r]);
      SB();
      if (s < 15) a[(s + 1) & 1][r] = *(LAS v4u*)(L + ((s + 1) * 4 + r) * 1024 + l * 16);
      SB();
    }
    if (T == 2 && two) {
#pragma unroll
      for (int r = 0; r < 4; ++r) MAC(s == 0, acc[r][T - 1], w3b[s], a[s & 1][r]);
      SB();
    }
    if (KIND == 1) {
      const int n0 = s < 4 ? 2 * s : 3 * s - 4;
      w4[n0] = LDW(n0, w4off);
      w4[n0 + 1] = LDW(n0 + 1, w4off);
      if (s >= 4) w4[n0 + 2] = LDW(n0 + 2, w4off);
      SB();
    }
  }
  MFMA_DONE();
}

template <int T> DI void mlp1_epi(const Ctx& c, bool two, int l, const float (&uvt)[T], f4 (&acc)[4][T]) {
  l = fresh(l);
  const int i = l & 15, q = l >> 4;
  float cf[4]; f4 rr[4];
#pragma unroll
  for (int r = 0; r < 4; ++r) {
    cf[r] = *(LAS float*)(c.L + L_COEF + (r * 16 + i) * 16 + q * 4);
    rr[r] = *(LAS f4*)(c.L + L_RR + (r * 16 + i) * 16);
  }
#pragma unroll
  for (int t = 0; t < T; ++t) {
    if (t && !two) break;
#pragma unroll
    for (int r = 0; r < 4; ++r) acc[r][t] = __builtin_amdgcn_mfma_f32_16x16x4f32(uvt[t], cf[r], acc[r][t], 0, 0, 0);
  }
  SB();
#pragma unroll
  for (int t = 0; t < T; ++t) {
    if (t && !two) break;
    const int tl = c.tile0 + t;
#pragma unroll
    for (int pp = 0; pp < 2; ++pp) {
      unsigned h[2][2];
#pragma unroll
      for (int r2 = 0; r2 < 2; ++r2) {
        const int r = 2 * pp + r2;
        const f2 k1 = {rr[r].x, rr[r].x}, k2 = {rr[r].y, rr[r].y}, ir = {rr[r].z, rr[r].z};
        h[r2][0] = gelu2(f2{acc[r][t].x, acc[r][t].y}, k1, k2, ir);
        h[r2][1] = gelu2(f2{acc[r][t].z, acc[r][t].w}, k1, k2, ir);
      }
      auto s0 = __builtin_amdgcn_permlane16_swap(h[0][0], h[1][0], false, false);
      auto s1 = __builtin_amdgcn_permlane16_swap(h[0][1], h[1][1], false, false);
      stf(c.rs, v4u{s0[0], s1[0], s0[1], s1[1]}, (q & 1) * 1024 + ((q >> 1) * 16 + i) * 16,
          OFF_H1 + ((c.g * 43 + (tl >> 1)) * 4 + 2 * pp) * 1024 + (tl & 1) * 512);
      SB();
    }
  }
}

DI void dma_in(v4u rs, unsigned voff, unsigned soff, unsigned lds) {
  unsigned keep;
  asm volatile("s_mov_b32 %0, m0\n\ts_mov_b32 m0, %2\n\ts_nop 0\n\tbuffer_load_dwordx4 %1, %3, %4 offen lds\n\ts_mov_b32 m0, %0"
               : "=&s"(keep) : "v"(voff), "s"(lds), "s"(rs), "s"(soff) : "memory");
}
DI void sum8x6(float& a, float& b, float& c, float& d, float& e, float& f) {
#define S6(ctl) "v_add_f32_dpp %0, %0, %0 " ctl "\n\tv_add_f32_dpp %1, %1, %1 " ctl "\n\tv_add_f32_dpp %2, %2, %2 " ctl "\n\t" \
                "v_add_f32_dpp %3, %3, %3 " ctl "\n\tv_add_f32_dpp %4, %4, %4 " ctl "\n\tv_add_f32_dpp %5, %5, %5 " ctl "\n\t"
  asm volatile("s_nop 1\n\t" S6("quad_perm:[1,0,3,2] row_mask:0xf bank_mask:0xf") S6("quad_perm:[2,3,0,1] row_mask:0xf bank_mask:0xf")
               S6("row_half_mirror row_mask:0xf bank_mask:0xf") "s_nop 0" : "+v"(a), "+v"(b), "+v"(c), "+v"(d), "+v"(e), "+v"(f));
#undef S6
}
DI void red_write(const Ctx& c, int l, int nh, int kh, const f4 (&acc)[4][2]) {
  l = fresh(l);
  const int rb = L_RED + kh * 17408 + (l & 15) * 272 + nh * 128 + (l >> 4) * 16;
#pragma unroll
  for (int r = 0; r < 4; ++r)
#pragma unroll
    for (int t = 0; t < 2; ++t) *(LAS f4*)(c.L + rb + r * 16 * 272 + t * 64) = acc[r][t];
  asm volatile("s_waitcnt lgkmcnt(0)" ::: "memory");
}
DI void red_read(const Ctx& c, int tok, int f8, f2 (&y)[4]) {
  const int ra = L_RED + tok * 272 + f8 * 32;
  const f4 y0 = *(LAS f4*)(c.L + ra) + *(LAS f4*)(c.L + ra + 17408), y1 = *(LAS f4*)(c.L + ra + 16) + *(LAS f4*)(c.L + ra + 17408 + 16);
  y[0] = f2{y0.x, y0.y}; y[1] = f2{y0.z, y0.w}; y[2] = f2{y1.x, y1.y}; y[3] = f2{y1.z, y1.w};
}
DI void ec_read(const Ctx& c, int sec, int f8, f2 (&y)[4]) {
  const f4 y0 = *(LAS f4*)(c.L + L_EC + sec * 256 + f8 * 32), y1 = *(LAS f4*)(c.L + L_EC + sec * 256 + f8 * 32 + 16);
  y[0] = f2{y0.x, y0.y}; y[1] = f2{y0.z, y0.w}; y[2] = f2{y1.x, y1.y}; y[3] = f2{y1.z, y1.w};
}
DI void epi2(const Ctx& c, int t, f2 (&x1)[2][4]) {
  t = fresh(t);
  const int tk = t >> 3, f8 = t & 7;
  f2 ga[4], a2[4], sb[4], p[2][4];
  ec_read(c, 0, f8, ga); ec_read(c, 1, f8, a2); ec_read(c, 2, f8, sb);
#pragma unroll
  for (int k = 0; k < 2; ++k) {
    f2 y[4];
    red_read(c, tk + 32 * k, f8, y);
#pragma unroll
    for (int e = 0; e < 4; ++e) { x1[k][e] = fma2(ga[e], y[e], x1[k][e]); p[k][e] = x1[k][e] * a2[e]; }
    stf(c.rs, v4u{pk2(p[k][0]), pk2(p[k][1]), pk2(p[k][2]), pk2(p[k][3])},
        ((f8 >> 2) * 4 + (tk >> 4) + 2 * k) * 1024 + ((f8 & 3) * 16 + (tk & 15)) * 16, OFF_XA + (c.g * 16 + 2 * c.j) * 4096);
  }
  SB();
#pragma unroll
  for (int k = 0; k < 2; ++k) {
    f2 vx = x1[k][0], vxx = x1[k][0] * x1[k][0], vp = p[k][0], vpp = p[k][0] * p[k][0], vpa = p[k][0] * a2[0], vps = p[k][0] * sb[0];
#pragma unroll
    for (int e = 1; e < 4; ++e) {
      vx += x1[k][e]; vxx = fma2(x1[k][e], x1[k][e], vxx); vp += p[k][e]; vpp = fma2(p[k][e], p[k][e], vpp);
      vpa = fma2(p[k][e], a2[e], vpa); vps = fma2(p[k][e], sb[e], vps);
    }
    float sx = vx.x + vx.y, sxx = vxx.x + vxx.y, sp = vp.x + vp.y, spp = vpp.x + vpp.y, spa = vpa.x + vpa.y, sps = vps.x + vps.y;
    sum8x6(sx, sxx, sp, spp, spa, sps);
    const v4u sw = (__builtin_bit_cast(v4u, f8 == 0 ? f4{sx, sxx, sp, spp} : f4{spa, sps, spa, sps}) & 0xffffff00u) | (c.tag & 0xffu);
    if (f8 < 2) stf(c.rs, sw, (tk + 32 * k) * 16 + f8 * 1024, OFF_STAT + (c.g * 8 + c.j) * 2048);
  }
}
DI void epi4(const Ctx& c, int t, v4u b2v, const f2 (&x1)[2][4]) {
  t = fresh(t);
  const int tk = t >> 3, f8 = t & 7;
  const rsrc_t ro = __builtin_amdgcn_make_buffer_rsrc((void*)c.out_, 0, 2048 * 512 * 2, 0x00020000);
  f2 gb[4];
  ec_read(c, 3, f8, gb);
#pragma unroll
  for (int k = 0; k < 2; ++k) {
    f2 y[4];
    red_read(c, tk + 32 * k, f8, y);
    v4u o;
#pragma unroll
    for (int e = 0; e < 4; ++e) o[e] = pk2(fma2(gb[e], y[e] + unpk(b2v[e]), x1[k][e]));
    __builtin_amdgcn_raw_buffer_store_b128(o, ro, (tk + 32 * k) * 1024 + f8 * 16 + c.g * 65536 + c.j * 128, 0, 0);
  }
}

DI void tail_c(const Ctx& c, bool two, int tid STP) {
  tid = fresh(tid);
  const int l = tid & 63, i = l & 15, w = c.w, g = c.g, j = c.j;
  LAS char* L = c.L;
  const unsigned w3off = OFF_W3T + c.tile0 * 16384;
  v4u w3a[16], w3b[16], w4[44];
  WAITV(0);
  meet4(L, L_W + 32, w, c.tag, l);
  if (tid == 128) __hip_atomic_store(c.gfl + j, c.tag, RLX_AGENT);
  ST(11);
  auto o_slot = [&](int slot) { dma<4>(c.rsv, l * 16, OFF_O + (g * 16 + ((slot + 2 * j) & 15)) * 4096, c.Lb + slot * 4096); };
  if (w < 2) o_slot(w);
  const float b1a = bflo(((const GAS u16*)c.b1_)[c.tile0 * 16 + i]), b1b = bflo(((const GAS u16*)c.b1_)[(c.tile0 + (two ? 1 : 0)) * 16 + i]);
  lds_wait(L, L_W, c.tag);
#pragma unroll
  for (int n = 0; n < 10; ++n) w3a[n] = LDW(n, w3off);
  if (w == 3) poll8(c.gfl, c.tag, l);
  WAITV(0);
  ST(17);
  SBAR();
  ST(8);
  o_slot(2 + w); o_slot(6 + w);
  WAITV_BAR(4); SL(20);
  o_slot(10 + w);
  v4u m1[4], m2[4];
  const unsigned o1 = (1536 + 256 * w + 4 * l) * 4, o2 = (((l & 16) ? 2560 : 1024) + 64 * j + 4 * i) * 4;
  WAITV_BAR(4);
  if (w < 2) o_slot(14 + w);
  mod_get(c.rs, o1, m1);
  mod_get(c.rs, o2, m2);
  if (w < 2) WAITV(12); else WAITV(8);
  SBAR();
  WAITV_BAR(8); SL(21);
  {
    const v2u p = mod_sum(c.rs, o1, m1, c.tag, w < 2 ? 0.f : 1.f);
    const int u = L_UV + (w < 2 ? 2048 : 0) + (w & 1) * 512 + l * 8;
    *(LAS v2u*)(L + u) = p; *(LAS v2u*)(L + u + 1024) = p;
    if ((w & 1) * 4 + (l >> 4) == j) *(LAS f4*)(L + L_EC + (w < 2 ? 512 : 256) + i * 16) = widen(p);
    const v2u p2 = mod_sum(c.rs, o2, m2, c.tag, 0.f);
    if (w == 3 && l < 32) *(LAS f4*)(L + L_EC + ((l & 16) ? 768 : 0) + i * 16) = widen(p2);
    if (w < 2) { const unsigned f = w ? 0u : 0x3f803f80u; *(LAS v4u*)(L + L_UV + 4096 + w * 1024 + l * 16) = v4u{f, f, f, f}; }
    WAITV(0);
    asm volatile("s_waitcnt lgkmcnt(0)" ::: "memory");
    post(L, L_W + 48 + 4 * w, c.tag);
  }
#pragma unroll
  for (int n = 10; n < 16; ++n) w3a[n] = LDW(n, w3off);
  if (two) {
#pragma unroll
    for (int n = 0; n < 16; ++n) w3b[n] = LDW(16 + n, w3off);
  }
  wait4(L, L_W + 48, c.tag, l);
  float S[5] = {0.f, 0.f, 0.f, 0.f, 0.f};
  if (w == 3) {
    const v4u av = *(LAS v4u*)(L + L_UV + l * 16), sv = *(LAS v4u*)(L + L_UV + 2048 + l * 16);
#pragma unroll
    for (int e = 0; e < 4; ++e) {
      const float a0 = bflo(av[e]), a1 = bfhi(av[e]), s0 = bflo(sv[e]), s1 = bfhi(sv[e]);
      S[0] += a0 + a1; S[1] += s0 + s1; S[2] += a0 * a0 + a1 * a1; S[3] += a0 * s0 + a1 * s1; S[4] += s0 * s0 + s1 * s1;
    }
#pragma unroll
    for (int e = 0; e < 5; ++e) S[e] = swap32_sum(swap16_sum(sum16(S[e])));
  }
  WAITV(0);
  ST(9);
  float uvt[2];
  col_terms<2>(c, two, l, w3a, w3b, b1a, b1b, uvt);
  USE(uvt[0]); USE(uvt[1]);
  if (w == 3) {
    poll8(c.gfl + 32 * 32, c.tag, l);
    ST(15);
#pragma unroll
    for (int k = 2; k < 10; ++k) dma<4>(c.rsv, l * 16, OFF_XA + (g * 16 + ((k + 2 * j) & 15)) * 4096, c.Lb + k * 4096);
  }
  SBAR();
  ST(10);
  f4 acc[4][2];
  mlp1_loop<2, 2>(c, l, w3a, w3b, acc, w4, 0, two, S STA);
  BAR();
  ST(12);
  mlp1_epi<2>(c, two, l, uvt, acc);
  ST(18);
  WAITV_BAR(0);
  ST(13);
  const int rot4 = (c.t03 + 1) >> 1;
  auto h_slot = [&](int sg) {
    int k = rot4 + sg; if (k >= 43) k -= 43;
    dma<4>(c.rsv, l * 16, OFF_H1 + (g * 43 + k) * 4096, c.Lb + (sg >= 38 ? sg - 38 : sg) * 4096);
  };
  if (w == 3) poll8(c.gfl + 64 * 32, c.tag, l);
  else { h_slot(w); if (w == 0) h_slot(3); }
  WAITV_BAR(0);
  ST(14);
  h_slot(4 + w); h_slot(8 + w); h_slot(12 + w);
#pragma unroll
  for (int m = 1; m < 10; ++m) {
    if (m == 1 || m == 7) WAITV_BAR(8);
    else if (m < 7) WAITV_BAR(12);
    else if (m == 8) WAITV_BAR(4);
    else WAITV_BAR(0);
    if (m == 1) SL(26);
    if (m == 5) SL(27);
    if (m == 9) SL(28);
    if (m == 1) h_slot(16 + w);
    if (m + 4 < 10 || (m + 4 == 10 && w < 3)) h_slot(4 * (m + 4) + w);
  }
}

extern "C" __global__ void __launch_bounds__(512, 2)
fused_block(const u16* __restrict__ x_, const u16* __restrict__ wqkv_, const u16* __restrict__ wo_, const u16* __restrict__ w1_,
            const u16* __restrict__ b1_, const u16* __restrict__ w2_, const u16* __restrict__ b2_, const u16* __restrict__ c_,
            const u16* __restrict__ wmod_, u16* __restrict__ out_, unsigned char* __restrict__ ws_) {
  extern __shared__ __attribute__((aligned(16))) char smem[];
  asm volatile("" ::"s"(x_), "s"(wqkv_), "s"(wo_), "s"(w1_), "s"(b1_), "s"(w2_), "s"(b2_), "s"(c_), "s"(wmod_), "s"(out_), "s"(ws_));
  unsigned long long pc;
  asm volatile("s_getpc_b64 %0" : "=s"(pc));
  LAS char* L = (LAS char*)smem;
  const unsigned Lb = (unsigned)(uintptr_t)L;
  const int tid0 = threadIdx.x, w = __builtin_amdgcn_readfirstlane(tid0 >> 6);
  const int b = blockIdx.x, xcd = b & 7, idx = b >> 3, g = xcd * 4 + (idx >> 3), j = idx & 7;
  GAS unsigned char* ws = (GAS unsigned char*)ws_;
  const rsrc_t rs = __builtin_amdgcn_make_buffer_rsrc((void*)ws_, 0, WS_USED, 0x00020000);
  const v4u rsv = {(unsigned)(uintptr_t)ws_, (unsigned)((uintptr_t)ws_ >> 32) & 0xffffu, WS_USED, 0x00020000u};
  SRT(32);
  ST(0);
  unsigned epoch = *(const __attribute__((address_space(4))) unsigned*)(uintptr_t)(ws_ + OFF_E);
#define GET_TAG asm volatile("" : "+s"(epoch) : : "memory"); const unsigned tag = epoch + 1u;
  const int rq = xcd >> 1;
  const int nt3 = 11 - (j >= 6), t03 = 11 * j - (j == 7);
  gu32* gfl = (gu32*)(ws + OFF_GFLAG) + g * 32;
  const v4u rsq = {(unsigned)(uintptr_t)wqkv_, (unsigned)((uintptr_t)wqkv_ >> 32) & 0xffffu, 512u * 1536u * 2u, 0x00020000u};
#define WSV(stride) ((8 * (w & 3) + (l >> 3)) * (stride) + 128 * j + 16 * ((l & 7) ^ ((((l >> 4) & 1) | ((w & 1) << 1)) << 1)))
#define LANE_IDS const int l = lane_id(), tid = w * 64 + l, i = l & 15, q = l >> 4; const unsigned w1v = WSV(3072);
  v4u xr[5][4];
  if (w < 4) {
    const int l = lane_id();
    unsigned dummy;
    v2u cw; v4u wm[4];
    if (b < 64) { __builtin_amdgcn_s_setprio(3); mod_loads(c_, wmod_, true, b & 15, b >> 4, w, l, cw, wm); }
    asm volatile("s_load_dword %0, %1, 0x0\n\ts_load_dword %0, %1, 0x40\n\ts_load_dword %0, %1, 0x80\n\t"
                 "s_load_dword %0, %1, 0xc0\n\ts_load_dword %0, %1, 0x100\n\ts_load_dword %0, %1, 0x140"
                 : "=&s"(dummy) : "s"((pc & ~255ull) + idx * CODE_STEP + w * 384));
    if (b < 64) {
      asm volatile("" : "+s"(epoch) : : "memory");
      MOD_WAIT(0);
      mod_publish(rs, b & 15, b >> 4, w, l, mod_job(L, L_MRED, w, l, epoch + 1u, cw, wm));
      __builtin_amdgcn_s_setprio(0);
    }
    ST(29);
    const unsigned w1v = WSV(3072);
#pragma unroll
    for (int s0 = 0; s0 < 9; ++s0) {
      const unsigned t = s0 + 2 * (xcd & 1), s = t < t - 9u ? t : t - 9u, y = L_R1 + s * 12288u, ps = y < y - 143360u ? y : y - 143360u;
      dma3(rsq, w1v, ((s + 4 * rq) & 15) * (32 * 3072), Lb + ps + w * 3072);
    }
    asm volatile("s_waitcnt lgkmcnt(0)" : "+s"(dummy) : : "memory");
    ST(30);
  } else {
    const int l = lane_id();
    __builtin_amdgcn_s_setprio(3);
#pragma unroll
    for (int p = 0; p < 5; ++p) {
      int tok = 64 * g - 16 + 20 * (w & 3) + (l >> 4) + 4 * p;
      tok = tok < 0 ? 0 : tok;
#pragma unroll
      for (int n = 0; n < 4; ++n)
        asm volatile("global_load_dwordx4 %0, %1, %2" : "=v"(xr[p][n]) : "v"(tok * 1024 + 256 * ((n + rq) & 3) + 16 * (l & 15)), "s"(x_));
    }
    ST(29);
    SB();
  }
  if (w < 4) {
    asm volatile("; matrix waves" ::: "memory");
    LANE_IDS
    GET_TAG
    WAITV(0);
    ST(2);
    meet4(L, L_SYNC, w, tag, l);
    ST(3);
    const int tb_ = q * 3072 + (i >> 2) * 128 + ((w ^ ((i >> 3) | ((q & 1) << 1))) * 32) + (i & 3) * 8;
    const int tb1 = fresh(tb_ + L_R1);
    v4u wf[16][3];
#pragma unroll
    for (int s = 0; s < 9; ++s)
#pragma unroll
      for (int sg = 0; sg < 3; ++sg) wf[s][sg] = trfrag(L + (s < 5 ? tb1 + s * 12288 : tb_ + (s - 5) * 12288) + sg * 1024);
    ST(7);
    BAR();
    ST(4);
#pragma unroll
    for (int s = 9; s < 11; ++s) dma3(rsq, w1v, ((s + 4 * rq) & 15) * (32 * 3072), Lb + L_R1 + (s - 9) * 12288 + w * 3072);
    f4 aq[4], ak[5], av[5];
    const int a0 = l * 16 ^ (q << 4), a1 = l * 16 ^ ((q | 4) << 4), a0h = fresh(a0 + 61440), a1h = fresh(a1 + 61440);
    v4u a[2][5];
    SBAR();
    ST(5);
#pragma unroll
    for (int r = 0; r < 5; ++r) a[0][r] = *(LAS v4u*)(L + r * 1024 + a0);
#pragma unroll
    for (int s = 0; s < 16; ++s) {
      if (s == 7) WAITV(0);
      if (s == 3 || s == 7 || s == 10) SBAR();
      if (s == 7) SL(31);
      if (s == 10) SL(34);
      SB();
#pragma unroll
      for (int r = 0; r < 5; ++r) {
        MAC(s == 0, ak[r], wf[s][1], a[s & 1][r]);
        SB();
        if (s < 11) a[(s + 1) & 1][r] = *(LAS v4u*)(L + ((s + 1) * 5 + r) * 1024 + (((s + 1) & 1) ? a1 : a0));
        else if (s < 15) a[(s + 1) & 1][r] = *(LAS v4u*)(L + ((s - 11) * 5 + r) * 1024 + (((s + 1) & 1) ? a1h : a0h));
        SB();
        MAC(s == 0, av[r], a[s & 1][r], wf[s][2]);
        SB();
        if (s >= 8 && s < 15 && r < 3) { wf[s + 1][r] = trfrag(L + (s < 13 ? tb1 + (s - 8) * 12288 : tb_ + (s - 13) * 12288) + r * 1024); SB(); }
        if (r) { MAC(s == 0, aq[r - 1], wf[s][0], a[s & 1][r]); SB(); }
      }
    }
    MFMA_DONE();
    post(L, L_W + 16 + 4 * w, tag);
    ST(6);
    {
      v2u qb[4], kb[5], vb[5];
#pragma unroll
      for (int r = 0; r < 5; ++r) {
        kb[r] = v2u{pk(ak[r].x, ak[r].y), pk(ak[r].z, ak[r].w)};
        vb[r] = v2u{pk(av[r].x, av[r].y), pk(av[r].z, av[r].w)};
        if (r) qb[r - 1] = v2u{pk(aq[r - 1].x, aq[r - 1].y), pk(aq[r - 1].z, aq[r - 1].w)};
      }
      const bool odd = i & 1;
      const bool cur0 = 4 * q + (i & 1) <= i, cur1 = 4 * q + 2 + (i & 1) <= i;
      const float sc = 0.25f * 1.4426950409f;
      const int hd = 4 * j + w;
      unsigned h[4][2];
#pragma unroll
      for (int r = 0; r < 4; ++r) {
        const f4 z = {0.f, 0.f, 0.f, 0.f};
        f4 sp = mfma16(kb[r], qb[r], z), scu = mfma16(kb[r + 1], qb[r], z);
        float p0 = odd ? sp.y : sp.x, p1 = odd ? sp.w : sp.z, c0 = odd ? scu.y : scu.x, c1 = odd ? scu.w : scu.z;
        if (r == 0 && g == 0) { p0 = -4e9f; p1 = -4e9f; }
        float v0 = cur0 ? c0 : p0, v1 = cur1 ? c1 : p1;
        const float mx = swap32_max(swap16_max(fmaxf(v0, v1)));
        v0 = __builtin_amdgcn_exp2f((v0 - mx) * sc); v1 = __builtin_amdgcn_exp2f((v1 - mx) * sc);
        const float rs_ = __builtin_amdgcn_rcpf(swap32_sum(swap16_sum(v0 + v1)));
        const unsigned pp = pk(v0 * rs_, v1 * rs_);
        const unsigned d0 = odd ? pp << 16 : pp & 0xffffu, d1 = odd ? pp & 0xffff0000u : pp >> 16;
        const v4u pf = {cur0 ? 0u : d0, cur1 ? 0u : d1, cur0 ? d0 : 0u, cur1 ? d1 : 0u};
        const f4 o = mfma32(v4u{vb[r].x, vb[r].y, vb[r + 1].x, vb[r + 1].y}, pf, z);
        h[r][0] = pk(o.x, o.y); h[r][1] = pk(o.z, o.w);
      }
#pragma unroll
      for (int pp = 0; pp < 2; ++pp) {
        auto s0 = __builtin_amdgcn_permlane16_swap(h[2 * pp][0], h[2 * pp + 1][0], false, false);
        auto s1 = __builtin_amdgcn_permlane16_swap(h[2 * pp][1], h[2 * pp + 1][1], false, false);
        stf(rs, v4u{s0[0], s1[0], s0[1], s1[1]}, (q & 1) * 1024 + ((q >> 1) * 16 + i) * 16,
            OFF_O + ((g * 16 + (hd >> 1)) * 4 + 2 * pp) * 1024 + (hd & 1) * 512);
      }
    }
    ST(19);
    const bool two = w < nt3 - 8;
    const Ctx c = {rs, rsv, L, Lb, tag, g, j, w, t03, t03 + (two ? 2 * w : nt3 - 8 + w), gfl, x_, b1_, b2_, out_};
    tail_c(c, two, tid STA);
    ST(16);
    SLF();
    __builtin_amdgcn_endpgm();
  } else {
    asm volatile("; helper waves" ::: "memory");
    LANE_IDS
    const int v = w - 4;
    const int row0 = 20 * v + q;
    GET_TAG
    ST(1);
    const int u = 4 * (b < 64 ? b : b >= 192 ? b - 128 : b + 64) + v;
    const bool retile = u < 696, isw1 = u < 352, job2 = b >= 64 && b < 192;
    const int rcb = isw1 ? u >> 4 : (int)((unsigned)(u - 352) / 43u), rks = isw1 ? (u & 15) : u - 352 - 43 * rcb;
    const int rnt = (isw1 && rcb == 21) ? 2 : 4, rst = L_RT + v * 4096;
    v2u cw; v4u wm[4];
    {
      const int nc = isw1 ? 2752 : 1024;
      const v4u rr = mkrs(isw1 ? w1_ : w2_, retile ? 1376u * 1024u : 0u);
      const unsigned vo = (32 * rks + (l >> 3)) * nc + 128 * rcb + 16 * (rnt == 2 ? (l & 3) : (l & 7));
#pragma unroll
      for (int e = 0; e < 4; ++e) dma_nt(rr, vo, 8 * e * nc, Lb + rst + e * 1024);
      SB();
    }
    f2 xn[5][16];
    v4u mv[4];
#pragma unroll
    for (int p = 0; p < 5; ++p) {
      f2 s2 = {0.f, 0.f}, q2 = {0.f, 0.f};
      asm volatile("s_waitcnt vmcnt(%4)" : "+v"(xr[p][0]), "+v"(xr[p][1]), "+v"(xr[p][2]), "+v"(xr[p][3]) : "i"(20 - 4 * p) : "memory");
#pragma unroll
      for (int e = 0; e < 16; ++e) {
        xn[p][e] = unpk(xr[p][e >> 2][e & 3]);
        s2 += xn[p][e];
        q2 = fma2(xn[p][e], xn[p][e], q2);
      }
      const float m = sum16(s2.x + s2.y) * (1.0f / 512.0f);
      const float r = __builtin_amdgcn_rsqf(fmaxf(sum16(q2.x + q2.y) * (1.0f / 512.0f) - m * m, 0.f) + 1e-5f);
      const f2 r2 = {r, r}, mr2 = {-m * r, -m * r};
#pragma unroll
      for (int e = 0; e < 16; ++e) { xn[p][e] = fma2(xn[p][e], r2, mr2); asm volatile("" : "+v"(xn[p][e])); }
    }
    int ha[5];
#pragma unroll
    for (int p = 0; p < 5; ++p) {
      const int r = row0 + 4 * p, qq = i & 3;
      ha[p] = ((i >> 2) * 5 + (r >> 4)) * 1024 + (qq * 16 + ((r & 15) ^ (qq | (i & 4)))) * 16;
      asm volatile("" : "+v"(ha[p]));
    }
    ST(2);
    mod_get(rs, (256 * v + 4 * l) * 4, mv);
    SB();
    WAITV(4);
    v4u rt[4];
    if (retile) {
#pragma unroll
      for (int t = 0; t < 4; ++t) rt[t] = trfrag(L + rst + (8 * q + (i >> 2)) * 128 + t * 32 + (i & 3) * 8);
    }
    *(LAS f4*)(L + L_MODF + (256 * v + 4 * l) * 4) = widen(mod_sum(rs, (256 * v + 4 * l) * 4, mv, tag, v < 2 ? 0.f : 1.f));
    WAITV(0);
    asm volatile("s_waitcnt lgkmcnt(0)" ::: "memory");
    ST(7);
    post(L, L_SYNC + 16 + 4 * v, tag);
    if (retile) {
      const int rcv = rcb, rkv = rks;
#pragma unroll
      for (int t = 0; t < 4; ++t)
        if (t < rnt) {
          const int tl = 4 * rcv + t;
          const int j3 = tl < 66 ? (int)((unsigned)tl / 11u) : (tl < 76 ? 6 : 7);
          int s4 = rkv - (rcv < 6 ? (11 * rcv + 1) >> 1 : 3 + 5 * rcv); if (s4 < 0) s4 += 43;
          const int dst = isw1 ? OFF_W3T + (tl * 16 + ((rkv - 2 * j3) & 15)) * 1024
                               : OFF_W4T + ((((rcv * 2 + (t >> 1)) * 2 + (s4 & 1)) * 22 + (s4 >> 1)) * 2 + (t & 1)) * 1024;
          stf(rs, rt[t], l * 16, dst);
        }
    }
    wait4(L, L_SYNC + 16, tag, l);
    __builtin_amdgcn_s_setprio(0);
    auto quarter = [&](int n, v4u (&o)[5], bool packed) {
      const int ch = L_MODF + (16 * ((n + rq) & 3) + i) * 32;
      const f4 sa0 = *(LAS f4*)(L + ch), sa1 = *(LAS f4*)(L + ch + 16), ca0 = *(LAS f4*)(L + ch + 2048), ca1 = *(LAS f4*)(L + ch + 2064);
      const f2 sa2[4] = {{sa0.x, sa0.y}, {sa0.z, sa0.w}, {sa1.x, sa1.y}, {sa1.z, sa1.w}}, ca2[4] = {{ca0.x, ca0.y}, {ca0.z, ca0.w}, {ca1.x, ca1.y}, {ca1.z, ca1.w}};
#pragma unroll
      for (int p = 0; p < 5; ++p) {
#pragma unroll
        for (int e = 0; e < 4; ++e)
          o[p][e] = packed ? pk2(fma2(xn[p][4 * n + e], ca2[e], sa2[e]))
                           : pk(fma1(xn[p][4 * n + e].x, ca2[e].x, sa2[e].x), fma1(xn[p][4 * n + e].y, ca2[e].y, sa2[e].y));
        asm volatile("" : "+v"(o[p]));
      }
    };
    v4u h0[5], h1[5];
    quarter(0, h0, true); quarter(1, h1, true);
    ST(3);
    WAITV_BAR(0);
    if (tid == 320) __hip_atomic_store((gu32*)(ws + OFF_RFLAG) + 32 * g + j, tag, RLX_AGENT);
    ST(4);
#pragma unroll
    for (int p = 0; p < 5; ++p) *(LAS v4u*)(L + ha[p]) = h0[p];
    BAR();
    ST(5);
#pragma unroll
    for (int p = 0; p < 5; ++p) *(LAS v4u*)(L + ha[p] + 20480) = h1[p];
    gdma4((const GAS char*)wo_ + (idx * 256 + v * 64 + l) * 64, Lb + L_EC + 1024 + v * 256);
    quarter(2, h0, false);
    BAR();
#pragma unroll
    for (int p = 0; p < 5; ++p) *(LAS v4u*)(L + ha[p] + 40960) = h0[p];
#pragma unroll
    for (int s = 11; s < 14; ++s) dma3(rsq, w1v, ((s + 4 * rq) & 15) * (32 * 3072), Lb + L_R1 + (s - 9) * 12288 + v * 3072);
    BAR();
    dma3(rsq, w1v, ((14 + 4 * rq) & 15) * (32 * 3072), Lb + v * 3072);
    dma3(rsq, w1v, ((15 + 4 * rq) & 15) * (32 * 3072), Lb + 12288 + v * 3072);
    v4u rf;
    asm volatile("buffer_load_dwordx4 %0, %1, %2, %3 offen sc1" : "=v"(rf) : "v"(v == 0 ? (l >> 1) * 128 + (l & 1) * 16 : 0x10000000), "s"(rsv), "s"(OFF_RFLAG));
    mod_loads(c_, wmod_, job2, 16 + ((b - 64) & 31), ((b - 64) >> 5) & 3, v, fresh(l), cw, wm);
    SB();
    quarter(3, h1, false);
#pragma unroll
    for (int p = 0; p < 5; ++p) *(LAS v4u*)(L + ha[p] + 61440) = h1[p];
    ST(34);
    WAITV_BAR(5);
    asm volatile("" : "+v"(rf));
    ST(6);
    if (w == 4) {
      _Pragma("nounroll") for (unsigned s = 0; s < SPIN_CAP; ++s) {
        if (__all(rf.x == tag && rf.y == tag && rf.z == tag && rf.w == tag)) break;
        __builtin_amdgcn_s_sleep(8);
        asm volatile("" ::: "memory");
        rf = ld_rflags(rs, l);
      }
      post(L, L_W, tag);
      if (b == 0 && l == 0) *(GAS unsigned*)(ws + OFF_E) = tag;
    } else lds_wait(L, L_W, tag);
    __builtin_amdgcn_fence(__ATOMIC_ACQUIRE, "wavefront");
    asm volatile("" ::: "memory");
    ST(35);
    const Ctx c = {rs, rsv, L, Lb, tag, g, j, w, t03, t03 + nt3 - 8 + w, gfl, x_, b1_, b2_, out_};
    const int nh = v & 1, kh = v >> 1;
    v4u w2[16], w3a[16], w3b[16], w4[44];
    const unsigned w3off = OFF_W3T + c.tile0 * 16384, w4off = OFF_W4T + ((j * 2 + nh) * 2 + kh) * 44 * 1024;
#pragma unroll
    for (int n = 0; n < 16; ++n) w3a[n] = LDW(n, w3off);
    MOD_WAIT(16);
    if (job2) mod_publish(rs, 16 + ((b - 64) & 31), (b - 64) >> 5, v, l, mod_job(L, L_MRED2, v, fresh(l), tag, cw, wm));
    ST(36);
    wait4(L, L_W + 16, tag, l);
    {
      const v4u rso = {(unsigned)(uintptr_t)wo_, (unsigned)((uintptr_t)wo_ >> 32) & 0xffffu, 512u * 512u * 2u, 0x00020000u};
      const unsigned w2v = WSV(1024);
#pragma unroll
      for (int s = 0; s < 16; ++s) dma_in(rso, w2v, ((s + 2 * j) & 15) * 32768, Lb + L_R1 + s * 4096 + v * 1024);
    }
    v4u xq[2];
    {
      const rsrc_t rx = __builtin_amdgcn_make_buffer_rsrc((void*)x_, 0, 2048 * 512 * 2, 0x00020000);
      const int t = tid - 256;
#pragma unroll
      for (int k = 0; k < 2; ++k) xq[k] = __builtin_amdgcn_raw_buffer_load_b128(rx, ((t >> 3) + 32 * k) * 1024 + (t & 7) * 16, g * 65536 + j * 128, 0);
    }
    const float b1a = bflo(((const GAS u16*)b1_)[c.tile0 * 16 + i]);
    ST(31);
    WAITV(0);
    meet4(L, L_W + 64, v, tag, l);
    {
      const int tb2 = fresh(L_R1 + kh * 4096 + (8 * q + (i >> 2)) * 128 + (i & 3) * 8), f = (i >> 3) | ((q & 1) << 1);
      const int t0 = tb2 + ((2 * nh) ^ f) * 32, t1 = tb2 + ((2 * nh + 1) ^ f) * 32;
#pragma unroll
      for (int n = 0; n < 8; ++n) { w2[2 * n] = trfrag(L + t0 + n * 8192); w2[2 * n + 1] = trfrag(L + t1 + n * 8192); }
    }
    f2 x1[2][4];
#pragma unroll
    for (int k = 0; k < 2; ++k)
#pragma unroll
      for (int e = 0; e < 4; ++e) { x1[k][e] = unpk(xq[k][e]); asm volatile("" : "+v"(x1[k][e])); }
    ST(19);
    BAR();
    ST(8);
    {
      f4 acc[4][2];
      v4u a2[2][4];
      const int ab = kh * 4096 + l * 16;
#pragma unroll
      for (int r = 0; r < 4; ++r) a2[0][r] = *(LAS v4u*)(L + ab + r * 1024);
#pragma unroll
      for (int n = 0; n < 8; ++n) {
        if (!(n & 1)) SBAR();
        if (n == 0) SL(20);
        SB();
#pragma unroll
        for (int r = 0; r < 4; ++r) {
          MAC(n == 0, acc[r][0], w2[2 * n], a2[n & 1][r]);
          SB();
          if (n < 7) a2[(n + 1) & 1][r] = *(LAS v4u*)(L + ab + ((n + 1) * 8 + r) * 1024);
          SB();
          MAC(n == 0, acc[r][1], w2[2 * n + 1], a2[n & 1][r]);
          SB();
        }
      }
      MFMA_DONE();
      SL(21);
      red_write(c, l, nh, kh, acc);
    }
    meet4(L, L_W + 80, v, tag, l);
    wait4(L, L_W + 48, tag, l);
    __builtin_amdgcn_s_setprio(1);
    epi2(c, tid - 256, x1);
    __builtin_amdgcn_s_setprio(0);
    ST(17);
    WAITV(2);
    meet4(L, L_W + 96, v, tag, l);
    if (tid == 384) __hip_atomic_store(gfl + 32 * 32 + j, tag, RLX_AGENT);
    ST(9);
    if (v < 2) dma<4>(rsv, l * 16, OFF_XA + (g * 16 + 2 * j + v) * 4096, Lb + v * 4096);
    float uvt[1];
    col_terms<1>(c, false, l, w3a, w3b, b1a, b1a, uvt);
    USE(uvt[0]);
    if (v < 2) WAITV(0);
    SBAR();
    ST(10);
    {
      f4 acc[4][1];
      const float S[5] = {0.f, 0.f, 0.f, 0.f, 0.f};
      mlp1_loop<1, 1>(c, l, w3a, w3b, acc, w4, w4off, false, S STA);
      BAR();
      ST(12);
      mlp1_epi<1>(c, false, l, uvt, acc);
    }
    asm volatile("" ::: "memory");
    const rsrc_t rb2 = __builtin_amdgcn_make_buffer_rsrc((void*)b2_, 0, 1024, 0x00020000);
    const v4u b2v = __builtin_amdgcn_raw_buffer_load_b128(rb2, (l & 7) * 16, j * 128, 0);
    ST(18);
    WAITV_BAR(1);
    if (tid == 256) __hip_atomic_store(gfl + 64 * 32 + j, tag, RLX_AGENT);
    ST(13);
    {
      f4 acc[4][2];
      v4u a4[2][4];
      const int ab = kh * 4096 + l * 16, abh = fresh(ab + 65536), abx = fresh(ab + 131072);
      SBAR();
      ST(14);
#pragma unroll
      for (int r = 0; r < 4; ++r) a4[0][r] = *(LAS v4u*)(L + ab + r * 1024);
#pragma unroll
      for (int n = 0; n < 22; ++n) {
        if ((n & 1) && n < 19) SBAR();
        if (n == 1) SL(26);
        if (n == 9) SL(27);
        if (n == 17) SL(28);
        SB();
        const int sp = 2 * (n + 1) >= 38 ? 2 * (n + 1) - 38 : 2 * (n + 1);
        if (n < 21 || kh == 0) {
#pragma unroll
          for (int r = 0; r < 4; ++r) {
            MAC(n == 0, acc[r][0], w4[2 * n], a4[n & 1][r]);
            SB();
            if (n < 21) a4[(n + 1) & 1][r] = *(LAS v4u*)(L + (sp < 16 ? ab : sp < 32 ? abh : abx) + (sp & 15) * 4096 + r * 1024);
            SB();
            MAC(n == 0, acc[r][1], w4[2 * n + 1], a4[n & 1][r]);
            SB();
          }
        }
      }
      MFMA_DONE();
      ST(15);
      red_write(c, l, nh, kh, acc);
    }
    meet4(L, L_W + 112, v, tag, l);
    epi4(c, tid - 256, b2v, x1);
    ST(16);
    SLF();
    __builtin_amdgcn_endpgm();
  }
}

extern "C" void kernel_launch(void* const* d_in, const int* in_sizes, int n_in, void* d_out, int out_size, void* d_ws, size_t ws_size,
                              hipStream_t stream) {
  (void)hipFuncSetAttribute((const void*)fused_block, hipFuncAttributeMaxDynamicSharedMemorySize, LDS_BYTES);
  fused_block<<<256, 512, LDS_BYTES, stream>>>((const u16*)d_in[0], (const u16*)d_in[1], (const u16*)d_in[2], (const u16*)d_in[4],
                                               (const u16*)d_in[5], (const u16*)d_in[6], (const u16*)d_in[7], (const u16*)d_in[8],
                                               (const u16*)d_in[9], (u16*)d_out, (unsigned char*)d_ws);
}
```

```cpp
#include <hip/hip_runtime.h>
#include <stdint.h>

#define GAS __attribute__((address_space(1)))
#define LAS __attribute__((address_space(3)))
#define DI __device__ __forceinline__
typedef unsigned short u16;
typedef unsigned v4u __attribute__((ext_vector_type(4)));
typedef unsigned v2u __attribute__((ext_vector_type(2)));
typedef float f4 __attribute__((ext_vector_type(4)));
typedef float f2 __attribute__((ext_vector_type(2)));
typedef __bf16 bf2 __attribute__((ext_vector_type(2)));
typedef __bf16 bf8 __attribute__((ext_vector_type(8)));
typedef short s4 __attribute__((ext_vector_type(4)));
typedef GAS unsigned gu32;
typedef GAS unsigned long long gu64;
#define RLX_AGENT __ATOMIC_RELAXED, __HIP_MEMORY_SCOPE_AGENT

constexpr unsigned OFF_E = 0;
constexpr unsigned OFF_RFLAG = 768u << 10;
constexpr unsigned OFF_GFLAG = 800u << 10;
constexpr unsigned OFF_MODP = 16384;
constexpr unsigned OFF_STAT = 65536;
constexpr unsigned OFF_W3T = 1u << 20;
constexpr unsigned OFF_W4T = OFF_W3T + 1376 * 1024;
constexpr unsigned OFF_O = OFF_W4T + 1408 * 1024;
constexpr unsigned OFF_XA = OFF_O + 2048 * 1024;
constexpr unsigned OFF_H1 = OFF_XA + 2048 * 1024;
constexpr unsigned WS_USED = OFF_H1 + 32 * 43 * 4096;
constexpr unsigned OFF_STAMP = 64u << 20;

constexpr int L_R1 = 81920;
constexpr int L_RED = 65536;
constexpr int L_STAT = 102400;
constexpr int L_UV = 147456;
constexpr int L_RT = 143360;
constexpr int L_MODF = 155648;
constexpr int L_EC = 159744;
constexpr int L_MRED = 77824;
constexpr int L_SYNC = 79872;
constexpr int L_COEF = L_MODF;
constexpr int L_RR = L_COEF + 1024;
constexpr int L_W = L_MODF + 3584;
constexpr int LDS_BYTES = 163840;

constexpr int CODE_STEP = 1216;
constexpr int FSTR = 32, GSEAM = 32 * 8 * 32;
constexpr unsigned SPIN_CAP = 1u << 18;

#define ST(n)
#define SL(n)
#define SRT(n)
#define SLF()

DI float bflo(unsigned u) { return __uint_as_float(u << 16); }
DI float bfhi(unsigned u) { return __uint_as_float(u & 0xffff0000u); }
DI unsigned pk(float a, float b) { f2 v = {a, b}; return __builtin_bit_cast(unsigned, __builtin_convertvector(v, bf2)); }
DI f2 unpk(unsigned u) { return f2{bflo(u), bfhi(u)}; }
DI f2 fma2(f2 a, f2 b, f2 c) { return __builtin_elementwise_fma(a, b, c); }
DI unsigned pk2(f2 v) { return __builtin_bit_cast(unsigned, __builtin_convertvector(v, bf2)); }
DI f4 mfma32(v4u a, v4u b, f4 c) { return __builtin_amdgcn_mfma_f32_16x16x32_bf16(__builtin_bit_cast(bf8, a), __builtin_bit_cast(bf8, b), c, 0, 0, 0); }
template <bool FIRST> DI void mac(f4& c, v4u a, v4u b) {
  if constexpr (FIRST) asm volatile("v_mfma_f32_16x16x32_bf16 %0, %1, %2, 0" : "=&v"(c) : "v"(a), "v"(b));
  else asm volatile("v_mfma_f32_16x16x32_bf16 %0, %1, %2, %0" : "+v"(c) : "v"(a), "v"(b));
}
#define MAC(first, c, a, b) do { if (first) mac<true>(c, a, b); else mac<false>(c, a, b); } while (0)
#define MFMA_DONE() do { asm volatile("s_nop 15" ::: "memory"); __builtin_amdgcn_sched_barrier(0); } while (0)
DI f4 mfma16(v2u a, v2u b, f4 c) { return __builtin_amdgcn_mfma_f32_16x16x16bf16_1k(__builtin_bit_cast(s4, a), __builtin_bit_cast(s4, b), c, 0, 0, 0); }
template <int CTRL> DI float dpp(float x) { return __builtin_bit_cast(float, __builtin_amdgcn_update_dpp(0, __builtin_bit_cast(int, x), CTRL, 0xf, 0xf, true)); }
DI float swap16_sum(float x) { auto r = __builtin_amdgcn_permlane16_swap(__float_as_uint(x), __float_as_uint(x), false, false); return __uint_as_float(r[0]) + __uint_as_float(r[1]); }
DI float swap32_sum(float x) { auto r = __builtin_amdgcn_permlane32_swap(__float_as_uint(x), __float_as_uint(x), false, false); return __uint_as_float(r[0]) + __uint_as_float(r[1]); }
DI float swap16_max(float x) { auto r = __builtin_amdgcn_permlane16_swap(__float_as_uint(x), __float_as_uint(x), false, false); return fmaxf(__uint_as_float(r[0]), __uint_as_float(r[1])); }
DI float swap32_max(float x) { auto r = __builtin_amdgcn_permlane32_swap(__float_as_uint(x), __float_as_uint(x), false, false); return fmaxf(__uint_as_float(r[0]), __uint_as_float(r[1])); }
DI float max2(float a, float b) { return __builtin_amdgcn_fmed3f(a, b, __builtin_inff()); }
template <bool MAX> DI float gather4(const float (&x)[4]) {
  auto a = __builtin_amdgcn_permlane16_swap(__float_as_uint(x[0]), __float_as_uint(x[1]), false, false);
  auto b = __builtin_amdgcn_permlane16_swap(__float_as_uint(x[2]), __float_as_uint(x[3]), false, false);
  const float u = MAX ? max2(__uint_as_float(a[0]), __uint_as_float(a[1])) : __uint_as_float(a[0]) + __uint_as_float(a[1]);
  const float v = MAX ? max2(__uint_as_float(b[0]), __uint_as_float(b[1])) : __uint_as_float(b[0]) + __uint_as_float(b[1]);
  auto c = __builtin_amdgcn_permlane32_swap(__float_as_uint(u), __float_as_uint(v), false, false);
  return MAX ? max2(__uint_as_float(c[0]), __uint_as_float(c[1])) : __uint_as_float(c[0]) + __uint_as_float(c[1]);
}
DI void spread4(float t, float (&x)[4]) {
  auto c = __builtin_amdgcn_permlane32_swap(__float_as_uint(t), __float_as_uint(t), false, false);
  auto a = __builtin_amdgcn_permlane16_swap(c[0], c[0], false, false);
  auto b = __builtin_amdgcn_permlane16_swap(c[1], c[1], false, false);
  x[0] = __uint_as_float(a[0]); x[1] = __uint_as_float(a[1]); x[2] = __uint_as_float(b[0]); x[3] = __uint_as_float(b[1]);
}
DI float sum8(float x) { x += dpp<0xB1>(x); x += dpp<0x4E>(x); x += dpp<0x141>(x); return x; }
DI float sum16(float x) { x = sum8(x); x += dpp<0x140>(x); return x; }

#define BAR() asm volatile("s_waitcnt lgkmcnt(0)\n\ts_barrier" ::: "memory")
#define WAITV(n) do { asm volatile("s_waitcnt vmcnt(%0)" ::"i"(n) : "memory"); if ((n) == 0) __builtin_amdgcn_s_waitcnt(0x0F70); } while (0)
#define WAITV_BAR(n) do { asm volatile("s_waitcnt vmcnt(%0) lgkmcnt(0)\n\ts_barrier" ::"i"(n) : "memory"); if ((n) == 0) __builtin_amdgcn_s_waitcnt(0x0F70); } while (0)
#define SBAR() asm volatile("s_barrier" ::: "memory")
#define USE(x) asm volatile("" ::"v"(x))

template <int N> DI void dma(v4u rs, unsigned voff, unsigned soff, unsigned lds) {
  unsigned keep;
  if constexpr (N == 1)
    asm volatile("s_mov_b32 %0, m0\n\ts_mov_b32 m0, %2\n\ts_nop 0\n\t"
                 "buffer_load_dwordx4 %1, %3, %4 offen sc1 lds\n\t"
                 "s_mov_b32 m0, %0" : "=&s"(keep) : "v"(voff), "s"(lds), "s"(rs), "s"(soff) : "memory");
  else if constexpr (N == 2)
    asm volatile("s_mov_b32 %0, m0\n\ts_mov_b32 m0, %2\n\ts_nop 0\n\t"
                 "buffer_load_dwordx4 %1, %3, %4 offen sc1 lds\n\t"
                 "buffer_load_dwordx4 %1, %3, %4 offen offset:1024 sc1 lds\n\t"
                 "s_mov_b32 m0, %0" : "=&s"(keep) : "v"(voff), "s"(lds), "s"(rs), "s"(soff) : "memory");
  else
    asm volatile("s_mov_b32 %0, m0\n\ts_mov_b32 m0, %2\n\ts_nop 0\n\t"
                 "buffer_load_dwordx4 %1, %3, %4 offen sc1 lds\n\t"
                 "buffer_load_dwordx4 %1, %3, %4 offen offset:1024 sc1 lds\n\t"
                 "buffer_load_dwordx4 %1, %3, %4 offen offset:2048 sc1 lds\n\t"
                 "buffer_load_dwordx4 %1, %3, %4 offen offset:3072 sc1 lds\n\t"
                 "s_mov_b32 m0, %0" : "=&s"(keep) : "v"(voff), "s"(lds), "s"(rs), "s"(soff) : "memory");
}
DI void dma3(v4u rs, unsigned voff, unsigned soff, unsigned lds) {
  unsigned keep;
  asm volatile("s_mov_b32 %0, m0\n\ts_mov_b32 m0, %2\n\ts_nop 0\n\t"
               "buffer_load_dwordx4 %1, %3, %4 offen lds\n\t"
               "buffer_load_dwordx4 %1, %3, %4 offen offset:1024 lds\n\t"
               "buffer_load_dwordx4 %1, %3, %4 offen offset:2048 lds\n\t"
               "s_mov_b32 m0, %0" : "=&s"(keep) : "v"(voff), "s"(lds), "s"(rs), "s"(soff) : "memory");
}
DI v4u trfrag(LAS char* p) {
  v2u lo = __builtin_bit_cast(v2u, __builtin_amdgcn_ds_read_tr16_b64_v4i16((LAS s4*)p));
  v2u hi = __builtin_bit_cast(v2u, __builtin_amdgcn_ds_read_tr16_b64_v4i16((LAS s4*)(p + 512)));
  return v4u{lo.x, lo.y, hi.x, hi.y};
}
DI int lane_id() { int r; asm volatile("v_mbcnt_lo_u32_b32 %0, -1, 0\n\tv_mbcnt_hi_u32_b32 %0, -1, %0" : "=v"(r)); return r; }
DI int fresh(int x) { asm volatile("" : "+v"(x)); return x; }
#define SB() __builtin_amdgcn_sched_barrier(0)
DI void dma_nt(v4u rs, unsigned voff, unsigned soff, unsigned lds) {
  unsigned keep;
  asm volatile("s_mov_b32 %0, m0\n\ts_mov_b32 m0, %2\n\ts_nop 0\n\tbuffer_load_dwordx4 %1, %3, %4 offen nt lds\n\ts_mov_b32 m0, %0"
               : "=&s"(keep) : "v"(voff), "s"(lds), "s"(rs), "s"(soff) : "memory");
}
DI void gdma4(const GAS void* p, unsigned lds) {
  unsigned keep;
  asm volatile("s_mov_b32 %0, m0\n\ts_mov_b32 m0, %2\n\ts_nop 0\n\tglobal_load_lds_dword %1, off\n\ts_mov_b32 m0, %0"
               : "=&s"(keep) : "v"(p), "s"(lds) : "memory");
}
typedef __amdgpu_buffer_rsrc_t rsrc_t;
DI v4u ldf(rsrc_t rs, unsigned voff, unsigned soff) { return __builtin_amdgcn_raw_buffer_load_b128(rs, voff, soff, 16); }
#define LDW(n, base) ldw_rt(c.rs, l * 16, base, n)
DI v4u ldw_rt(rsrc_t rs, unsigned voff, unsigned base, int n) { return __builtin_amdgcn_raw_buffer_load_b128(rs, voff + (n & 3) * 1024, base + (n >> 2) * 4096, 16); }
DI v4u ld_rflags(rsrc_t rs, int l) { return ldf(rs, (l >> 1) * 128 + (l & 1) * 16, OFF_RFLAG); }
DI void stf(rsrc_t rs, v4u v, unsigned voff, unsigned soff) { __builtin_amdgcn_raw_buffer_store_b128(v, rs, voff + soff, 0, 16); }

DI void meet4(LAS char* L, int at, int me, unsigned tag, int l) {
  volatile LAS unsigned* f = (volatile LAS unsigned*)(L + at);
  f[me] = tag;
  _Pragma("nounroll") for (unsigned s = 0; s < 16 * SPIN_CAP; ++s)
    if (__all(f[l & 3] == tag)) break;
  asm volatile("" ::: "memory");
}
DI void post(LAS char* L, int at, unsigned tag) { *(volatile LAS unsigned*)(L + at) = tag; }
DI void wait4(LAS char* L, int at, unsigned tag, int l) {
  volatile LAS unsigned* f = (volatile LAS unsigned*)(L + at);
  _Pragma("nounroll") for (unsigned s = 0; s < 16 * SPIN_CAP; ++s) {
    if (__all(f[l & 3] == tag)) break;
    __builtin_amdgcn_s_sleep(1);
  }
  asm volatile("" ::: "memory");
}
DI void lds_wait(LAS char* L, int at, unsigned tag) {
  volatile LAS unsigned* f = (volatile LAS unsigned*)(L + at);
  _Pragma("nounroll") for (unsigned s = 0; s < 16 * SPIN_CAP; ++s) {
    if (*f == tag) break;
    __builtin_amdgcn_s_sleep(1);
  }
  asm volatile("" ::: "memory");
}
template <int DELAY> DI void poll8(gu32* fl, unsigned tag, int l) {
  __builtin_amdgcn_s_sleep(DELAY);
  _Pragma("nounroll") for (unsigned s = 0; s < SPIN_CAP; ++s) {
    unsigned f = __hip_atomic_load(fl + (l & 7) * FSTR, RLX_AGENT);
    if (__all(f == tag)) break;
    __builtin_amdgcn_s_sleep(4);
  }
  __builtin_amdgcn_fence(__ATOMIC_ACQUIRE, "wavefront");
}

DI unsigned gelu2(f2 a, f2 k1, f2 k2, f2 ir) {
  const f2 t = a * fma2(a * a, k2, k1);
  const f2 d = fma2(f2{__builtin_amdgcn_exp2f(t.x), __builtin_amdgcn_exp2f(t.y)}, ir, ir);
  return pk2(a * f2{__builtin_amdgcn_rcpf(d.x), __builtin_amdgcn_rcpf(d.y)});
}

DI void mod_get(rsrc_t rs, unsigned voff, v4u (&v)[4]) {
#pragma unroll
  for (int k = 0; k < 4; ++k) v[k] = ldf(rs, voff, OFF_MODP + k * 12288);
}
DI v2u mod_sum(rsrc_t rs, unsigned voff, v4u (&v)[4], unsigned tag, float plus) {
  const unsigned tb = tag & 0xffu;
  _Pragma("nounroll") for (unsigned s = 0; s < SPIN_CAP; ++s) {
#pragma unroll
    for (int k = 0; k < 4; ++k) v[k] ^= tb;
    const v4u d = (v[0] | v[1]) | (v[2] | v[3]);
    if (__all(((d.x | d.y) | (d.z | d.w)) << 24 == 0u)) break;
    __builtin_amdgcn_s_sleep(1);
    asm volatile("" ::: "memory");
    mod_get(rs, voff, v);
    WAITV(0);
  }
  const f4 s = (__builtin_bit_cast(f4, v[0]) + __builtin_bit_cast(f4, v[1])) + (__builtin_bit_cast(f4, v[2]) + __builtin_bit_cast(f4, v[3]));
  const f2 p2 = {plus, plus};
  return v2u{pk2(unpk(pk(s.x, s.y)) + p2), pk2(unpk(pk(s.z, s.w)) + p2)};
}
DI f4 widen(v2u p) { return f4{bflo(p.x), bfhi(p.x), bflo(p.y), bfhi(p.y)}; }

#define STP
#define STA

DI v4u mkrs(const void* p, unsigned bytes) { return v4u{(unsigned)(uintptr_t)p, (unsigned)((uintptr_t)p >> 32) & 0xffffu, bytes, 0x00020000u}; }
DI void mod_loads(const u16* c_, const u16* wmod_, bool on, int mcb, int mks, int v, int l, v2u& cw, v4u (&wm)[4]) {
  const int r0 = 128 * mks + 32 * v + 4 * (l >> 3);
  const v4u rc = mkrs(c_, on ? 1024u : 0u), rw = mkrs(wmod_, on ? 512u * 6144u : 0u);
  asm volatile("buffer_load_dwordx2 %0, %1, %2, 0 offen" : "=v"(cw) : "v"(r0 * 2), "s"(rc));
#pragma unroll
  for (int e = 0; e < 4; ++e) asm volatile("buffer_load_dwordx4 %0, %1, %2, %3 offen nt" : "=v"(wm[e]) : "v"(r0 * 6144 + 128 * mcb + 16 * (l & 7)), "s"(rw), "s"(e * 6144));
}
#define MOD_WAIT(n) asm volatile("s_waitcnt vmcnt(%5)" : "+v"(cw), "+v"(wm[0]), "+v"(wm[1]), "+v"(wm[2]), "+v"(wm[3]) : "i"(n) : "memory")
DI v4u mod_job(LAS char* L, int red, int v, int l, unsigned tag, v2u cw, const v4u (&wm)[4]) {
  v4u res = {0u, 0u, 0u, 0u};
  f2 a2[4];
#pragma unroll
  for (int e = 0; e < 4; ++e) {
    const float cv = (e & 1) ? bfhi(cw[e >> 1]) : bflo(cw[e >> 1]);
    float sv = cv * __builtin_amdgcn_rcpf(1.0f + __builtin_amdgcn_exp2f(-1.4426950409f * cv));
    sv = bflo(pk(sv, sv));
    const f2 s2 = {sv, sv};
#pragma unroll
    for (int k = 0; k < 4; ++k) a2[k] = e ? fma2(s2, unpk(wm[e][k]), a2[k]) : s2 * unpk(wm[e][k]);
  }
  float a[4];
#pragma unroll
  for (int k = 0; k < 4; ++k) {
    auto r = __builtin_amdgcn_permlane32_swap(__float_as_uint(a2[k >> 1][k & 1]), __float_as_uint(a2[2 + (k >> 1)][k & 1]), false, false);
    a[k] = __uint_as_float(r[0]) + __uint_as_float(r[1]);
  }
#pragma unroll
  for (int k = 0; k < 2; ++k) {
    auto r = __builtin_amdgcn_permlane16_swap(__float_as_uint(a[k]), __float_as_uint(a[k + 2]), false, false);
    a[k] = __uint_as_float(r[0]) + __uint_as_float(r[1]);
    a[k] += dpp<0x128>(a[k]);
  }
  if (!(l & 8)) *(LAS f2*)(L + red + v * 256 + (8 * (l & 7) + 2 * ((l >> 4) & 1) + 4 * (l >> 5)) * 4) = f2{a[0], a[1]};
  asm volatile("s_waitcnt lgkmcnt(0)" ::: "memory");
  volatile LAS unsigned* mf = (volatile LAS unsigned*)(L + red + 1024);
  if (v < 3) mf[v] = tag;
  else {
    _Pragma("nounroll") for (unsigned s = 0; s < SPIN_CAP; ++s)
      if (__all(mf[l < 3 ? l : 0] == tag)) break;
    asm volatile("" ::: "memory");
    f4 s = *(LAS f4*)(L + red + (l & 15) * 16);
#pragma unroll
    for (int ww = 1; ww < 4; ++ww) s += *(LAS f4*)(L + red + ww * 256 + (l & 15) * 16);
    res = (__builtin_bit_cast(v4u, s) & 0xffffff00u) | (tag & 0xffu);
  }
  return res;
}
DI void mod_publish(rsrc_t rs, int mcb, int mks, int v, int l, v4u res) {
  if (v == 3 && l < 16) stf(rs, res, l * 16, OFF_MODP + mks * 12288 + mcb * 256);
}

struct Ctx {
  rsrc_t rs; v4u rsv; LAS char* L; unsigned Lb, tag; int g, j, w, t03, tile0; gu32* gfl;
  const u16 *x_, *b1_, *b2_; u16* out_; gu32* epoch;
};

template <int T> DI void col_terms(const Ctx& c, bool two, int l, const v4u (&w3a)[16], const v4u (&w3b)[16], float b1a, float b1b, float (&uvt)[T]) {
  l = fresh(l);
  const int i = l & 15, q = l >> 4, ty = i >> 2;
  const int base = L_UV + (ty < 2 ? ty * 2048 + c.j * 128 : 2048 + ty * 1024) + q * 16;
  f4 a[2][T];
  v4u am[16];
#pragma unroll
  for (int s = 0; s < 16; ++s) am[s] = *(LAS v4u*)(c.L + base + s * 64);
  SB();
#pragma unroll
  for (int s = 0; s < 16; ++s) MAC(s < 2, a[s & 1][0], am[s], w3a[s]);
  if (T == 2 && two) {
#pragma unroll
    for (int s = 0; s < 16; ++s) MAC(s < 2, a[s & 1][T - 1], am[s], w3b[s]);
  }
  MFMA_DONE();
  uvt[0] = a[0][0].x + a[1][0].x + (q == 3 ? b1a : 0.f);
  if (T == 2) uvt[T - 1] = a[0][T - 1].x + a[1][T - 1].x + (q == 3 ? b1b : 0.f);
}

template <int T, int KIND>
DI void mlp1_loop(const Ctx& c, int l, const v4u (&w3a)[16], const v4u (&w3b)[16], f4 (&acc)[4][T], v4u (&w4)[44], unsigned w4off, bool two,
                  const float (&S)[5] STP) {
  LAS char* L = c.L;
  const int tid = c.w * 64 + l;
  auto xa_slot = [&](int slot) { dma<4>(c.rsv, l * 16, OFF_XA + (c.g * 16 + ((slot + 2 * c.j) & 15)) * 4096, c.Lb + slot * 4096); };
  v4u a[2][4];
#pragma unroll
  for (int r = 0; r < 4; ++r) a[0][r] = *(LAS v4u*)(L + r * 1024 + l * 16);
#pragma unroll
  for (int s = 0; s < 16; ++s) {
    if (s == 1 || s == 5 || s == 9) {
      if (KIND == 2 && c.w == 3) {
        if (s == 1) WAITV(16);
        if (s == 5) WAITV(24);
        if (s == 9) WAITV(16);
      }
      SBAR();
      SL(22 + s / 4);
      if (KIND == 2 && c.w == 3) {
        if (s == 1) {
#pragma unroll
          for (int k = 10; k < 16; ++k) xa_slot(k);
        }
        if (s == 5) {
#pragma unroll
          for (int p = 0; p < 4; ++p) dma<4>(c.rsv, l * 16, OFF_STAT + c.g * 16384 + p * 4096, c.Lb + L_STAT + p * 4096);
        }
        if (s == 9) {
          float sx, sxx, sp, spp, spa, sps;
          _Pragma("nounroll") for (unsigned it = 0; it < SPIN_CAP; ++it) {
            WAITV(0);
            v4u a4[8]; v2u b2[8];
            unsigned d = 0;
#pragma unroll
            for (int p = 0; p < 8; ++p) {
              a4[p] = *(LAS v4u*)(L + L_STAT + p * 2048 + l * 16) ^ (c.tag & 0xffu);
              b2[p] = *(LAS v2u*)(L + L_STAT + p * 2048 + 1024 + l * 16) ^ (c.tag & 0xffu);
              d |= (a4[p].x | a4[p].y) | (a4[p].z | a4[p].w) | (b2[p].x | b2[p].y);
            }
            f4 ta = __builtin_bit_cast(f4, a4[0]); f2 tb = __builtin_bit_cast(f2, b2[0]);
#pragma unroll
            for (int p = 1; p < 8; ++p) { ta += __builtin_bit_cast(f4, a4[p]); tb += __builtin_bit_cast(f2, b2[p]); }
            sx = ta.x; sxx = ta.y; sp = ta.z; spp = ta.w; spa = tb.x; sps = tb.y;
            if (__all(d << 24 == 0u)) break;
            asm volatile("s_waitcnt lgkmcnt(0)" ::: "memory");
#pragma unroll
            for (int p = 0; p < 4; ++p) dma<4>(c.rsv, l * 16, OFF_STAT + c.g * 16384 + p * 4096, c.Lb + L_STAT + p * 4096);
          }
          const float inv = 1.0f / 512.0f;
          const float m = sx * inv, var = fmaxf(sxx * inv - m * m, 0.f);
          const float sd = __builtin_amdgcn_sqrtf(var + 1e-5f), r = __builtin_amdgcn_rcpf(sd);
          const float m2 = (r * (sp - m * S[0]) + S[1]) * inv;
          const float e2 = (r * r * (spp - 2.f * m * spa + m * m * S[2]) + 2.f * r * (sps - m * S[3]) + S[4]) * inv;
          const float sd2 = __builtin_amdgcn_sqrtf(fmaxf(e2 - m2 * m2, 0.f) + 1e-5f);
          const float R = r * __builtin_amdgcn_rcpf(sd2), k1 = -2.302208198f * R;
          *(LAS f4*)(L + L_COEF + l * 16) = f4{-m, sd, -m2 * sd, sd * sd2};
          *(LAS f4*)(L + L_RR + l * 16) = f4{k1, 0.044715f * k1 * R * R, sd * sd2, 0.f};
        }
      }
    }
    SB();
#pragma unroll
    for (int r = 0; r < 4; ++r) {
      MAC(s == 0, acc[r][0], w3a[s], a[s & 1][r]);
      SB();
      if (s < 15) a[(s + 1) & 1][r] = *(LAS v4u*)(L + ((s + 1) * 4 + r) * 1024 + l * 16);
      SB();
    }
    if (T == 2 && two) {
#pragma unroll
      for (int r = 0; r < 4; ++r) MAC(s == 0, acc[r][T - 1], w3b[s], a[s & 1][r]);
      SB();
    }
    if (KIND == 1) {
      const int n0 = s < 4 ? 2 * s : 3 * s - 4;
      w4[n0] = LDW(n0, w4off);
      w4[n0 + 1] = LDW(n0 + 1, w4off);
      if (s >= 4) w4[n0 + 2] = LDW(n0 + 2, w4off);
      SB();
    }
  }
  MFMA_DONE();
}

template <int T> DI void mlp1_epi(const Ctx& c, bool two, int l, const float (&uvt)[T], f4 (&acc)[4][T]) {
  l = fresh(l);
  const int i = l & 15, q = l >> 4;
  float cf[4]; f4 rr[4];
#pragma unroll
  for (int r = 0; r < 4; ++r) {
    cf[r] = *(LAS float*)(c.L + L_COEF + (r * 16 + i) * 16 + q * 4);
    rr[r] = *(LAS f4*)(c.L + L_RR + (r * 16 + i) * 16);
  }
#pragma unroll
  for (int t = 0; t < T; ++t) {
    if (t && !two) break;
#pragma unroll
    for (int r = 0; r < 4; ++r) acc[r][t] = __builtin_amdgcn_mfma_f32_16x16x4f32(uvt[t], cf[r], acc[r][t], 0, 0, 0);
  }
  SB();
#pragma unroll
  for (int t = 0; t < T; ++t) {
    if (t && !two) break;
    const int tl = c.tile0 + t;
#pragma unroll
    for (int pp = 0; pp < 2; ++pp) {
      unsigned h[2][2];
#pragma unroll
      for (int r2 = 0; r2 < 2; ++r2) {
        const int r = 2 * pp + r2;
        const f2 k1 = {rr[r].x, rr[r].x}, k2 = {rr[r].y, rr[r].y}, ir = {rr[r].z, rr[r].z};
        h[r2][0] = gelu2(f2{acc[r][t].x, acc[r][t].y}, k1, k2, ir);
        h[r2][1] = gelu2(f2{acc[r][t].z, acc[r][t].w}, k1, k2, ir);
      }
      auto s0 = __builtin_amdgcn_permlane16_swap(h[0][0], h[1][0], false, false);
      auto s1 = __builtin_amdgcn_permlane16_swap(h[0][1], h[1][1], false, false);
      stf(c.rs, v4u{s0[0], s1[0], s0[1], s1[1]}, (q & 1) * 1024 + ((q >> 1) * 16 + i) * 16,
          OFF_H1 + ((c.g * 43 + (tl >> 1)) * 4 + 2 * pp) * 1024 + (tl & 1) * 512);
      SB();
    }
  }
}

DI void dma_in(v4u rs, unsigned voff, unsigned soff, unsigned lds) {
  unsigned keep;
  asm volatile("s_mov_b32 %0, m0\n\ts_mov_b32 m0, %2\n\ts_nop 0\n\tbuffer_load_dwordx4 %1, %3, %4 offen lds\n\ts_mov_b32 m0, %0"
               : "=&s"(keep) : "v"(voff), "s"(lds), "s"(rs), "s"(soff) : "memory");
}
DI void sum8x6(float& a, float& b, float& c, float& d, float& e, float& f) {
#define S6(ctl) "v_add_f32_dpp %0, %0, %0 " ctl "\n\tv_add_f32_dpp %1, %1, %1 " ctl "\n\tv_add_f32_dpp %2, %2, %2 " ctl "\n\t" \
                "v_add_f32_dpp %3, %3, %3 " ctl "\n\tv_add_f32_dpp %4, %4, %4 " ctl "\n\tv_add_f32_dpp %5, %5, %5 " ctl "\n\t"
  asm volatile("s_nop 1\n\t" S6("quad_perm:[1,0,3,2] row_mask:0xf bank_mask:0xf") S6("quad_perm:[2,3,0,1] row_mask:0xf bank_mask:0xf")
               S6("row_half_mirror row_mask:0xf bank_mask:0xf") "s_nop 0" : "+v"(a), "+v"(b), "+v"(c), "+v"(d), "+v"(e), "+v"(f));
#undef S6
}
DI void red_write(const Ctx& c, int l, int nh, int kh, const f4 (&acc)[4][2]) {
  l = fresh(l);
  const int rb = L_RED + kh * 17408 + (l & 15) * 272 + nh * 128 + (l >> 4) * 16;
#pragma unroll
  for (int r = 0; r < 4; ++r)
#pragma unroll
    for (int t = 0; t < 2; ++t) *(LAS f4*)(c.L + rb + r * 16 * 272 + t * 64) = acc[r][t];
  asm volatile("s_waitcnt lgkmcnt(0)" ::: "memory");
}
DI void red_read(const Ctx& c, int tok, int f8, f2 (&y)[4]) {
  const int ra = L_RED + tok * 272 + f8 * 32;
  const f4 y0 = *(LAS f4*)(c.L + ra) + *(LAS f4*)(c.L + ra + 17408), y1 = *(LAS f4*)(c.L + ra + 16) + *(LAS f4*)(c.L + ra + 17408 + 16);
  y[0] = f2{y0.x, y0.y}; y[1] = f2{y0.z, y0.w}; y[2] = f2{y1.x, y1.y}; y[3] = f2{y1.z, y1.w};
}
DI void ec_read(const Ctx& c, int sec, int f8, f2 (&y)[4]) {
  const f4 y0 = *(LAS f4*)(c.L + L_EC + sec * 256 + f8 * 32), y1 = *(LAS f4*)(c.L + L_EC + sec * 256 + f8 * 32 + 16);
  y[0] = f2{y0.x, y0.y}; y[1] = f2{y0.z, y0.w}; y[2] = f2{y1.x, y1.y}; y[3] = f2{y1.z, y1.w};
}
DI void epi2(const Ctx& c, int t, f2 (&x1)[2][4]) {
  t = fresh(t);
  const int tk = t >> 3, f8 = t & 7;
  f2 ga[4], a2[4], sb[4], p[2][4];
  ec_read(c, 0, f8, ga); ec_read(c, 1, f8, a2); ec_read(c, 2, f8, sb);
#pragma unroll
  for (int k = 0; k < 2; ++k) {
    f2 y[4];
    red_read(c, tk + 32 * k, f8, y);
#pragma unroll
    for (int e = 0; e < 4; ++e) { x1[k][e] = fma2(ga[e], y[e], x1[k][e]); p[k][e] = x1[k][e] * a2[e]; }
    stf(c.rs, v4u{pk2(p[k][0]), pk2(p[k][1]), pk2(p[k][2]), pk2(p[k][3])},
        ((f8 >> 2) * 4 + (tk >> 4) + 2 * k) * 1024 + ((f8 & 3) * 16 + (tk & 15)) * 16, OFF_XA + (c.g * 16 + 2 * c.j) * 4096);
  }
  SB();
#pragma unroll
  for (int k = 0; k < 2; ++k) {
    f2 vx = x1[k][0], vxx = x1[k][0] * x1[k][0], vp = p[k][0], vpp = p[k][0] * p[k][0], vpa = p[k][0] * a2[0], vps = p[k][0] * sb[0];
#pragma unroll
    for (int e = 1; e < 4; ++e) {
      vx += x1[k][e]; vxx = fma2(x1[k][e], x1[k][e], vxx); vp += p[k][e]; vpp = fma2(p[k][e], p[k][e], vpp);
      vpa = fma2(p[k][e], a2[e], vpa); vps = fma2(p[k][e], sb[e], vps);
    }
    float sx = vx.x + vx.y, sxx = vxx.x + vxx.y, sp = vp.x + vp.y, spp = vpp.x + vpp.y, spa = vpa.x + vpa.y, sps = vps.x + vps.y;
    sum8x6(sx, sxx, sp, spp, spa, sps);
    const v4u sw = (__builtin_bit_cast(v4u, f8 == 0 ? f4{sx, sxx, sp, spp} : f4{spa, sps, spa, sps}) & 0xffffff00u) | (c.tag & 0xffu);
    if (f8 < 2) stf(c.rs, sw, (tk + 32 * k) * 16 + f8 * 1024, OFF_STAT + (c.g * 8 + c.j) * 2048);
  }
}
DI void epi4(const Ctx& c, int t, v4u b2v, const f2 (&x1)[2][4]) {
  t = fresh(t);
  const int tk = t >> 3, f8 = t & 7;
  const rsrc_t ro = __builtin_amdgcn_make_buffer_rsrc((void*)c.out_, 0, 2048 * 512 * 2, 0x00020000);
  f2 gb[4];
  ec_read(c, 3, f8, gb);
#pragma unroll
  for (int k = 0; k < 2; ++k) {
    f2 y[4];
    red_read(c, tk + 32 * k, f8, y);
    v4u o;
#pragma unroll
    for (int e = 0; e < 4; ++e) o[e] = pk2(fma2(gb[e], y[e] + unpk(b2v[e]), x1[k][e]));
    __builtin_amdgcn_raw_buffer_store_b128(o, ro, (tk + 32 * k) * 1024 + f8 * 16 + c.g * 65536 + c.j * 128, 0, 0);
  }
}

DI void tail_c(const Ctx& c, bool two, int tid STP) {
  tid = fresh(tid);
  const int l = tid & 63, i = l & 15, w = c.w, g = c.g, j = c.j;
  LAS char* L = c.L;
  const unsigned w3off = OFF_W3T + c.tile0 * 16384;
  v4u w3a[16], w3b[16], w4[44];
  WAITV(0);
  meet4(L, L_W + 32, w, c.tag, l);
  if (tid == 128) __hip_atomic_store(c.gfl + j * FSTR, c.tag, RLX_AGENT);
  ST(11);
  auto o_slot = [&](int slot) { dma<4>(c.rsv, l * 16, OFF_O + (g * 16 + ((slot + 2 * j) & 15)) * 4096, c.Lb + slot * 4096); };
  if (w < 2) o_slot(w);
  const float b1a = bflo(((const GAS u16*)c.b1_)[c.tile0 * 16 + i]), b1b = bflo(((const GAS u16*)c.b1_)[(c.tile0 + (two ? 1 : 0)) * 16 + i]);
  if (w == 3) poll8<14>(c.gfl, c.tag, l);
  if (w == 2) {
    __builtin_amdgcn_s_sleep(14);
    _Pragma("nounroll") for (unsigned s = 0; s < SPIN_CAP; ++s) {
      const v4u rf = ld_rflags(c.rs, l);
      if (__all(rf.x == c.tag && rf.y == c.tag && rf.z == c.tag && rf.w == c.tag)) break;
      __builtin_amdgcn_s_sleep(8);
      asm volatile("" ::: "memory");
    }
    if (g == 0 && j == 0 && l == 0) *c.epoch = c.tag;
  }
  WAITV(0);
  ST(17);
  SBAR();
  ST(8);
  o_slot(2 + w); o_slot(6 + w); o_slot(10 + w); o_slot(14 + (w & 1));
  v4u m1[4], m2[4];
  const unsigned o1 = (1536 + 256 * w + 4 * l) * 4, o2 = (((l & 16) ? 2560 : 1024) + 64 * j + 4 * i) * 4;
  WAITV_BAR(12); SL(20);
  mod_get(c.rs, o1, m1);
  mod_get(c.rs, o2, m2);
  WAITV_BAR(16);
#pragma unroll
  for (int n = 0; n < 4; ++n) w3a[n] = LDW(n, w3off);
  WAITV_BAR(16);
#pragma unroll
  for (int n = 4; n < 8; ++n) w3a[n] = LDW(n, w3off);
  WAITV_BAR(16); SL(21);
  {
    const v2u p = mod_sum(c.rs, o1, m1, c.tag, w < 2 ? 0.f : 1.f);
    const int u = L_UV + (w < 2 ? 2048 : 0) + (w & 1) * 512 + l * 8;
    *(LAS v2u*)(L + u) = p; *(LAS v2u*)(L + u + 1024) = p;
    if ((w & 1) * 4 + (l >> 4) == j) *(LAS f4*)(L + L_EC + (w < 2 ? 512 : 256) + i * 16) = widen(p);
    const v2u p2 = mod_sum(c.rs, o2, m2, c.tag, 0.f);
    if (w == 3 && l < 32) *(LAS f4*)(L + L_EC + ((l & 16) ? 768 : 0) + i * 16) = widen(p2);
    if (w < 2) { const unsigned f = w ? 0u : 0x3f803f80u; *(LAS v4u*)(L + L_UV + 4096 + w * 1024 + l * 16) = v4u{f, f, f, f}; }
    asm volatile("s_waitcnt lgkmcnt(0)" ::: "memory");
    post(L, L_W + 48 + 4 * w, c.tag);
  }
#pragma unroll
  for (int n = 8; n < 16; ++n) w3a[n] = LDW(n, w3off);
  if (two) {
#pragma unroll
    for (int n = 0; n < 16; ++n) w3b[n] = LDW(16 + n, w3off);
  }
  wait4(L, L_W + 48, c.tag, l);
  float S[5] = {0.f, 0.f, 0.f, 0.f, 0.f};
  if (w == 3) {
    const v4u av = *(LAS v4u*)(L + L_UV + l * 16), sv = *(LAS v4u*)(L + L_UV + 2048 + l * 16);
#pragma unroll
    for (int e = 0; e < 4; ++e) {
      const float a0 = bflo(av[e]), a1 = bfhi(av[e]), s0 = bflo(sv[e]), s1 = bfhi(sv[e]);
      S[0] += a0 + a1; S[1] += s0 + s1; S[2] += a0 * a0 + a1 * a1; S[3] += a0 * s0 + a1 * s1; S[4] += s0 * s0 + s1 * s1;
    }
#pragma unroll
    for (int e = 0; e < 5; ++e) S[e] = swap32_sum(swap16_sum(sum16(S[e])));
  }
  WAITV(0);
  ST(9);
  float uvt[2];
  col_terms<2>(c, two, l, w3a, w3b, b1a, b1b, uvt);
  USE(uvt[0]); USE(uvt[1]);
  if (w == 3) {
    wait4(L, L_W + 96, c.tag, l);
    poll8<14>(c.gfl + GSEAM, c.tag, l);
    ST(15);
#pragma unroll
    for (int k = 2; k < 10; ++k) dma<4>(c.rsv, l * 16, OFF_XA + (g * 16 + ((k + 2 * j) & 15)) * 4096, c.Lb + k * 4096);
  }
  SBAR();
  ST(10);
  f4 acc[4][2];
  mlp1_loop<2, 2>(c, l, w3a, w3b, acc, w4, 0, two, S STA);
  BAR();
  ST(12);
  mlp1_epi<2>(c, two, l, uvt, acc);
  ST(18);
  WAITV_BAR(0);
  ST(13);
  const int rot4 = (c.t03 + 1) >> 1;
  auto h_slot = [&](int sg) {
    int k = rot4 + sg; if (k >= 43) k -= 43;
    dma<4>(c.rsv, l * 16, OFF_H1 + (g * 43 + k) * 4096, c.Lb + (sg >= 38 ? sg - 38 : sg) * 4096);
  };
  if (w == 3) poll8<14>(c.gfl + 2 * GSEAM, c.tag, l);
  else { h_slot(w); if (w == 0) h_slot(3); }
  WAITV_BAR(0);
  ST(14);
  h_slot(4 + w); h_slot(8 + w); h_slot(12 + w);
#pragma unroll
  for (int m = 1; m < 10; ++m) {
    if (m == 1 || m == 7) WAITV_BAR(8);
    else if (m < 7) WAITV_BAR(12);
    else if (m == 8) WAITV_BAR(4);
    else WAITV_BAR(0);
    if (m == 1) SL(26);
    if (m == 5) SL(27);
    if (m == 9) SL(28);
    if (m == 1) h_slot(16 + w);
    if (m + 4 < 10 || (m + 4 == 10 && w < 3)) h_slot(4 * (m + 4) + w);
  }
}

extern "C" __global__ void __launch_bounds__(512, 2)
fused_block(const u16* __restrict__ x_, const u16* __restrict__ wqkv_, const u16* __restrict__ wo_, const u16* __restrict__ w1_,
            const u16* __restrict__ b1_, const u16* __restrict__ w2_, const u16* __restrict__ b2_, const u16* __restrict__ c_,
            const u16* __restrict__ wmod_, u16* __restrict__ out_, unsigned char* __restrict__ ws_) {
  extern __shared__ __attribute__((aligned(16))) char smem[];
  asm volatile("" ::"s"(x_), "s"(wqkv_), "s"(wo_), "s"(w1_), "s"(b1_), "s"(w2_), "s"(b2_), "s"(c_), "s"(wmod_), "s"(out_), "s"(ws_));
  unsigned long long pc;
  asm volatile("s_getpc_b64 %0" : "=s"(pc));
  LAS char* L = (LAS char*)smem;
  const unsigned Lb = (unsigned)(uintptr_t)L;
  const int tid0 = threadIdx.x, w = __builtin_amdgcn_readfirstlane(tid0 >> 6);
  const int b = blockIdx.x, xcd = b & 7, idx = b >> 3, g = xcd * 4 + (idx >> 3), j = idx & 7;
  GAS unsigned char* ws = (GAS unsigned char*)ws_;
  const rsrc_t rs = __builtin_amdgcn_make_buffer_rsrc((void*)ws_, 0, WS_USED, 0x00020000);
  const v4u rsv = {(unsigned)(uintptr_t)ws_, (unsigned)((uintptr_t)ws_ >> 32) & 0xffffu, WS_USED, 0x00020000u};
  SRT(32);
  ST(0);
  unsigned epoch = *(const __attribute__((address_space(4))) unsigned*)(uintptr_t)(ws_ + OFF_E);
#define GET_TAG asm volatile("" : "+s"(epoch) : : "memory"); const unsigned tag = epoch + 1u;
  const int rq = xcd >> 1;
  const int nt3 = 11 - (j >= 6), t03 = 11 * j - (j == 7);
  gu32* gfl = (gu32*)(ws + OFF_GFLAG) + g * 8 * FSTR;
  const v4u rsq = {(unsigned)(uintptr_t)wqkv_, (unsigned)((uintptr_t)wqkv_ >> 32) & 0xffffu, 512u * 1536u * 2u, 0x00020000u};
#define WSV(stride) ((8 * (w & 3) + (l >> 3)) * (stride) + 128 * j + 16 * ((l & 7) ^ ((((l >> 4) & 1) | ((w & 1) << 1)) << 1)))
#define LANE_IDS const int l = lane_id(), tid = w * 64 + l, i = l & 15, q = l >> 4; const unsigned w1v = WSV(3072);
#define RETILE_IDS(v) \
    const int u = 4 * (b < 64 ? b : b >= 192 ? b - 128 : b + 64) + v; \
    const bool retile = u < 696, isw1 = u < 352; \
    const int rcb = isw1 ? u >> 4 : (int)((unsigned)(u - 352) / 43u), rks = isw1 ? (u & 15) : u - 352 - 43 * rcb; \
    const int rnt = (isw1 && rcb == 21) ? 2 : 4, rst = v < 3 ? L_RT + v * 4096 : L_EC; \

  v4u xr[5][4];
  if (w < 4) {
    const int l = lane_id();
    unsigned dummy;
    v2u cw; v4u wm[4];
    const int mcb = b & 15, mks = b >> 4;
    if (b < 64) { __builtin_amdgcn_s_setprio(3); mod_loads(c_, wmod_, true, mcb, mks, w, l, cw, wm); }
    asm volatile("s_load_dword %0, %1, 0x0\n\ts_load_dword %0, %1, 0x40\n\ts_load_dword %0, %1, 0x80\n\t"
                 "s_load_dword %0, %1, 0xc0\n\ts_load_dword %0, %1, 0x100\n\ts_load_dword %0, %1, 0x140"
                 : "=&s"(dummy) : "s"((pc & ~255ull) + idx * CODE_STEP + w * 384));
    if (b < 64) {
      asm volatile("" : "+s"(epoch) : : "memory");
      MOD_WAIT(0);
      mod_publish(rs, mcb, mks, w, l, mod_job(L, L_MRED, w, l, epoch + 1u, cw, wm));
      __builtin_amdgcn_s_setprio(0);
    }
    ST(29);
    const unsigned w1v = WSV(3072);
#pragma unroll
    for (int s0 = 0; s0 < 9; ++s0) {
      const unsigned t = s0 + 2 * (xcd & 1), s = s0 >= 6 ? s0 : t < t - 6u ? t : t - 6u;
      dma3(rsq, w1v, ((s + 4 * rq) & 15) * (32 * 3072), Lb + (s0 >= 6 ? L_R1 + (s0 - 6) * 12288 : s * 12288) + w * 3072);
    }
    asm volatile("s_waitcnt lgkmcnt(0)" : "+s"(dummy) : : "memory");
    ST(30);
    WAITV(9);
  } else {
    const int l = lane_id();
    __builtin_amdgcn_s_setprio(3);
#pragma unroll
    for (int p = 0; p < 5; ++p) {
      int tok = 64 * g - 16 + 20 * (w & 3) + (l >> 4) + 4 * p;
      tok = tok < 0 ? 0 : tok;
#pragma unroll
      for (int n = 0; n < 4; ++n)
        asm volatile("global_load_dwordx4 %0, %1, %2" : "=v"(xr[p][n]) : "v"(tok * 1024 + 256 * ((n + rq) & 3) + 16 * (l & 15)), "s"(x_));
    }
    ST(29);
    SB();
  }
  if (w < 4) {
    asm volatile("; matrix waves" ::: "memory");
    LANE_IDS
    GET_TAG
    ST(2);
    meet4(L, L_SYNC, w, tag, l);
    ST(3);
    const int tb_ = q * 3072 + (i >> 2) * 128 + ((w ^ ((i >> 3) | ((q & 1) << 1))) * 32) + (i & 3) * 8;
    const int tb1 = fresh(tb_ + L_R1);
    v4u wf[16][3];
#pragma unroll
    for (int s = 0; s < 6; ++s)
#pragma unroll
      for (int sg = 0; sg < 3; ++sg) wf[s][sg] = trfrag(L + tb_ + s * 12288 + sg * 1024);
    dma3(rsq, w1v, ((9 + 4 * rq) & 15) * (32 * 3072), Lb + L_R1 + 3 * 12288 + w * 3072);
    dma3(rsq, w1v, ((10 + 4 * rq) & 15) * (32 * 3072), Lb + L_R1 + 4 * 12288 + w * 3072);
    {
      const int v = w;
      RETILE_IDS(v)
    {
      const int nc = isw1 ? 2752 : 1024;
      const v4u rr = mkrs(isw1 ? w1_ : w2_, retile ? 1376u * 1024u : 0u);
      const unsigned vo = (32 * rks + (l >> 3)) * nc + 128 * rcb + 16 * (rnt == 2 ? (l & 3) : (l & 7));
#pragma unroll
      for (int e = 0; e < 4; ++e) dma_nt(rr, vo, 8 * e * nc, Lb + rst + e * 1024);
    }
    }
    ST(7);
    BAR();
    ST(4);
    f4 aq[4], ak[5], av[5];
    const int a0 = l * 16 ^ (q << 4), a1 = l * 16 ^ ((q | 4) << 4), a0h = fresh(a0 + 61440), a1h = fresh(a1 + 61440);
    v4u a[2][5];
    SBAR();
    ST(5);
#pragma unroll
    for (int r = 0; r < 5; ++r) a[0][r] = *(LAS v4u*)(L + r * 1024 + a0);
#pragma unroll
    for (int s = 0; s < 16; ++s) {
      if (s == 5) WAITV(4);
      if (s == 10) WAITV(0);
      if (s == 3 || s == 5 || s == 8 || s == 10 || s == 12) SBAR();
      if (s == 5) SL(31);
      if (s == 10) SL(34);
      SB();
#pragma unroll
      for (int r = 0; r < 5; ++r) {
        MAC(s == 0, ak[r], wf[s][1], a[s & 1][r]);
        SB();
        if (s < 11) a[(s + 1) & 1][r] = *(LAS v4u*)(L + ((s + 1) * 5 + r) * 1024 + (((s + 1) & 1) ? a1 : a0));
        else if (s < 15) a[(s + 1) & 1][r] = *(LAS v4u*)(L + ((s - 11) * 5 + r) * 1024 + (((s + 1) & 1) ? a1h : a0h));
        SB();
        MAC(s == 0, av[r], a[s & 1][r], wf[s][2]);
        SB();
        if (s >= 5 && s < 15 && r < 3) { wf[s + 1][r] = trfrag(L + (s < 10 ? tb1 + (s - 5) * 12288 : s < 12 ? tb_ + (s - 10) * 12288 : tb1 + (s - 12) * 12288) + r * 1024); SB(); }
        if (r) { MAC(s == 0, aq[r - 1], wf[s][0], a[s & 1][r]); SB(); }
      }
    }
    MFMA_DONE();
    post(L, L_W + 16 + 4 * w, tag);
    SB();
    ST(6);
    {
      v2u qb[4], kb[5], vb[5];
#pragma unroll
      for (int r = 0; r < 5; ++r) {
        kb[r] = v2u{pk(ak[r].x, ak[r].y), pk(ak[r].z, ak[r].w)};
        vb[r] = v2u{pk(av[r].x, av[r].y), pk(av[r].z, av[r].w)};
        if (r) qb[r - 1] = v2u{pk(aq[r - 1].x, aq[r - 1].y), pk(aq[r - 1].z, aq[r - 1].w)};
      }
      const bool odd = i & 1;
      const bool cur0 = 4 * q + (i & 1) <= i, cur1 = 4 * q + 2 + (i & 1) <= i;
      const float sc = 0.25f * 1.4426950409f;
      const int hd = 4 * j + w;
      const f4 z = {0.f, 0.f, 0.f, 0.f};
      f4 sp[4], scu[4];
#pragma unroll
      for (int r = 0; r < 4; ++r) { sp[r] = mfma16(kb[r], qb[r], z); scu[r] = mfma16(kb[r + 1], qb[r], z); }
      const unsigned lo = odd ? 0x01000c0cu : 0x0c0c0100u, hi = odd ? 0x03020c0cu : 0x0c0c0302u, no = 0x0c0c0c0cu;
      const unsigned sel0 = cur0 ? no : lo, sel1 = cur1 ? no : hi, sel2 = cur0 ? lo : no, sel3 = cur1 ? hi : no;
      float v0[4], v1[4], m[4];
#pragma unroll
      for (int r = 0; r < 4; ++r) {
        float p0 = odd ? sp[r].y : sp[r].x, p1 = odd ? sp[r].w : sp[r].z;
        const float c0 = odd ? scu[r].y : scu[r].x, c1 = odd ? scu[r].w : scu[r].z;
        if (r == 0 && g == 0) { p0 = -4e9f; p1 = -4e9f; }
        v0[r] = cur0 ? c0 : p0; v1[r] = cur1 ? c1 : p1;
        m[r] = max2(v0[r], v1[r]);
      }
      spread4(gather4<true>(m) * sc, m);
#pragma unroll
      for (int r = 0; r < 4; ++r) {
        v0[r] = __builtin_amdgcn_exp2f(__builtin_fmaf(v0[r], sc, -m[r])); v1[r] = __builtin_amdgcn_exp2f(__builtin_fmaf(v1[r], sc, -m[r]));
        m[r] = v0[r] + v1[r];
      }
      spread4(__builtin_amdgcn_rcpf(gather4<false>(m)), m);
      f4 o[4];
#pragma unroll
      for (int r = 0; r < 4; ++r) {
        const unsigned pp = pk(v0[r] * m[r], v1[r] * m[r]);
        const v4u pf = {__builtin_amdgcn_perm(0u, pp, sel0), __builtin_amdgcn_perm(0u, pp, sel1), __builtin_amdgcn_perm(0u, pp, sel2), __builtin_amdgcn_perm(0u, pp, sel3)};
        o[r] = mfma32(v4u{vb[r].x, vb[r].y, vb[r + 1].x, vb[r + 1].y}, pf, z);
      }
      unsigned h[4][2];
#pragma unroll
      for (int r = 0; r < 4; ++r) { h[r][0] = pk(o[r].x, o[r].y); h[r][1] = pk(o[r].z, o[r].w); }
#pragma unroll
      for (int pp = 0; pp < 2; ++pp) {
        auto s0 = __builtin_amdgcn_permlane16_swap(h[2 * pp][0], h[2 * pp + 1][0], false, false);
        auto s1 = __builtin_amdgcn_permlane16_swap(h[2 * pp][1], h[2 * pp + 1][1], false, false);
        stf(rs, v4u{s0[0], s1[0], s0[1], s1[1]}, (q & 1) * 1024 + ((q >> 1) * 16 + i) * 16,
            OFF_O + ((g * 16 + (hd >> 1)) * 4 + 2 * pp) * 1024 + (hd & 1) * 512);
      }
    }
    ST(19);
    const bool two = w < nt3 - 8;
    const Ctx c = {rs, rsv, L, Lb, tag, g, j, w, t03, t03 + (two ? 2 * w : nt3 - 8 + w), gfl, x_, b1_, b2_, out_, (gu32*)(ws + OFF_E)};
    tail_c(c, two, tid STA);
    ST(16);
    SLF();
    __builtin_amdgcn_endpgm();
  } else {
    asm volatile("; helper waves" ::: "memory");
    LANE_IDS
    const int v = w - 4;
    const int row0 = 20 * v + q;
    GET_TAG
    ST(1);
    f2 xn[5][16];
    v4u mv[4];
#pragma unroll
    for (int p = 0; p < 5; ++p) {
      f2 s2 = {0.f, 0.f}, q2 = {0.f, 0.f};
      asm volatile("s_waitcnt vmcnt(%4)" : "+v"(xr[p][0]), "+v"(xr[p][1]), "+v"(xr[p][2]), "+v"(xr[p][3]) : "i"(16 - 4 * p) : "memory");
      if (p == 4) { mod_get(rs, (256 * v + 4 * l) * 4, mv); SB(); }
#pragma unroll
      for (int e = 0; e < 16; ++e) {
        xn[p][e] = unpk(xr[p][e >> 2][e & 3]);
        s2 += xn[p][e];
        q2 = fma2(xn[p][e], xn[p][e], q2);
      }
      const float m = sum16(s2.x + s2.y) * (1.0f / 512.0f);
      const float r = __builtin_amdgcn_rsqf(fmaxf(sum16(q2.x + q2.y) * (1.0f / 512.0f) - m * m, 0.f) + 1e-5f);
      const f2 r2 = {r, r}, mr2 = {-m * r, -m * r};
#pragma unroll
      for (int e = 0; e < 16; ++e) { xn[p][e] = fma2(xn[p][e], r2, mr2); asm volatile("" : "+v"(xn[p][e])); }
    }
    int ha[5];
#pragma unroll
    for (int p = 0; p < 5; ++p) {
      const int r = row0 + 4 * p, qq = i & 3;
      ha[p] = ((i >> 2) * 5 + (r >> 4)) * 1024 + (qq * 16 + ((r & 15) ^ (qq | (i & 4)))) * 16;
      asm volatile("" : "+v"(ha[p]));
    }
    ST(2);
    *(LAS f4*)(L + L_MODF + (256 * v + 4 * l) * 4) = widen(mod_sum(rs, (256 * v + 4 * l) * 4, mv, tag, v < 2 ? 0.f : 1.f));
    WAITV(0);
    asm volatile("s_waitcnt lgkmcnt(0)" ::: "memory");
    ST(7);
    post(L, L_SYNC + 16 + 4 * v, tag);
    wait4(L, L_SYNC + 16, tag, l);
    auto quarter = [&](int n, v4u (&o)[5]) {
      const int ch = L_MODF + (16 * ((n + rq) & 3) + i) * 32;
      const f4 sa0 = *(LAS f4*)(L + ch), sa1 = *(LAS f4*)(L + ch + 16), ca0 = *(LAS f4*)(L + ch + 2048), ca1 = *(LAS f4*)(L + ch + 2064);
      const f2 sa2[4] = {{sa0.x, sa0.y}, {sa0.z, sa0.w}, {sa1.x, sa1.y}, {sa1.z, sa1.w}}, ca2[4] = {{ca0.x, ca0.y}, {ca0.z, ca0.w}, {ca1.x, ca1.y}, {ca1.z, ca1.w}};
#pragma unroll
      for (int p = 0; p < 5; ++p) {
#pragma unroll
        for (int e = 0; e < 4; ++e) o[p][e] = pk2(fma2(xn[p][4 * n + e], ca2[e], sa2[e]));
        asm volatile("" : "+v"(o[p]));
      }
    };
    v4u ho[4][5];
#pragma unroll
    for (int n = 0; n < 4; ++n) quarter(n, ho[n]);
    __builtin_amdgcn_s_setprio(0);
    ST(3);
    SBAR();
    ST(4);
#pragma unroll
    for (int p = 0; p < 5; ++p) *(LAS v4u*)(L + ha[p]) = ho[0][p];
    BAR();
    ST(5);
    auto w1_step = [&](int s, int at) { dma3(rsq, w1v, ((s + 4 * rq) & 15) * (32 * 3072), Lb + at + v * 3072); };
#pragma unroll
    for (int p = 0; p < 5; ++p) *(LAS v4u*)(L + ha[p] + 20480) = ho[1][p];
    BAR();
#pragma unroll
    for (int n = 2; n < 4; ++n)
#pragma unroll
      for (int p = 0; p < 5; ++p) *(LAS v4u*)(L + ha[p] + n * 20480) = ho[n][p];
    BAR();
    w1_step(11, 0); w1_step(12, 12288);
    RETILE_IDS(v)
    int rdst[4];
#pragma unroll
    for (int t = 0; t < 4; ++t) {
      const int tl = 4 * rcb + t;
      const int j3 = tl < 66 ? (int)((unsigned)tl / 11u) : (tl < 76 ? 6 : 7);
      int s4 = rks - (rcb < 6 ? (11 * rcb + 1) >> 1 : 3 + 5 * rcb); if (s4 < 0) s4 += 43;
      rdst[t] = isw1 ? OFF_W3T + (tl * 16 + ((rks - 2 * j3) & 15)) * 1024
                     : OFF_W4T + ((((rcb * 2 + (t >> 1)) * 2 + (s4 & 1)) * 22 + (s4 >> 1)) * 2 + (t & 1)) * 1024;
      if (!retile || t >= rnt) rdst[t] = 0x10000000;
      asm volatile("" : "+s"(rdst[t]));
    }
    SBAR();
    w1_step(13, L_R1); w1_step(14, L_R1 + 12288); w1_step(15, L_R1 + 2 * 12288);
    ST(34);
    WAITV(9);
    SBAR();
#pragma unroll
    for (int t = 0; t < 4; ++t) stf(rs, trfrag(L + rst + (8 * q + (i >> 2)) * 128 + t * 32 + (i & 3) * 8), l * 16, rdst[t]);
    WAITV(4);
    SBAR();
    ST(6);
    const bool job2 = b >= 64 && b < 192;
    v2u cw; v4u wm[4];
    mod_loads(c_, wmod_, job2, 16 + ((b - 64) & 31), ((b - 64) >> 5) & 3, v, fresh(l), cw, wm);
    gdma4((const GAS char*)wo_ + (idx * 256 + v * 64 + l) * 64, Lb + L_MODF + v * 256);
    WAITV(6);
    meet4(L, L_W + 128, v, tag, l);
    if (tid == 320) __hip_atomic_store((gu32*)(ws + OFF_RFLAG) + 32 * g + j, tag, RLX_AGENT);
    ST(35);
    const Ctx c = {rs, rsv, L, Lb, tag, g, j, w, t03, t03 + nt3 - 8 + w, gfl, x_, b1_, b2_, out_, (gu32*)(ws + OFF_E)};
    const int nh = v & 1, kh = v >> 1;
    v4u w2[16], w3a[16], w3b[16], w4[44];
    wait4(L, L_W + 16, tag, l);
    {
      const v4u rso = {(unsigned)(uintptr_t)wo_, (unsigned)((uintptr_t)wo_ >> 32) & 0xffffu, 512u * 512u * 2u, 0x00020000u};
      const unsigned w2v = WSV(1024);
#pragma unroll
      for (int s = 0; s < 16; ++s) dma_in(rso, w2v, ((s + 2 * j) & 15) * 32768, Lb + L_R1 + s * 4096 + v * 1024);
    }
    MOD_WAIT(16);
    v4u res;
    if (job2) res = mod_job(L, L_MODF + 2048, v, fresh(l), tag, cw, wm);
    v4u xq[2];
    {
      const rsrc_t rx = __builtin_amdgcn_make_buffer_rsrc((void*)x_, 0, 2048 * 512 * 2, 0x00020000);
      const int t = tid - 256;
#pragma unroll
      for (int k = 0; k < 2; ++k) xq[k] = __builtin_amdgcn_raw_buffer_load_b128(rx, ((t >> 3) + 32 * k) * 1024 + (t & 7) * 16, g * 65536 + j * 128, 0);
    }
    const float b1a = bflo(((const GAS u16*)b1_)[c.tile0 * 16 + i]);
    ST(31);
    WAITV(0);
    if (job2) mod_publish(rs, 16 + ((b - 64) & 31), (b - 64) >> 5, v, l, res);
    meet4(L, L_W + 64, v, tag, l);
    {
      const int tb2 = fresh(L_R1 + kh * 4096 + (8 * q + (i >> 2)) * 128 + (i & 3) * 8), f = (i >> 3) | ((q & 1) << 1);
      const int t0 = tb2 + ((2 * nh) ^ f) * 32, t1 = tb2 + ((2 * nh + 1) ^ f) * 32;
#pragma unroll
      for (int n = 0; n < 8; ++n) { w2[2 * n] = trfrag(L + t0 + n * 8192); w2[2 * n + 1] = trfrag(L + t1 + n * 8192); }
    }
    f2 x1[2][4];
#pragma unroll
    for (int k = 0; k < 2; ++k)
#pragma unroll
      for (int e = 0; e < 4; ++e) { x1[k][e] = unpk(xq[k][e]); asm volatile("" : "+v"(x1[k][e])); }
    ST(19);
    BAR();
    ST(8);
    {
      f4 acc[4][2];
      v4u a2[2][4];
      const int ab = kh * 4096 + l * 16;
#pragma unroll
      for (int r = 0; r < 4; ++r) a2[0][r] = *(LAS v4u*)(L + ab + r * 1024);
#pragma unroll
      for (int n = 0; n < 8; ++n) {
        if (!(n & 1)) SBAR();
        if (n == 0) SL(20);
        SB();
#pragma unroll
        for (int r = 0; r < 4; ++r) {
          MAC(n == 0, acc[r][0], w2[2 * n], a2[n & 1][r]);
          SB();
          if (n < 7) a2[(n + 1) & 1][r] = *(LAS v4u*)(L + ab + ((n + 1) * 8 + r) * 1024);
          SB();
          MAC(n == 0, acc[r][1], w2[2 * n + 1], a2[n & 1][r]);
          SB();
        }
      }
      MFMA_DONE();
      SL(21);
      red_write(c, l, nh, kh, acc);
    }
    meet4(L, L_W + 80, v, tag, l);
    wait4(L, L_W + 48, tag, l);
    __builtin_amdgcn_s_setprio(1);
    epi2(c, tid - 256, x1);
    __builtin_amdgcn_s_setprio(0);
    ST(17);
    asm volatile("" ::: "memory");
    const unsigned w3off = OFF_W3T + c.tile0 * 16384, w4off = OFF_W4T + ((j * 2 + nh) * 2 + kh) * 44 * 1024;
#pragma unroll
    for (int n = 0; n < 6; ++n) w3a[n] = LDW(n, w3off);
    WAITV(8);
    meet4(L, L_W + 96, v, tag, l);
    if (tid == 384) __hip_atomic_store(gfl + GSEAM + j * FSTR, tag, RLX_AGENT);
    ST(9);
    if (v < 2) dma<4>(rsv, l * 16, OFF_XA + (g * 16 + 2 * j + v) * 4096, Lb + v * 4096);
#pragma unroll
    for (int n = 6; n < 16; ++n) w3a[n] = LDW(n, w3off);
    SB();
    float uvt[1];
    col_terms<1>(c, false, l, w3a, w3b, b1a, b1a, uvt);
    USE(uvt[0]);
    if (v < 2) WAITV(0);
    SBAR();
    ST(10);
    {
      f4 acc[4][1];
      const float S[5] = {0.f, 0.f, 0.f, 0.f, 0.f};
      mlp1_loop<1, 1>(c, l, w3a, w3b, acc, w4, w4off, false, S STA);
      BAR();
      ST(12);
      mlp1_epi<1>(c, false, l, uvt, acc);
    }
    asm volatile("" ::: "memory");
    const rsrc_t rb2 = __builtin_amdgcn_make_buffer_rsrc((void*)b2_, 0, 1024, 0x00020000);
    const v4u b2v = __builtin_amdgcn_raw_buffer_load_b128(rb2, (l & 7) * 16, j * 128, 0);
    ST(18);
    WAITV_BAR(1);
    if (tid == 256) __hip_atomic_store(gfl + 2 * GSEAM + j * FSTR, tag, RLX_AGENT);
    ST(13);
    {
      f4 acc[4][2];
      v4u a4[2][4];
      const int ab = kh * 4096 + l * 16, abh = fresh(ab + 65536), abx = fresh(ab + 131072);
      SBAR();
      ST(14);
#pragma unroll
      for (int r = 0; r < 4; ++r) a4[0][r] = *(LAS v4u*)(L + ab + r * 1024);
#pragma unroll
      for (int n = 0; n < 22; ++n) {
        if ((n & 1) && n < 19) SBAR();
        if (n == 1) SL(26);
        if (n == 9) SL(27);
        if (n == 17) SL(28);
        SB();
        const int sp = 2 * (n + 1) >= 38 ? 2 * (n + 1) - 38 : 2 * (n + 1);
        if (n < 21 || kh == 0) {
#pragma unroll
          for (int r = 0; r < 4; ++r) {
            MAC(n == 0, acc[r][0], w4[2 * n], a4[n & 1][r]);
            SB();
            if (n < 21) a4[(n + 1) & 1][r] = *(LAS v4u*)(L + (sp < 16 ? ab : sp < 32 ? abh : abx) + (sp & 15) * 4096 + r * 1024);
            SB();
            MAC(n == 0, acc[r][1], w4[2 * n + 1], a4[n & 1][r]);
            SB();
          }
        }
      }
      MFMA_DONE();
      ST(15);
      red_write(c, l, nh, kh, acc);
    }
    meet4(L, L_W + 112, v, tag, l);
    epi4(c, tid - 256, b2v, x1);
    ST(16);
    SLF();
    __builtin_amdgcn_s_waitcnt(0x0F70);
    __builtin_amdgcn_endpgm();
  }
}

extern "C" void kernel_launch(void* const* d_in, const int* in_sizes, int n_in, void* d_out, int out_size, void* d_ws, size_t ws_size,
                              hipStream_t stream) {
  (void)hipFuncSetAttribute((const void*)fused_block, hipFuncAttributeMaxDynamicSharedMemorySize, LDS_BYTES);
  fused_block<<<256, 512, LDS_BYTES, stream>>>((const u16*)d_in[0], (const u16*)d_in[1], (const u16*)d_in[2], (const u16*)d_in[4],
                                               (const u16*)d_in[5], (const u16*)d_in[6], (const u16*)d_in[7], (const u16*)d_in[8],
                                               (const u16*)d_in[9], (u16*)d_out, (unsigned char*)d_ws);
}
```

```cpp
#include <hip/hip_runtime.h>
#include <stdint.h>

#define GAS __attribute__((address_space(1)))
#define LAS __attribute__((address_space(3)))
#define DI __device__ __forceinline__
typedef unsigned short u16;
typedef unsigned v4u __attribute__((ext_vector_type(4)));
typedef unsigned v2u __attribute__((ext_vector_type(2)));
typedef float f4 __attribute__((ext_vector_type(4)));
typedef float f2 __attribute__((ext_vector_type(2)));
typedef __bf16 bf2 __attribute__((ext_vector_type(2)));
typedef __bf16 bf8 __attribute__((ext_vector_type(8)));
typedef short s4 __attribute__((ext_vector_type(4)));
typedef GAS unsigned gu32;
typedef GAS unsigned long long gu64;
#define RLX_AGENT __ATOMIC_RELAXED, __HIP_MEMORY_SCOPE_AGENT

constexpr unsigned OFF_E = 0;
constexpr unsigned OFF_RFLAG = 768u << 10;
constexpr unsigned OFF_GFLAG = 800u << 10;
constexpr unsigned OFF_MODP = 16384;
constexpr unsigned OFF_STAT = 65536;
constexpr unsigned OFF_W3T = 1u << 20;
constexpr unsigned OFF_W4T = OFF_W3T + 1376 * 1024;
constexpr unsigned OFF_O = OFF_W4T + 1408 * 1024;
constexpr unsigned OFF_XA = OFF_O + 2048 * 1024;
constexpr unsigned OFF_H1 = OFF_XA + 2048 * 1024;
constexpr unsigned WS_USED = OFF_H1 + 32 * 43 * 4096;
constexpr unsigned OFF_STAMP = 64u << 20;

constexpr int L_R1 = 81920;
constexpr int L_RED = 65536;
constexpr int L_STAT = 102400;
constexpr int L_UV = 147456;
constexpr int L_RT = 143360;
constexpr int L_MODF = 155648;
constexpr int L_EC = 159744;
constexpr int L_MRED = 77824;
constexpr int L_SYNC = 79872;
constexpr int L_COEF = L_MODF;
constexpr int L_RR = L_COEF + 1024;
constexpr int L_W = L_MODF + 3584;
constexpr int LDS_BYTES = 163840;

constexpr int CODE_STEP = 1216;
constexpr int FSTR = 32, GSEAM = 32 * 8 * 32;
#define W4MODE 0
#define XFIRST 2
#define NUP 9
constexpr unsigned SPIN_CAP = 1u << 18;

#define ST(n)
#define SL(n)
#define SRT(n)
#define SLF()

DI float bflo(unsigned u) { return __uint_as_float(u << 16); }
DI float bfhi(unsigned u) { return __uint_as_float(u & 0xffff0000u); }
DI unsigned pk(float a, float b) { f2 v = {a, b}; return __builtin_bit_cast(unsigned, __builtin_convertvector(v, bf2)); }
DI f2 unpk(unsigned u) { return f2{bflo(u), bfhi(u)}; }
DI f2 fma2(f2 a, f2 b, f2 c) { return __builtin_elementwise_fma(a, b, c); }
DI unsigned pk2(f2 v) { return __builtin_bit_cast(unsigned, __builtin_convertvector(v, bf2)); }
DI f4 mfma32(v4u a, v4u b, f4 c) { return __builtin_amdgcn_mfma_f32_16x16x32_bf16(__builtin_bit_cast(bf8, a), __builtin_bit_cast(bf8, b), c, 0, 0, 0); }
template <bool FIRST> DI void mac(f4& c, v4u a, v4u b) {
  if constexpr (FIRST) asm volatile("v_mfma_f32_16x16x32_bf16 %0, %1, %2, 0" : "=&v"(c) : "v"(a), "v"(b));
  else asm volatile("v_mfma_f32_16x16x32_bf16 %0, %1, %2, %0" : "+v"(c) : "v"(a), "v"(b));
}
#define MAC(first, c, a, b) do { if (first) mac<true>(c, a, b); else mac<false>(c, a, b); } while (0)
#define MFMA_DONE() do { asm volatile("s_nop 15" ::: "memory"); __builtin_amdgcn_sched_barrier(0); } while (0)
DI f4 mfma16(v2u a, v2u b, f4 c) { return __builtin_amdgcn_mfma_f32_16x16x16bf16_1k(__builtin_bit_cast(s4, a), __builtin_bit_cast(s4, b), c, 0, 0, 0); }
template <int CTRL> DI float dpp(float x) { return __builtin_bit_cast(float, __builtin_amdgcn_update_dpp(0, __builtin_bit_cast(int, x), CTRL, 0xf, 0xf, true)); }
DI float swap16_sum(float x) { auto r = __builtin_amdgcn_permlane16_swap(__float_as_uint(x), __float_as_uint(x), false, false); return __uint_as_float(r[0]) + __uint_as_float(r[1]); }
DI float swap32_sum(float x) { auto r = __builtin_amdgcn_permlane32_swap(__float_as_uint(x), __float_as_uint(x), false, false); return __uint_as_float(r[0]) + __uint_as_float(r[1]); }
DI float swap16_max(float x) { auto r = __builtin_amdgcn_permlane16_swap(__float_as_uint(x), __float_as_uint(x), false, false); return fmaxf(__uint_as_float(r[0]), __uint_as_float(r[1])); }
DI float swap32_max(float x) { auto r = __builtin_amdgcn_permlane32_swap(__float_as_uint(x), __float_as_uint(x), false, false); return fmaxf(__uint_as_float(r[0]), __uint_as_float(r[1])); }
DI float max2(float a, float b) { return __builtin_amdgcn_fmed3f(a, b, __builtin_inff()); }
template <bool MAX> DI float gather4(const float (&x)[4]) {
  auto a = __builtin_amdgcn_permlane16_swap(__float_as_uint(x[0]), __float_as_uint(x[1]), false, false);
  auto b = __builtin_amdgcn_permlane16_swap(__float_as_uint(x[2]), __float_as_uint(x[3]), false, false);
  const float u = MAX ? max2(__uint_as_float(a[0]), __uint_as_float(a[1])) : __uint_as_float(a[0]) + __uint_as_float(a[1]);
  const float v = MAX ? max2(__uint_as_float(b[0]), __uint_as_float(b[1])) : __uint_as_float(b[0]) + __uint_as_float(b[1]);
  auto c = __builtin_amdgcn_permlane32_swap(__float_as_uint(u), __float_as_uint(v), false, false);
  return MAX ? max2(__uint_as_float(c[0]), __uint_as_float(c[1])) : __uint_as_float(c[0]) + __uint_as_float(c[1]);
}
DI void spread4(float t, float (&x)[4]) {
  auto c = __builtin_amdgcn_permlane32_swap(__float_as_uint(t), __float_as_uint(t), false, false);
  auto a = __builtin_amdgcn_permlane16_swap(c[0], c[0], false, false);
  auto b = __builtin_amdgcn_permlane16_swap(c[1], c[1], false, false);
  x[0] = __uint_as_float(a[0]); x[1] = __uint_as_float(a[1]); x[2] = __uint_as_float(b[0]); x[3] = __uint_as_float(b[1]);
}
DI float sum8(float x) { x += dpp<0xB1>(x); x += dpp<0x4E>(x); x += dpp<0x141>(x); return x; }
DI float sum16(float x) { x = sum8(x); x += dpp<0x140>(x); return x; }

#define BAR() asm volatile("s_waitcnt lgkmcnt(0)\n\ts_barrier" ::: "memory")
#define WAITV(n) do { asm volatile("s_waitcnt vmcnt(%0)" ::"i"(n) : "memory"); if ((n) == 0) __builtin_amdgcn_s_waitcnt(0x0F70); } while (0)
#define WAITV_BAR(n) do { asm volatile("s_waitcnt vmcnt(%0) lgkmcnt(0)\n\ts_barrier" ::"i"(n) : "memory"); if ((n) == 0) __builtin_amdgcn_s_waitcnt(0x0F70); } while (0)
#define SBAR() asm volatile("s_barrier" ::: "memory")
#define USE(x) asm volatile("" ::"v"(x))

template <int N> DI void dma(v4u rs, unsigned voff, unsigned soff, unsigned lds) {
  unsigned keep;
  if constexpr (N == 1)
    asm volatile("s_mov_b32 %0, m0\n\ts_mov_b32 m0, %2\n\ts_nop 0\n\t"
                 "buffer_load_dwordx4 %1, %3, %4 offen sc1 lds\n\t"
                 "s_mov_b32 m0, %0" : "=&s"(keep) : "v"(voff), "s"(lds), "s"(rs), "s"(soff) : "memory");
  else if constexpr (N == 2)
    asm volatile("s_mov_b32 %0, m0\n\ts_mov_b32 m0, %2\n\ts_nop 0\n\t"
                 "buffer_load_dwordx4 %1, %3, %4 offen sc1 lds\n\t"
                 "buffer_load_dwordx4 %1, %3, %4 offen offset:1024 sc1 lds\n\t"
                 "s_mov_b32 m0, %0" : "=&s"(keep) : "v"(voff), "s"(lds), "s"(rs), "s"(soff) : "memory");
  else
    asm volatile("s_mov_b32 %0, m0\n\ts_mov_b32 m0, %2\n\ts_nop 0\n\t"
                 "buffer_load_dwordx4 %1, %3, %4 offen sc1 lds\n\t"
                 "buffer_load_dwordx4 %1, %3, %4 offen offset:1024 sc1 lds\n\t"
                 "buffer_load_dwordx4 %1, %3, %4 offen offset:2048 sc1 lds\n\t"
                 "buffer_load_dwordx4 %1, %3, %4 offen offset:3072 sc1 lds\n\t"
                 "s_mov_b32 m0, %0" : "=&s"(keep) : "v"(voff), "s"(lds), "s"(rs), "s"(soff) : "memory");
}
DI void dma3(v4u rs, unsigned voff, unsigned soff, unsigned lds) {
  unsigned keep;
  asm volatile("s_mov_b32 %0, m0\n\ts_mov_b32 m0, %2\n\ts_nop 0\n\t"
               "buffer_load_dwordx4 %1, %3, %4 offen lds\n\t"
               "buffer_load_dwordx4 %1, %3, %4 offen offset:1024 lds\n\t"
               "buffer_load_dwordx4 %1, %3, %4 offen offset:2048 lds\n\t"
               "s_mov_b32 m0, %0" : "=&s"(keep) : "v"(voff), "s"(lds), "s"(rs), "s"(soff) : "memory");
}
DI v4u trfrag(LAS char* p) {
  v2u lo = __builtin_bit_cast(v2u, __builtin_amdgcn_ds_read_tr16_b64_v4i16((LAS s4*)p));
  v2u hi = __builtin_bit_cast(v2u, __builtin_amdgcn_ds_read_tr16_b64_v4i16((LAS s4*)(p + 512)));
  return v4u{lo.x, lo.y, hi.x, hi.y};
}
DI int lane_id() { int r; asm volatile("v_mbcnt_lo_u32_b32 %0, -1, 0\n\tv_mbcnt_hi_u32_b32 %0, -1, %0" : "=v"(r)); return r; }
DI int fresh(int x) { asm volatile("" : "+v"(x)); return x; }
#define SB() __builtin_amdgcn_sched_barrier(0)
DI void dma_nt(v4u rs, unsigned voff, unsigned soff, unsigned lds) {
  unsigned keep;
  asm volatile("s_mov_b32 %0, m0\n\ts_mov_b32 m0, %2\n\ts_nop 0\n\tbuffer_load_dwordx4 %1, %3, %4 offen nt lds\n\ts_mov_b32 m0, %0"
               : "=&s"(keep) : "v"(voff), "s"(lds), "s"(rs), "s"(soff) : "memory");
}
DI void gdma4(const GAS void* p, unsigned lds) {
  unsigned keep;
  asm volatile("s_mov_b32 %0, m0\n\ts_mov_b32 m0, %2\n\ts_nop 0\n\tglobal_load_lds_dword %1, off\n\ts_mov_b32 m0, %0"
               : "=&s"(keep) : "v"(p), "s"(lds) : "memory");
}
typedef __amdgpu_buffer_rsrc_t rsrc_t;
DI v4u ldf(rsrc_t rs, unsigned voff, unsigned soff) { return __builtin_amdgcn_raw_buffer_load_b128(rs, voff, soff, 16); }
#define LDW(n, base) ldw_rt(c.rs, l * 16, base, n)
DI v4u ldw_rt(rsrc_t rs, unsigned voff, unsigned base, int n) { return __builtin_amdgcn_raw_buffer_load_b128(rs, voff + (n & 3) * 1024, base + (n >> 2) * 4096, 16); }
DI v4u ld_rflags(rsrc_t rs, int l) { return ldf(rs, (l >> 1) * 128 + (l & 1) * 16, OFF_RFLAG); }
DI void stf(rsrc_t rs, v4u v, unsigned voff, unsigned soff) { __builtin_amdgcn_raw_buffer_store_b128(v, rs, voff + soff, 0, 16); }

DI void meet4(LAS char* L, int at, int me, unsigned tag, int l) {
  volatile LAS unsigned* f = (volatile LAS unsigned*)(L + at);
  f[me] = tag;
  _Pragma("nounroll") for (unsigned s = 0; s < 16 * SPIN_CAP; ++s)
    if (__all(f[l & 3] == tag)) break;
  asm volatile("" ::: "memory");
}
DI void post(LAS char* L, int at, unsigned tag) { *(volatile LAS unsigned*)(L + at) = tag; }
DI void wait4(LAS char* L, int at, unsigned tag, int l) {
  volatile LAS unsigned* f = (volatile LAS unsigned*)(L + at);
  _Pragma("nounroll") for (unsigned s = 0; s < 16 * SPIN_CAP; ++s) {
    if (__all(f[l & 3] == tag)) break;
    __builtin_amdgcn_s_sleep(1);
  }
  asm volatile("" ::: "memory");
}
DI void lds_wait(LAS char* L, int at, unsigned tag) {
  volatile LAS unsigned* f = (volatile LAS unsigned*)(L + at);
  _Pragma("nounroll") for (unsigned s = 0; s < 16 * SPIN_CAP; ++s) {
    if (*f == tag) break;
    __builtin_amdgcn_s_sleep(1);
  }
  asm volatile("" ::: "memory");
}
template <int DELAY> DI void poll8(gu32* fl, unsigned tag, int l) {
  __builtin_amdgcn_s_sleep(DELAY);
  _Pragma("nounroll") for (unsigned s = 0; s < SPIN_CAP; ++s) {
    unsigned f = __hip_atomic_load(fl + (l & 7) * FSTR, RLX_AGENT);
    if (__all(f == tag)) break;
    __builtin_amdgcn_s_sleep(4);
  }
  __builtin_amdgcn_fence(__ATOMIC_ACQUIRE, "wavefront");
}

DI unsigned gelu2(f2 a, f2 k1, f2 k2, f2 ir) {
  const f2 t = a * fma2(a * a, k2, k1);
  const f2 d = fma2(f2{__builtin_amdgcn_exp2f(t.x), __builtin_amdgcn_exp2f(t.y)}, ir, ir);
  return pk2(a * f2{__builtin_amdgcn_rcpf(d.x), __builtin_amdgcn_rcpf(d.y)});
}

DI void mod_get(rsrc_t rs, unsigned voff, v4u (&v)[4]) {
#pragma unroll
  for (int k = 0; k < 4; ++k) v[k] = ldf(rs, voff, OFF_MODP + k * 12288);
}
DI v2u mod_sum(rsrc_t rs, unsigned voff, v4u (&v)[4], unsigned tag, float plus) {
  const unsigned tb = tag & 0xffu;
  _Pragma("nounroll") for (unsigned s = 0; s < SPIN_CAP; ++s) {
#pragma unroll
    for (int k = 0; k < 4; ++k) v[k] ^= tb;
    const v4u d = (v[0] | v[1]) | (v[2] | v[3]);
    if (__all(((d.x | d.y) | (d.z | d.w)) << 24 == 0u)) break;
    __builtin_amdgcn_s_sleep(1);
    asm volatile("" ::: "memory");
    mod_get(rs, voff, v);
    WAITV(0);
  }
  const f4 s = (__builtin_bit_cast(f4, v[0]) + __builtin_bit_cast(f4, v[1])) + (__builtin_bit_cast(f4, v[2]) + __builtin_bit_cast(f4, v[3]));
  const f2 p2 = {plus, plus};
  return v2u{pk2(unpk(pk(s.x, s.y)) + p2), pk2(unpk(pk(s.z, s.w)) + p2)};
}
DI f4 widen(v2u p) { return f4{bflo(p.x), bfhi(p.x), bflo(p.y), bfhi(p.y)}; }

#define STP
#define STA

DI v4u mkrs(const void* p, unsigned bytes) { return v4u{(unsigned)(uintptr_t)p, (unsigned)((uintptr_t)p >> 32) & 0xffffu, bytes, 0x00020000u}; }
DI void mod_loads(const u16* c_, const u16* wmod_, bool on, int mcb, int mks, int v, int l, v2u& cw, v4u (&wm)[4]) {
  const int r0 = 128 * mks + 32 * v + 4 * (l >> 3);
  const v4u rc = mkrs(c_, on ? 1024u : 0u), rw = mkrs(wmod_, on ? 512u * 6144u : 0u);
  asm volatile("buffer_load_dwordx2 %0, %1, %2, 0 offen" : "=v"(cw) : "v"(r0 * 2), "s"(rc));
#pragma unroll
  for (int e = 0; e < 4; ++e) asm volatile("buffer_load_dwordx4 %0, %1, %2, %3 offen nt" : "=v"(wm[e]) : "v"(r0 * 6144 + 128 * mcb + 16 * (l & 7)), "s"(rw), "s"(e * 6144));
}
#define MOD_WAIT(n) asm volatile("s_waitcnt vmcnt(%5)" : "+v"(cw), "+v"(wm[0]), "+v"(wm[1]), "+v"(wm[2]), "+v"(wm[3]) : "i"(n) : "memory")
DI v4u mod_job(LAS char* L, int red, int v, int l, unsigned tag, v2u cw, const v4u (&wm)[4]) {
  v4u res = {0u, 0u, 0u, 0u};
  f2 a2[4];
#pragma unroll
  for (int e = 0; e < 4; ++e) {
    const float cv = (e & 1) ? bfhi(cw[e >> 1]) : bflo(cw[e >> 1]);
    float sv = cv * __builtin_amdgcn_rcpf(1.0f + __builtin_amdgcn_exp2f(-1.4426950409f * cv));
    sv = bflo(pk(sv, sv));
    const f2 s2 = {sv, sv};
#pragma unroll
    for (int k = 0; k < 4; ++k) a2[k] = e ? fma2(s2, unpk(wm[e][k]), a2[k]) : s2 * unpk(wm[e][k]);
  }
  float a[4];
#pragma unroll
  for (int k = 0; k < 4; ++k) {
    auto r = __builtin_amdgcn_permlane32_swap(__float_as_uint(a2[k >> 1][k & 1]), __float_as_uint(a2[2 + (k >> 1)][k & 1]), false, false);
    a[k] = __uint_as_float(r[0]) + __uint_as_float(r[1]);
  }
#pragma unroll
  for (int k = 0; k < 2; ++k) {
    auto r = __builtin_amdgcn_permlane16_swap(__float_as_uint(a[k]), __float_as_uint(a[k + 2]), false, false);
    a[k] = __uint_as_float(r[0]) + __uint_as_float(r[1]);
    a[k] += dpp<0x128>(a[k]);
  }
  if (!(l & 8)) *(LAS f2*)(L + red + v * 256 + (8 * (l & 7) + 2 * ((l >> 4) & 1) + 4 * (l >> 5)) * 4) = f2{a[0], a[1]};
  asm volatile("s_waitcnt lgkmcnt(0)" ::: "memory");
  volatile LAS unsigned* mf = (volatile LAS unsigned*)(L + red + 1024);
  if (v < 3) mf[v] = tag;
  else {
    _Pragma("nounroll") for (unsigned s = 0; s < SPIN_CAP; ++s)
      if (__all(mf[l < 3 ? l : 0] == tag)) break;
    asm volatile("" ::: "memory");
    f4 s = *(LAS f4*)(L + red + (l & 15) * 16);
#pragma unroll
    for (int ww = 1; ww < 4; ++ww) s += *(LAS f4*)(L + red + ww * 256 + (l & 15) * 16);
    res = (__builtin_bit_cast(v4u, s) & 0xffffff00u) | (tag & 0xffu);
  }
  return res;
}
DI void mod_publish(rsrc_t rs, int mcb, int mks, int v, int l, v4u res) {
  if (v == 3 && l < 16) stf(rs, res, l * 16, OFF_MODP + mks * 12288 + mcb * 256);
}

struct Ctx {
  rsrc_t rs; v4u rsv; LAS char* L; unsigned Lb, tag; int g, j, w, t03, tile0; gu32* gfl;
  const u16 *x_, *b1_, *b2_; u16* out_; gu32* epoch;
};

template <int T> DI void col_terms(const Ctx& c, bool two, int l, const v4u (&w3a)[16], const v4u (&w3b)[16], float b1a, float b1b, float (&uvt)[T]) {
  l = fresh(l);
  const int i = l & 15, q = l >> 4, ty = i >> 2;
  const int base = L_UV + (ty < 2 ? ty * 2048 + c.j * 128 : 2048 + ty * 1024) + q * 16;
  f4 a[2][T];
  v4u am[16];
#pragma unroll
  for (int s = 0; s < 16; ++s) am[s] = *(LAS v4u*)(c.L + base + s * 64);
  SB();
#pragma unroll
  for (int s = 0; s < 16; ++s) MAC(s < 2, a[s & 1][0], am[s], w3a[s]);
  if (T == 2 && two) {
#pragma unroll
    for (int s = 0; s < 16; ++s) MAC(s < 2, a[s & 1][T - 1], am[s], w3b[s]);
  }
  MFMA_DONE();
  uvt[0] = a[0][0].x + a[1][0].x + (q == 3 ? b1a : 0.f);
  if (T == 2) uvt[T - 1] = a[0][T - 1].x + a[1][T - 1].x + (q == 3 ? b1b : 0.f);
}

template <int T, int KIND>
DI void mlp1_loop(const Ctx& c, int l, const v4u (&w3a)[16], const v4u (&w3b)[16], f4 (&acc)[4][T], v4u (&w4)[44], unsigned w4off, bool two,
                  const float (&S)[5] STP) {
  LAS char* L = c.L;
  const int tid = c.w * 64 + l;
  auto xa_slot = [&](int slot) { dma<4>(c.rsv, l * 16, OFF_XA + (c.g * 16 + ((slot + 2 * c.j) & 15)) * 4096, c.Lb + slot * 4096); };
  v4u a[2][4];
#pragma unroll
  for (int r = 0; r < 4; ++r) a[0][r] = *(LAS v4u*)(L + r * 1024 + l * 16);
#pragma unroll
  for (int s = 0; s < 16; ++s) {
    if (s == 1 || s == 5 || s == 9) {
      if (KIND == 2 && c.w == 3) {
        if (s == 1) WAITV(16);
        if (s == 5) WAITV(24);
        if (s == 9) WAITV(16);
      }
      SBAR();
      SL(22 + s / 4);
      if (KIND == 2 && c.w == 3) {
        if (s == 1) {
#pragma unroll
          for (int k = 10; k < 16; ++k) xa_slot(k);
        }
        if (s == 5) {
#pragma unroll
          for (int p = 0; p < 4; ++p) dma<4>(c.rsv, l * 16, OFF_STAT + c.g * 16384 + p * 4096, c.Lb + L_STAT + p * 4096);
        }
        if (s == 9) {
          float sx, sxx, sp, spp, spa, sps;
          _Pragma("nounroll") for (unsigned it = 0; it < SPIN_CAP; ++it) {
            WAITV(0);
            v4u a4[8]; v2u b2[8];
            unsigned d = 0;
#pragma unroll
            for (int p = 0; p < 8; ++p) {
              a4[p] = *(LAS v4u*)(L + L_STAT + p * 2048 + l * 16) ^ (c.tag & 0xffu);
              b2[p] = *(LAS v2u*)(L + L_STAT + p * 2048 + 1024 + l * 16) ^ (c.tag & 0xffu);
              d |= (a4[p].x | a4[p].y) | (a4[p].z | a4[p].w) | (b2[p].x | b2[p].y);
            }
            f4 ta = __builtin_bit_cast(f4, a4[0]); f2 tb = __builtin_bit_cast(f2, b2[0]);
#pragma unroll
            for (int p = 1; p < 8; ++p) { ta += __builtin_bit_cast(f4, a4[p]); tb += __builtin_bit_cast(f2, b2[p]); }
            sx = ta.x; sxx = ta.y; sp = ta.z; spp = ta.w; spa = tb.x; sps = tb.y;
            if (__all(d << 24 == 0u)) break;
            asm volatile("s_waitcnt lgkmcnt(0)" ::: "memory");
#pragma unroll
            for (int p = 0; p < 4; ++p) dma<4>(c.rsv, l * 16, OFF_STAT + c.g * 16384 + p * 4096, c.Lb + L_STAT + p * 4096);
          }
          const float inv = 1.0f / 512.0f;
          const float m = sx * inv, var = fmaxf(sxx * inv - m * m, 0.f);
          const float sd = __builtin_amdgcn_sqrtf(var + 1e-5f), r = __builtin_amdgcn_rcpf(sd);
          const float m2 = (r * (sp - m * S[0]) + S[1]) * inv;
          const float e2 = (r * r * (spp - 2.f * m * spa + m * m * S[2]) + 2.f * r * (sps - m * S[3]) + S[4]) * inv;
          const float sd2 = __builtin_amdgcn_sqrtf(fmaxf(e2 - m2 * m2, 0.f) + 1e-5f);
          const float R = r * __builtin_amdgcn_rcpf(sd2), k1 = -2.302208198f * R;
          *(LAS f4*)(L + L_COEF + l * 16) = f4{-m, sd, -m2 * sd, sd * sd2};
          *(LAS f4*)(L + L_RR + l * 16) = f4{k1, 0.044715f * k1 * R * R, sd * sd2, 0.f};
        }
      }
    }
    SB();
#pragma unroll
    for (int r = 0; r < 4; ++r) {
      MAC(s == 0, acc[r][0], w3a[s], a[s & 1][r]);
      SB();
      if (s < 15) a[(s + 1) & 1][r] = *(LAS v4u*)(L + ((s + 1) * 4 + r) * 1024 + l * 16);
      SB();
    }
    if (T == 2 && two) {
#pragma unroll
      for (int r = 0; r < 4; ++r) MAC(s == 0, acc[r][T - 1], w3b[s], a[s & 1][r]);
      SB();
    }
    if (KIND == 1) {
      const int n0 = s < 4 ? 2 * s : 3 * s - 4;
      w4[n0] = LDW(n0, w4off);
      w4[n0 + 1] = LDW(n0 + 1, w4off);
      if (s >= 4) w4[n0 + 2] = LDW(n0 + 2, w4off);
      SB();
    }
  }
  MFMA_DONE();
}

template <int T> DI void mlp1_epi(const Ctx& c, bool two, int l, const float (&uvt)[T], f4 (&acc)[4][T]) {
  l = fresh(l);
  const int i = l & 15, q = l >> 4;
  float cf[4]; f4 rr[4];
#pragma unroll
  for (int r = 0; r < 4; ++r) {
    cf[r] = *(LAS float*)(c.L + L_COEF + (r * 16 + i) * 16 + q * 4);
    rr[r] = *(LAS f4*)(c.L + L_RR + (r * 16 + i) * 16);
  }
#pragma unroll
  for (int t = 0; t < T; ++t) {
    if (t && !two) break;
#pragma unroll
    for (int r = 0; r < 4; ++r) acc[r][t] = __builtin_amdgcn_mfma_f32_16x16x4f32(uvt[t], cf[r], acc[r][t], 0, 0, 0);
  }
  SB();
#pragma unroll
  for (int t = 0; t < T; ++t) {
    if (t && !two) break;
    const int tl = c.tile0 + t;
#pragma unroll
    for (int pp = 0; pp < 2; ++pp) {
      unsigned h[2][2];
#pragma unroll
      for (int r2 = 0; r2 < 2; ++r2) {
        const int r = 2 * pp + r2;
        const f2 k1 = {rr[r].x, rr[r].x}, k2 = {rr[r].y, rr[r].y}, ir = {rr[r].z, rr[r].z};
        h[r2][0] = gelu2(f2{acc[r][t].x, acc[r][t].y}, k1, k2, ir);
        h[r2][1] = gelu2(f2{acc[r][t].z, acc[r][t].w}, k1, k2, ir);
      }
      auto s0 = __builtin_amdgcn_permlane16_swap(h[0][0], h[1][0], false, false);
      auto s1 = __builtin_amdgcn_permlane16_swap(h[0][1], h[1][1], false, false);
      stf(c.rs, v4u{s0[0], s1[0], s0[1], s1[1]}, (q & 1) * 1024 + ((q >> 1) * 16 + i) * 16,
          OFF_H1 + ((c.g * 43 + (tl >> 1)) * 4 + 2 * pp) * 1024 + (tl & 1) * 512);
      SB();
    }
  }
}

DI void dma_in(v4u rs, unsigned voff, unsigned soff, unsigned lds) {
  unsigned keep;
  asm volatile("s_mov_b32 %0, m0\n\ts_mov_b32 m0, %2\n\ts_nop 0\n\tbuffer_load_dwordx4 %1, %3, %4 offen lds\n\ts_mov_b32 m0, %0"
               : "=&s"(keep) : "v"(voff), "s"(lds), "s"(rs), "s"(soff) : "memory");
}
DI void sum8x6(float& a, float& b, float& c, float& d, float& e, float& f) {
#define S6(ctl) "v_add_f32_dpp %0, %0, %0 " ctl "\n\tv_add_f32_dpp %1, %1, %1 " ctl "\n\tv_add_f32_dpp %2, %2, %2 " ctl "\n\t" \
                "v_add_f32_dpp %3, %3, %3 " ctl "\n\tv_add_f32_dpp %4, %4, %4 " ctl "\n\tv_add_f32_dpp %5, %5, %5 " ctl "\n\t"
  asm volatile("s_nop 1\n\t" S6("quad_perm:[1,0,3,2] row_mask:0xf bank_mask:0xf") S6("quad_perm:[2,3,0,1] row_mask:0xf bank_mask:0xf")
               S6("row_half_mirror row_mask:0xf bank_mask:0xf") "s_nop 0" : "+v"(a), "+v"(b), "+v"(c), "+v"(d), "+v"(e), "+v"(f));
#undef S6
}
DI void red_write(const Ctx& c, int l, int nh, int kh, const f4 (&acc)[4][2]) {
  l = fresh(l);
  const int rb = L_RED + kh * 17408 + (l & 15) * 272 + nh * 128 + (l >> 4) * 16;
#pragma unroll
  for (int r = 0; r < 4; ++r)
#pragma unroll
    for (int t = 0; t < 2; ++t) *(LAS f4*)(c.L + rb + r * 16 * 272 + t * 64) = acc[r][t];
  asm volatile("s_waitcnt lgkmcnt(0)" ::: "memory");
}
DI void red_read(const Ctx& c, int tok, int f8, f2 (&y)[4]) {
  const int ra = L_RED + tok * 272 + f8 * 32;
  const f4 y0 = *(LAS f4*)(c.L + ra) + *(LAS f4*)(c.L + ra + 17408), y1 = *(LAS f4*)(c.L + ra + 16) + *(LAS f4*)(c.L + ra + 17408 + 16);
  y[0] = f2{y0.x, y0.y}; y[1] = f2{y0.z, y0.w}; y[2] = f2{y1.x, y1.y}; y[3] = f2{y1.z, y1.w};
}
DI void ec_read(const Ctx& c, int sec, int f8, f2 (&y)[4]) {
  const f4 y0 = *(LAS f4*)(c.L + L_EC + sec * 256 + f8 * 32), y1 = *(LAS f4*)(c.L + L_EC + sec * 256 + f8 * 32 + 16);
  y[0] = f2{y0.x, y0.y}; y[1] = f2{y0.z, y0.w}; y[2] = f2{y1.x, y1.y}; y[3] = f2{y1.z, y1.w};
}
DI void epi2(const Ctx& c, int t, f2 (&x1)[2][4]) {
  t = fresh(t);
  const int tk = t >> 3, f8 = t & 7;
  f2 ga[4], a2[4], sb[4], p[2][4];
  ec_read(c, 0, f8, ga); ec_read(c, 1, f8, a2); ec_read(c, 2, f8, sb);
#pragma unroll
  for (int k = 0; k < 2; ++k) {
    f2 y[4];
    red_read(c, tk + 32 * k, f8, y);
#pragma unroll
    for (int e = 0; e < 4; ++e) { x1[k][e] = fma2(ga[e], y[e], x1[k][e]); p[k][e] = x1[k][e] * a2[e]; }
    stf(c.rs, v4u{pk2(p[k][0]), pk2(p[k][1]), pk2(p[k][2]), pk2(p[k][3])},
        ((f8 >> 2) * 4 + (tk >> 4) + 2 * k) * 1024 + ((f8 & 3) * 16 + (tk & 15)) * 16, OFF_XA + (c.g * 16 + 2 * c.j) * 4096);
  }
  SB();
#pragma unroll
  for (int k = 0; k < 2; ++k) {
    f2 vx = x1[k][0], vxx = x1[k][0] * x1[k][0], vp = p[k][0], vpp = p[k][0] * p[k][0], vpa = p[k][0] * a2[0], vps = p[k][0] * sb[0];
#pragma unroll
    for (int e = 1; e < 4; ++e) {
      vx += x1[k][e]; vxx = fma2(x1[k][e], x1[k][e], vxx); vp += p[k][e]; vpp = fma2(p[k][e], p[k][e], vpp);
      vpa = fma2(p[k][e], a2[e], vpa); vps = fma2(p[k][e], sb[e], vps);
    }
    float sx = vx.x + vx.y, sxx = vxx.x + vxx.y, sp = vp.x + vp.y, spp = vpp.x + vpp.y, spa = vpa.x + vpa.y, sps = vps.x + vps.y;
    sum8x6(sx, sxx, sp, spp, spa, sps);
    const v4u sw = (__builtin_bit_cast(v4u, f8 == 0 ? f4{sx, sxx, sp, spp} : f4{spa, sps, spa, sps}) & 0xffffff00u) | (c.tag & 0xffu);
    if (f8 < 2) stf(c.rs, sw, (tk + 32 * k) * 16 + f8 * 1024, OFF_STAT + (c.g * 8 + c.j) * 2048);
  }
}
DI void epi4(const Ctx& c, int t, v4u b2v, const f2 (&x1)[2][4]) {
  t = fresh(t);
  const int tk = t >> 3, f8 = t & 7;
  const rsrc_t ro = __builtin_amdgcn_make_buffer_rsrc((void*)c.out_, 0, 2048 * 512 * 2, 0x00020000);
  f2 gb[4];
  ec_read(c, 3, f8, gb);
#pragma unroll
  for (int k = 0; k < 2; ++k) {
    f2 y[4];
    red_read(c, tk + 32 * k, f8, y);
    v4u o;
#pragma unroll
    for (int e = 0; e < 4; ++e) o[e] = pk2(fma2(gb[e], y[e] + unpk(b2v[e]), x1[k][e]));
    __builtin_amdgcn_raw_buffer_store_b128(o, ro, (tk + 32 * k) * 1024 + f8 * 16 + c.g * 65536 + c.j * 128, 0, 0);
  }
}

DI void tail_c(const Ctx& c, bool two, int tid STP) {
  tid = fresh(tid);
  const int l = tid & 63, i = l & 15, w = c.w, g = c.g, j = c.j;
  LAS char* L = c.L;
  const unsigned w3off = OFF_W3T + c.tile0 * 16384;
  v4u w3a[16], w3b[16], w4[44];
  WAITV(0);
  meet4(L, L_W + 32, w, c.tag, l);
  if (tid == 128) __hip_atomic_store(c.gfl + j * FSTR, c.tag, RLX_AGENT);
  ST(11);
  auto o_slot = [&](int slot) { dma<4>(c.rsv, l * 16, OFF_O + (g * 16 + ((slot + 2 * j) & 15)) * 4096, c.Lb + slot * 4096); };
  if (w < 2) o_slot(w);
  const float b1a = bflo(((const GAS u16*)c.b1_)[c.tile0 * 16 + i]), b1b = bflo(((const GAS u16*)c.b1_)[(c.tile0 + (two ? 1 : 0)) * 16 + i]);
  if (w == 3) poll8<14>(c.gfl, c.tag, l);
  if (w == 2) {
    __builtin_amdgcn_s_sleep(14);
    _Pragma("nounroll") for (unsigned s = 0; s < SPIN_CAP; ++s) {
      const v4u rf = ld_rflags(c.rs, l);
      if (__all(rf.x == c.tag && rf.y == c.tag && rf.z == c.tag && rf.w == c.tag)) break;
      __builtin_amdgcn_s_sleep(8);
      asm volatile("" ::: "memory");
    }
    if (g == 0 && j == 0 && l == 0) *c.epoch = c.tag;
  }
  WAITV(0);
  ST(17);
  SBAR();
  ST(8);
  o_slot(2 + w); o_slot(6 + w);
  v4u m1[4], m2[4];
  const unsigned o1 = (1536 + 256 * w + 4 * l) * 4, o2 = (((l & 16) ? 2560 : 1024) + 64 * j + 4 * i) * 4;
  WAITV_BAR(4); SL(20);
  o_slot(10 + w); o_slot(14 + (w & 1));
  WAITV_BAR(8);
  mod_get(c.rs, o1, m1);
  mod_get(c.rs, o2, m2);
  WAITV_BAR(12);
#pragma unroll
  for (int n = 0; n < 4; ++n) w3a[n] = LDW(n, w3off);
  WAITV_BAR(12); SL(21);
  {
    const v2u p = mod_sum(c.rs, o1, m1, c.tag, w < 2 ? 0.f : 1.f);
    const int u = L_UV + (w < 2 ? 2048 : 0) + (w & 1) * 512 + l * 8;
    *(LAS v2u*)(L + u) = p; *(LAS v2u*)(L + u + 1024) = p;
    if ((w & 1) * 4 + (l >> 4) == j) *(LAS f4*)(L + L_EC + (w < 2 ? 512 : 256) + i * 16) = widen(p);
    const v2u p2 = mod_sum(c.rs, o2, m2, c.tag, 0.f);
    if (w == 3 && l < 32) *(LAS f4*)(L + L_EC + ((l & 16) ? 768 : 0) + i * 16) = widen(p2);
    if (w < 2) { const unsigned f = w ? 0u : 0x3f803f80u; *(LAS v4u*)(L + L_UV + 4096 + w * 1024 + l * 16) = v4u{f, f, f, f}; }
    asm volatile("s_waitcnt lgkmcnt(0)" ::: "memory");
    post(L, L_W + 48 + 4 * w, c.tag);
  }
#pragma unroll
  for (int n = 4; n < 16; ++n) w3a[n] = LDW(n, w3off);
  if (two) {
#pragma unroll
    for (int n = 0; n < 16; ++n) w3b[n] = LDW(16 + n, w3off);
  }
  wait4(L, L_W + 48, c.tag, l);
  float S[5] = {0.f, 0.f, 0.f, 0.f, 0.f};
  if (w == 3) {
    const v4u av = *(LAS v4u*)(L + L_UV + l * 16), sv = *(LAS v4u*)(L + L_UV + 2048 + l * 16);
#pragma unroll
    for (int e = 0; e < 4; ++e) {
      const float a0 = bflo(av[e]), a1 = bfhi(av[e]), s0 = bflo(sv[e]), s1 = bfhi(sv[e]);
      S[0] += a0 + a1; S[1] += s0 + s1; S[2] += a0 * a0 + a1 * a1; S[3] += a0 * s0 + a1 * s1; S[4] += s0 * s0 + s1 * s1;
    }
#pragma unroll
    for (int e = 0; e < 5; ++e) S[e] = swap32_sum(swap16_sum(sum16(S[e])));
  }
  WAITV(0);
  ST(9);
  float uvt[2];
  col_terms<2>(c, two, l, w3a, w3b, b1a, b1b, uvt);
  USE(uvt[0]); USE(uvt[1]);
  if (w == 3) {
    wait4(L, L_W + 96, c.tag, l);
    poll8<14>(c.gfl + GSEAM, c.tag, l);
    ST(15);
#pragma unroll
    for (int k = 2; k < 10; ++k) dma<4>(c.rsv, l * 16, OFF_XA + (g * 16 + ((k + 2 * j) & 15)) * 4096, c.Lb + k * 4096);
  }
  SBAR();
  ST(10);
  f4 acc[4][2];
  mlp1_loop<2, 2>(c, l, w3a, w3b, acc, w4, 0, two, S STA);
  BAR();
  ST(12);
  mlp1_epi<2>(c, two, l, uvt, acc);
  ST(18);
  WAITV_BAR(0);
  ST(13);
  const int rot4 = (c.t03 + 1) >> 1;
  auto h_slot = [&](int sg) {
    int k = rot4 + sg; if (k >= 43) k -= 43;
    dma<4>(c.rsv, l * 16, OFF_H1 + (g * 43 + k) * 4096, c.Lb + (sg >= 38 ? sg - 38 : sg) * 4096);
  };
  if (w == 3) poll8<14>(c.gfl + 2 * GSEAM, c.tag, l);
  else { h_slot(w); if (w == 0) h_slot(3); }
  WAITV_BAR(0);
  ST(14);
  h_slot(4 + w); h_slot(8 + w); h_slot(12 + w);
#pragma unroll
  for (int m = 1; m < 10; ++m) {
    if (m == 1 || m == 7) WAITV_BAR(8);
    else if (m < 7) WAITV_BAR(12);
    else if (m == 8) WAITV_BAR(4);
    else WAITV_BAR(0);
    if (m == 1) SL(26);
    if (m == 5) SL(27);
    if (m == 9) SL(28);
    if (m == 1) h_slot(16 + w);
    if (m + 4 < 10 || (m + 4 == 10 && w < 3)) h_slot(4 * (m + 4) + w);
  }
}

extern "C" __global__ void __launch_bounds__(512, 2)
fused_block(const u16* __restrict__ x_, const u16* __restrict__ wqkv_, const u16* __restrict__ wo_, const u16* __restrict__ w1_,
            const u16* __restrict__ b1_, const u16* __restrict__ w2_, const u16* __restrict__ b2_, const u16* __restrict__ c_,
            const u16* __restrict__ wmod_, u16* __restrict__ out_, unsigned char* __restrict__ ws_) {
  extern __shared__ __attribute__((aligned(16))) char smem[];
  asm volatile("" ::"s"(x_), "s"(wqkv_), "s"(wo_), "s"(w1_), "s"(b1_), "s"(w2_), "s"(b2_), "s"(c_), "s"(wmod_), "s"(out_), "s"(ws_));
  unsigned long long pc;
  asm volatile("s_getpc_b64 %0" : "=s"(pc));
  LAS char* L = (LAS char*)smem;
  const unsigned Lb = (unsigned)(uintptr_t)L;
  const int tid0 = threadIdx.x, w = __builtin_amdgcn_readfirstlane(tid0 >> 6);
  const int b = blockIdx.x, xcd = b & 7, idx = b >> 3, g = xcd * 4 + (idx >> 3), j = idx & 7;
  GAS unsigned char* ws = (GAS unsigned char*)ws_;
  const rsrc_t rs = __builtin_amdgcn_make_buffer_rsrc((void*)ws_, 0, WS_USED, 0x00020000);
  const v4u rsv = {(unsigned)(uintptr_t)ws_, (unsigned)((uintptr_t)ws_ >> 32) & 0xffffu, WS_USED, 0x00020000u};
  SRT(32);
  ST(0);
  unsigned epoch = *(const __attribute__((address_space(4))) unsigned*)(uintptr_t)(ws_ + OFF_E);
#define GET_TAG asm volatile("" : "+s"(epoch) : : "memory"); const unsigned tag = epoch + 1u;
  const int rq = xcd >> 1;
  const int nt3 = 11 - (j >= 6), t03 = 11 * j - (j == 7);
  gu32* gfl = (gu32*)(ws + OFF_GFLAG) + g * 8 * FSTR;
  const v4u rsq = {(unsigned)(uintptr_t)wqkv_, (unsigned)((uintptr_t)wqkv_ >> 32) & 0xffffu, 512u * 1536u * 2u, 0x00020000u};
#define WSV(stride) ((8 * (w & 3) + (l >> 3)) * (stride) + 128 * j + 16 * ((l & 7) ^ ((((l >> 4) & 1) | ((w & 1) << 1)) << 1)))
#define LANE_IDS const int l = lane_id(), tid = w * 64 + l, i = l & 15, q = l >> 4; const unsigned w1v = WSV(3072);
#define RETILE_IDS(v) \
    const int u = 4 * (b < 64 ? b : b >= 192 ? b - 128 : b + 64) + v; \
    const bool retile = u < 696, isw1 = u < 352; \
    const int rcb = isw1 ? u >> 4 : (int)((unsigned)(u - 352) / 43u), rks = isw1 ? (u & 15) : u - 352 - 43 * rcb; \
    const int rnt = (isw1 && rcb == 21) ? 2 : 4, rst = v < 3 ? L_RT + v * 4096 : L_EC; \

  v4u xr[5][4];
  if (w < 4) {
    const int l = lane_id();
    unsigned dummy;
    v2u cw; v4u wm[4];
    const int mcb = b & 15, mks = b >> 4;
    if (b < 64) { __builtin_amdgcn_s_setprio(3); mod_loads(c_, wmod_, true, mcb, mks, w, l, cw, wm); }
    asm volatile("s_load_dword %0, %1, 0x0\n\ts_load_dword %0, %1, 0x40\n\ts_load_dword %0, %1, 0x80\n\t"
                 "s_load_dword %0, %1, 0xc0\n\ts_load_dword %0, %1, 0x100\n\ts_load_dword %0, %1, 0x140"
                 : "=&s"(dummy) : "s"((pc & ~255ull) + idx * CODE_STEP + w * 384));
    if (b < 64) {
      asm volatile("" : "+s"(epoch) : : "memory");
      MOD_WAIT(0);
      mod_publish(rs, mcb, mks, w, l, mod_job(L, L_MRED, w, l, epoch + 1u, cw, wm));
      __builtin_amdgcn_s_setprio(0);
      SBAR();
    }
    ST(29);
    const unsigned w1v = WSV(3072);
#pragma unroll
    for (int s0 = 0; s0 < NUP; ++s0) {
      const unsigned t = s0 + 2 * (xcd & 1), s = s0 >= 6 ? s0 : t < t - 6u ? t : t - 6u;
      dma3(rsq, w1v, ((s + 4 * rq) & 15) * (32 * 3072), Lb + (s0 >= 6 ? L_R1 + (s0 - 6) * 12288 : s * 12288) + w * 3072);
    }
    asm volatile("s_waitcnt lgkmcnt(0)" : "+s"(dummy) : : "memory");
    ST(30);
    WAITV(3 * (NUP - 6));
  } else {
    const int l = lane_id();
    __builtin_amdgcn_s_setprio(3);
#pragma unroll
    for (int p = 0; p < 5; ++p) {
      int tok = 64 * g - 16 + 20 * (w & 3) + (l >> 4) + 4 * p;
      tok = tok < 0 ? 0 : tok;
#pragma unroll
      for (int n = 0; n < 4; ++n)
        asm volatile("global_load_dwordx4 %0, %1, %2" : "=v"(xr[p][n]) : "v"(tok * 1024 + 256 * ((n + rq) & 3) + 16 * (l & 15)), "s"(x_));
      if (p == XFIRST - 1 && b < 64) SBAR();
    }
    ST(29);
    SB();
  }
  if (w < 4) {
    asm volatile("; matrix waves" ::: "memory");
    LANE_IDS
    GET_TAG
    ST(2);
    meet4(L, L_SYNC, w, tag, l);
    ST(3);
    const int tb_ = q * 3072 + (i >> 2) * 128 + ((w ^ ((i >> 3) | ((q & 1) << 1))) * 32) + (i & 3) * 8;
    const int tb1 = fresh(tb_ + L_R1);
    v4u wf[16][3];
#pragma unroll
    for (int s = 0; s < 6; ++s)
#pragma unroll
      for (int sg = 0; sg < 3; ++sg) wf[s][sg] = trfrag(L + tb_ + s * 12288 + sg * 1024);
#pragma unroll
    for (int s = NUP; s < 11; ++s) dma3(rsq, w1v, ((s + 4 * rq) & 15) * (32 * 3072), Lb + L_R1 + (s - 6) * 12288 + w * 3072);
    {
      const int v = w;
      RETILE_IDS(v)
    {
      const int nc = isw1 ? 2752 : 1024;
      const v4u rr = mkrs(isw1 ? w1_ : w2_, retile ? 1376u * 1024u : 0u);
      const unsigned vo = (32 * rks + (l >> 3)) * nc + 128 * rcb + 16 * (rnt == 2 ? (l & 3) : (l & 7));
#pragma unroll
      for (int e = 0; e < 4; ++e) dma_nt(rr, vo, 8 * e * nc, Lb + rst + e * 1024);
    }
    }
    ST(7);
    BAR();
    ST(4);
    f4 aq[4], ak[5], av[5];
    const int a0 = l * 16 ^ (q << 4), a1 = l * 16 ^ ((q | 4) << 4), a0h = fresh(a0 + 61440), a1h = fresh(a1 + 61440);
    v4u a[2][5];
    SBAR();
    ST(5);
#pragma unroll
    for (int r = 0; r < 5; ++r) a[0][r] = *(LAS v4u*)(L + r * 1024 + a0);
#pragma unroll
    for (int s = 0; s < 16; ++s) {
      if (s == 5) WAITV(4);
      if (s == 10) WAITV(0);
      if (s == 3 || s == 5 || s == 8 || s == 10 || s == 12 || s == 13) SBAR();
      if (s == 5) SL(31);
      if (s == 10) SL(34);
      SB();
#pragma unroll
      for (int r = 0; r < 5; ++r) {
        MAC(s == 0, ak[r], wf[s][1], a[s & 1][r]);
        SB();
        if (s < 11) a[(s + 1) & 1][r] = *(LAS v4u*)(L + ((s + 1) * 5 + r) * 1024 + (((s + 1) & 1) ? a1 : a0));
        else if (s < 15) a[(s + 1) & 1][r] = *(LAS v4u*)(L + ((s - 11) * 5 + r) * 1024 + (((s + 1) & 1) ? a1h : a0h));
        SB();
        MAC(s == 0, av[r], a[s & 1][r], wf[s][2]);
        SB();
        if (s >= 5 && s < 15 && r < 3) { wf[s + 1][r] = trfrag(L + (s < 10 ? tb1 + (s - 5) * 12288 : s < 12 ? tb_ + (s - 10) * 12288 : tb1 + (s - 12) * 12288) + r * 1024); SB(); }
        if (r) { MAC(s == 0, aq[r - 1], wf[s][0], a[s & 1][r]); SB(); }
      }
    }
    MFMA_DONE();
    post(L, L_W + 16 + 4 * w, tag);
    SB();
    ST(6);
    {
      v2u qb[4], kb[5], vb[5];
#pragma unroll
      for (int r = 0; r < 5; ++r) {
        kb[r] = v2u{pk(ak[r].x, ak[r].y), pk(ak[r].z, ak[r].w)};
        vb[r] = v2u{pk(av[r].x, av[r].y), pk(av[r].z, av[r].w)};
        if (r) qb[r - 1] = v2u{pk(aq[r - 1].x, aq[r - 1].y), pk(aq[r - 1].z, aq[r - 1].w)};
      }
      const bool odd = i & 1;
      const bool cur0 = 4 * q + (i & 1) <= i, cur1 = 4 * q + 2 + (i & 1) <= i;
      const float sc = 0.25f * 1.4426950409f;
      const int hd = 4 * j + w;
      const f4 z = {0.f, 0.f, 0.f, 0.f};
      f4 sp[4], scu[4];
#pragma unroll
      for (int r = 0; r < 4; ++r) { sp[r] = mfma16(kb[r], qb[r], z); scu[r] = mfma16(kb[r + 1], qb[r], z); }
      const unsigned lo = odd ? 0x01000c0cu : 0x0c0c0100u, hi = odd ? 0x03020c0cu : 0x0c0c0302u, no = 0x0c0c0c0cu;
      const unsigned sel0 = cur0 ? no : lo, sel1 = cur1 ? no : hi, sel2 = cur0 ? lo : no, sel3 = cur1 ? hi : no;
      float v0[4], v1[4], m[4];
#pragma unroll
      for (int r = 0; r < 4; ++r) {
        float p0 = odd ? sp[r].y : sp[r].x, p1 = odd ? sp[r].w : sp[r].z;
        const float c0 = odd ? scu[r].y : scu[r].x, c1 = odd ? scu[r].w : scu[r].z;
        if (r == 0 && g == 0) { p0 = -4e9f; p1 = -4e9f; }
        v0[r] = cur0 ? c0 : p0; v1[r] = cur1 ? c1 : p1;
        m[r] = max2(v0[r], v1[r]);
      }
      spread4(gather4<true>(m) * sc, m);
#pragma unroll
      for (int r = 0; r < 4; ++r) {
        v0[r] = __builtin_amdgcn_exp2f(__builtin_fmaf(v0[r], sc, -m[r])); v1[r] = __builtin_amdgcn_exp2f(__builtin_fmaf(v1[r], sc, -m[r]));
        m[r] = v0[r] + v1[r];
      }
      spread4(__builtin_amdgcn_rcpf(gather4<false>(m)), m);
      f4 o[4];
#pragma unroll
      for (int r = 0; r < 4; ++r) {
        const unsigned pp = pk(v0[r] * m[r], v1[r] * m[r]);
        const v4u pf = {__builtin_amdgcn_perm(0u, pp, sel0), __builtin_amdgcn_perm(0u, pp, sel1), __builtin_amdgcn_perm(0u, pp, sel2), __builtin_amdgcn_perm(0u, pp, sel3)};
        o[r] = mfma32(v4u{vb[r].x, vb[r].y, vb[r + 1].x, vb[r + 1].y}, pf, z);
      }
      unsigned h[4][2];
#pragma unroll
      for (int r = 0; r < 4; ++r) { h[r][0] = pk(o[r].x, o[r].y); h[r][1] = pk(o[r].z, o[r].w); }
#pragma unroll
      for (int pp = 0; pp < 2; ++pp) {
        auto s0 = __builtin_amdgcn_permlane16_swap(h[2 * pp][0], h[2 * pp + 1][0], false, false);
        auto s1 = __builtin_amdgcn_permlane16_swap(h[2 * pp][1], h[2 * pp + 1][1], false, false);
        stf(rs, v4u{s0[0], s1[0], s0[1], s1[1]}, (q & 1) * 1024 + ((q >> 1) * 16 + i) * 16,
            OFF_O + ((g * 16 + (hd >> 1)) * 4 + 2 * pp) * 1024 + (hd & 1) * 512);
      }
    }
    ST(19);
    const bool two = w < nt3 - 8;
    const Ctx c = {rs, rsv, L, Lb, tag, g, j, w, t03, t03 + (two ? 2 * w : nt3 - 8 + w), gfl, x_, b1_, b2_, out_, (gu32*)(ws + OFF_E)};
    tail_c(c, two, tid STA);
    ST(16);
    SLF();
    __builtin_amdgcn_endpgm();
  } else {
    asm volatile("; helper waves" ::: "memory");
    LANE_IDS
    const int v = w - 4;
    const int row0 = 20 * v + q;
    GET_TAG
    ST(1);
    f2 xn[5][16];
    v4u mv[4];
#pragma unroll
    for (int p = 0; p < 5; ++p) {
      f2 s2 = {0.f, 0.f}, q2 = {0.f, 0.f};
      asm volatile("s_waitcnt vmcnt(%4)" : "+v"(xr[p][0]), "+v"(xr[p][1]), "+v"(xr[p][2]), "+v"(xr[p][3]) : "i"(16 - 4 * p) : "memory");
#pragma unroll
      for (int e = 0; e < 16; ++e) {
        xn[p][e] = unpk(xr[p][e >> 2][e & 3]);
        s2 += xn[p][e];
        q2 = fma2(xn[p][e], xn[p][e], q2);
      }
      const float m = sum16(s2.x + s2.y) * (1.0f / 512.0f);
      const float r = __builtin_amdgcn_rsqf(fmaxf(sum16(q2.x + q2.y) * (1.0f / 512.0f) - m * m, 0.f) + 1e-5f);
      const f2 r2 = {r, r}, mr2 = {-m * r, -m * r};
#pragma unroll
      for (int e = 0; e < 16; ++e) { xn[p][e] = fma2(xn[p][e], r2, mr2); asm volatile("" : "+v"(xn[p][e])); }
    }
    int ha[5];
#pragma unroll
    for (int p = 0; p < 5; ++p) {
      const int r = row0 + 4 * p, qq = i & 3;
      ha[p] = ((i >> 2) * 5 + (r >> 4)) * 1024 + (qq * 16 + ((r & 15) ^ (qq | (i & 4)))) * 16;
      asm volatile("" : "+v"(ha[p]));
    }
    ST(2);
    mod_get(rs, (256 * v + 4 * l) * 4, mv);
    *(LAS f4*)(L + L_MODF + (256 * v + 4 * l) * 4) = widen(mod_sum(rs, (256 * v + 4 * l) * 4, mv, tag, v < 2 ? 0.f : 1.f));
    WAITV(0);
    asm volatile("s_waitcnt lgkmcnt(0)" ::: "memory");
    ST(7);
    post(L, L_SYNC + 16 + 4 * v, tag);
    wait4(L, L_SYNC + 16, tag, l);
    v4u ho[4][5];
    {
      f4 md[2][4];
      auto get = [&](int n) {
        const int ch = L_MODF + (16 * ((n + rq) & 3) + i) * 32;
        md[n & 1][0] = *(LAS f4*)(L + ch); md[n & 1][1] = *(LAS f4*)(L + ch + 16); md[n & 1][2] = *(LAS f4*)(L + ch + 2048); md[n & 1][3] = *(LAS f4*)(L + ch + 2064);
      };
      get(0);
#pragma unroll
      for (int n = 0; n < 4; ++n) {
        if (n < 3) get(n + 1);
        SB();
        const f4 (&m)[4] = md[n & 1];
        const f2 sa2[4] = {{m[0].x, m[0].y}, {m[0].z, m[0].w}, {m[1].x, m[1].y}, {m[1].z, m[1].w}}, ca2[4] = {{m[2].x, m[2].y}, {m[2].z, m[2].w}, {m[3].x, m[3].y}, {m[3].z, m[3].w}};
#pragma unroll
        for (int p = 0; p < 5; ++p)
#pragma unroll
          for (int e = 0; e < 4; ++e) xn[p][4 * n + e] = fma2(xn[p][4 * n + e], ca2[e], sa2[e]);
        SB();
#pragma unroll
        for (int p = 0; p < 5; ++p)
#pragma unroll
          for (int e = 0; e < 4; ++e) ho[n][p][e] = pk2(xn[p][4 * n + e]);
        SB();
      }
#pragma unroll
      for (int n = 0; n < 4; ++n)
#pragma unroll
        for (int p = 0; p < 5; ++p) asm volatile("" : "+v"(ho[n][p]));
    }
    __builtin_amdgcn_s_setprio(0);
    ST(3);
    SBAR();
    ST(4);
#pragma unroll
    for (int p = 0; p < 5; ++p) *(LAS v4u*)(L + ha[p]) = ho[0][p];
    BAR();
    ST(5);
    auto w1_step = [&](int s, int at) { dma3(rsq, w1v, ((s + 4 * rq) & 15) * (32 * 3072), Lb + at + v * 3072); };
#pragma unroll
    for (int p = 0; p < 5; ++p) *(LAS v4u*)(L + ha[p] + 20480) = ho[1][p];
    BAR();
#pragma unroll
    for (int n = 2; n < 4; ++n)
#pragma unroll
      for (int p = 0; p < 5; ++p) *(LAS v4u*)(L + ha[p] + n * 20480) = ho[n][p];
    BAR();
    w1_step(11, 0); w1_step(12, 12288);
    RETILE_IDS(v)
    int rdst[4];
#pragma unroll
    for (int t = 0; t < 4; ++t) {
      const int tl = 4 * rcb + t;
      const int j3 = tl < 66 ? (int)((unsigned)tl / 11u) : (tl < 76 ? 6 : 7);
      int s4 = rks - (rcb < 6 ? (11 * rcb + 1) >> 1 : 3 + 5 * rcb); if (s4 < 0) s4 += 43;
      rdst[t] = isw1 ? OFF_W3T + (tl * 16 + ((rks - 2 * j3) & 15)) * 1024
                     : OFF_W4T + ((((rcb * 2 + (t >> 1)) * 2 + (s4 & 1)) * 22 + (s4 >> 1)) * 2 + (t & 1)) * 1024;
      if (!retile || t >= rnt) rdst[t] = 0x10000000;
      asm volatile("" : "+s"(rdst[t]));
    }
    SBAR();
    w1_step(13, L_R1); w1_step(14, L_R1 + 12288); w1_step(15, L_R1 + 2 * 12288);
    ST(34);
    WAITV(9);
    SBAR();
#pragma unroll
    for (int t = 0; t < 4; ++t) stf(rs, trfrag(L + rst + (8 * q + (i >> 2)) * 128 + t * 32 + (i & 3) * 8), l * 16, rdst[t]);
    WAITV(10);
    SBAR();
    WAITV(4);
    SBAR();
    ST(6);
    const bool job2 = b >= 64 && b < 192;
    v2u cw; v4u wm[4];
    mod_loads(c_, wmod_, job2, 16 + ((b - 64) & 31), ((b - 64) >> 5) & 3, v, fresh(l), cw, wm);
    gdma4((const GAS char*)wo_ + (idx * 256 + v * 64 + l) * 64, Lb + L_MODF + v * 256);
    WAITV(6);
    meet4(L, L_W + 128, v, tag, l);
    if (tid == 320) __hip_atomic_store((gu32*)(ws + OFF_RFLAG) + 32 * g + j, tag, RLX_AGENT);
    ST(35);
    const Ctx c = {rs, rsv, L, Lb, tag, g, j, w, t03, t03 + nt3 - 8 + w, gfl, x_, b1_, b2_, out_, (gu32*)(ws + OFF_E)};
    const int nh = v & 1, kh = v >> 1;
    v4u w2[16], w3a[16], w3b[16], w4[44];
    wait4(L, L_W + 16, tag, l);
    {
      const v4u rso = {(unsigned)(uintptr_t)wo_, (unsigned)((uintptr_t)wo_ >> 32) & 0xffffu, 512u * 512u * 2u, 0x00020000u};
      const unsigned w2v = WSV(1024);
#pragma unroll
      for (int s = 0; s < 16; ++s) dma_in(rso, w2v, ((s + 2 * j) & 15) * 32768, Lb + L_R1 + s * 4096 + v * 1024);
    }
    MOD_WAIT(16);
    v4u res;
    if (job2) res = mod_job(L, L_MODF + 2048, v, fresh(l), tag, cw, wm);
    v4u xq[2];
    {
      const rsrc_t rx = __builtin_amdgcn_make_buffer_rsrc((void*)x_, 0, 2048 * 512 * 2, 0x00020000);
      const int t = tid - 256;
#pragma unroll
      for (int k = 0; k < 2; ++k) xq[k] = __builtin_amdgcn_raw_buffer_load_b128(rx, ((t >> 3) + 32 * k) * 1024 + (t & 7) * 16, g * 65536 + j * 128, 0);
    }
    const float b1a = bflo(((const GAS u16*)b1_)[c.tile0 * 16 + i]);
    ST(31);
    WAITV(0);
    if (job2) mod_publish(rs, 16 + ((b - 64) & 31), (b - 64) >> 5, v, l, res);
    meet4(L, L_W + 64, v, tag, l);
    {
      const int tb2 = fresh(L_R1 + kh * 4096 + (8 * q + (i >> 2)) * 128 + (i & 3) * 8), f = (i >> 3) | ((q & 1) << 1);
      const int t0 = tb2 + ((2 * nh) ^ f) * 32, t1 = tb2 + ((2 * nh + 1) ^ f) * 32;
#pragma unroll
      for (int n = 0; n < 8; ++n) { w2[2 * n] = trfrag(L + t0 + n * 8192); w2[2 * n + 1] = trfrag(L + t1 + n * 8192); }
    }
    f2 x1[2][4];
#pragma unroll
    for (int k = 0; k < 2; ++k)
#pragma unroll
      for (int e = 0; e < 4; ++e) { x1[k][e] = unpk(xq[k][e]); asm volatile("" : "+v"(x1[k][e])); }
    ST(19);
    BAR();
    ST(8);
    {
      f4 acc[4][2];
      v4u a2[2][4];
      const int ab = kh * 4096 + l * 16;
#pragma unroll
      for (int r = 0; r < 4; ++r) a2[0][r] = *(LAS v4u*)(L + ab + r * 1024);
#pragma unroll
      for (int n = 0; n < 8; ++n) {
        if (!(n & 1)) SBAR();
        if (n == 0) SL(20);
        SB();
#pragma unroll
        for (int r = 0; r < 4; ++r) {
          MAC(n == 0, acc[r][0], w2[2 * n], a2[n & 1][r]);
          SB();
          if (n < 7) a2[(n + 1) & 1][r] = *(LAS v4u*)(L + ab + ((n + 1) * 8 + r) * 1024);
          SB();
          MAC(n == 0, acc[r][1], w2[2 * n + 1], a2[n & 1][r]);
          SB();
        }
      }
      MFMA_DONE();
      SL(21);
      red_write(c, l, nh, kh, acc);
    }
    meet4(L, L_W + 80, v, tag, l);
    ST(36);
    wait4(L, L_W + 48, tag, l);
    ST(25);
    __builtin_amdgcn_s_setprio(1);
    epi2(c, tid - 256, x1);
    __builtin_amdgcn_s_setprio(0);
    ST(17);
    asm volatile("" ::: "memory");
    const unsigned w3off = OFF_W3T + c.tile0 * 16384, w4off = OFF_W4T + ((j * 2 + nh) * 2 + kh) * 44 * 1024;
#pragma unroll
    for (int n = 0; n < 6; ++n) w3a[n] = LDW(n, w3off);
    WAITV(8);
    meet4(L, L_W + 96, v, tag, l);
    if (tid == 384) __hip_atomic_store(gfl + GSEAM + j * FSTR, tag, RLX_AGENT);
    ST(9);
    if (v < 2) dma<4>(rsv, l * 16, OFF_XA + (g * 16 + 2 * j + v) * 4096, Lb + v * 4096);
#pragma unroll
    for (int n = 6; n < 16; ++n) w3a[n] = LDW(n, w3off);
    SB();
    float uvt[1];
    col_terms<1>(c, false, l, w3a, w3b, b1a, b1a, uvt);
    USE(uvt[0]);
    if (v < 2) WAITV(0);
    SBAR();
    ST(10);
    {
      f4 acc[4][1];
      const float S[5] = {0.f, 0.f, 0.f, 0.f, 0.f};
      mlp1_loop<1, 1>(c, l, w3a, w3b, acc, w4, w4off, false, S STA);
      BAR();
      ST(12);
      mlp1_epi<1>(c, false, l, uvt, acc);
    }
    asm volatile("" ::: "memory");
    const rsrc_t rb2 = __builtin_amdgcn_make_buffer_rsrc((void*)b2_, 0, 1024, 0x00020000);
    const v4u b2v = __builtin_amdgcn_raw_buffer_load_b128(rb2, (l & 7) * 16, j * 128, 0);
    ST(18);
    WAITV_BAR(1);
    if (tid == 256) __hip_atomic_store(gfl + 2 * GSEAM + j * FSTR, tag, RLX_AGENT);
    ST(13);
    {
      f4 acc[4][2];
      v4u a4[2][4];
      const int ab = kh * 4096 + l * 16, abh = fresh(ab + 65536), abx = fresh(ab + 131072);
      SBAR();
      ST(14);
#pragma unroll
      for (int r = 0; r < 4; ++r) a4[0][r] = *(LAS v4u*)(L + ab + r * 1024);
#pragma unroll
      for (int n = 0; n < 22; ++n) {
        if ((n & 1) && n < 19) SBAR();
        if (n == 1) SL(26);
        if (n == 9) SL(27);
        if (n == 17) SL(28);
        SB();
        const int sp = 2 * (n + 1) >= 38 ? 2 * (n + 1) - 38 : 2 * (n + 1);
        if (n < 21 || kh == 0) {
#pragma unroll
          for (int r = 0; r < 4; ++r) {
            MAC(n == 0, acc[r][0], w4[2 * n], a4[n & 1][r]);
            SB();
            if (n < 21) a4[(n + 1) & 1][r] = *(LAS v4u*)(L + (sp < 16 ? ab : sp < 32 ? abh : abx) + (sp & 15) * 4096 + r * 1024);
            SB();
            MAC(n == 0, acc[r][1], w4[2 * n + 1], a4[n & 1][r]);
            SB();
          }
        }
      }
      MFMA_DONE();
      ST(15);
      red_write(c, l, nh, kh, acc);
    }
    meet4(L, L_W + 112, v, tag, l);
    epi4(c, tid - 256, b2v, x1);
    ST(16);
    SLF();
    __builtin_amdgcn_s_waitcnt(0x0F70);
    __builtin_amdgcn_endpgm();
  }
}

extern "C" void kernel_launch(void* const* d_in, const int* in_sizes, int n_in, void* d_out, int out_size, void* d_ws, size_t ws_size,
                              hipStream_t stream) {
  (void)hipFuncSetAttribute((const void*)fused_block, hipFuncAttributeMaxDynamicSharedMemorySize, LDS_BYTES);
  fused_block<<<256, 512, LDS_BYTES, stream>>>((const u16*)d_in[0], (const u16*)d_in[1], (const u16*)d_in[2], (const u16*)d_in[4],
                                               (const u16*)d_in[5], (const u16*)d_in[6], (const u16*)d_in[7], (const u16*)d_in[8],
                                               (const u16*)d_in[9], (u16*)d_out, (unsigned char*)d_ws + ((ws_size / 2) & ~(size_t)0xfffff));
}
```

```cpp
#include <hip/hip_runtime.h>
#include <stdint.h>

#define GAS __attribute__((address_space(1)))
#define LAS __attribute__((address_space(3)))
#define DI __device__ __forceinline__
typedef unsigned short u16;
typedef unsigned v4u __attribute__((ext_vector_type(4)));
typedef unsigned v2u __attribute__((ext_vector_type(2)));
typedef float f4 __attribute__((ext_vector_type(4)));
typedef float f2 __attribute__((ext_vector_type(2)));
typedef __bf16 bf2 __attribute__((ext_vector_type(2)));
typedef __bf16 bf8 __attribute__((ext_vector_type(8)));
typedef short s4 __attribute__((ext_vector_type(4)));
typedef GAS unsigned gu32;
typedef GAS unsigned long long gu64;
#define RLX_AGENT __ATOMIC_RELAXED, __HIP_MEMORY_SCOPE_AGENT

constexpr unsigned OFF_E = 0;
constexpr unsigned OFF_RFLAG = 768u << 10;
constexpr unsigned OFF_GFLAG = 800u << 10;
constexpr unsigned OFF_MODP = 16384;
constexpr unsigned OFF_STAT = 65536;
constexpr unsigned OFF_W3T = 1u << 20;
constexpr unsigned OFF_W4T = OFF_W3T + 1376 * 1024;
constexpr unsigned OFF_O = OFF_W4T + 1408 * 1024;
constexpr unsigned OFF_XA = OFF_O + 2048 * 1024;
constexpr unsigned OFF_H1 = OFF_XA + 2048 * 1024;
constexpr unsigned WS_USED = OFF_H1 + 32 * 43 * 4096;
constexpr unsigned OFF_STAMP = 64u << 20;

constexpr int L_R1 = 81920;
constexpr int L_W2 = 81920;
constexpr int L_RED = 65536;
constexpr int L_STAT = 102400;
constexpr int L_UV = 147456;
constexpr int L_RT = 143360;
constexpr int L_MODF = 155648;
constexpr int L_EC = 159744;
constexpr int L_MRED = 77824;
constexpr int L_SYNC = 79872;
constexpr int L_COEF = L_MODF;
constexpr int L_RR = L_COEF + 1024;
constexpr int L_W = L_MODF + 3584;
constexpr int LDS_BYTES = 163840;

constexpr int CODE_STEP = 1216;
constexpr int FSTR = 32, GSEAM = 32 * 8 * 32;
#define PD1 14
#define PD2 18
#define PD3 16
#define PINT 1
#define W4MODE 0
#define XFIRST 2
constexpr unsigned SPIN_CAP = 1u << 18;

#define ST(n)
#define SL(n)
#define SRT(n)
#define SLF()

DI float bflo(unsigned u) { return __uint_as_float(u << 16); }
DI float bfhi(unsigned u) { return __uint_as_float(u & 0xffff0000u); }
DI unsigned pk(float a, float b) { f2 v = {a, b}; return __builtin_bit_cast(unsigned, __builtin_convertvector(v, bf2)); }
DI f2 unpk(unsigned u) { return f2{bflo(u), bfhi(u)}; }
DI f2 fma2(f2 a, f2 b, f2 c) { return __builtin_elementwise_fma(a, b, c); }
DI unsigned pk2(f2 v) { return __builtin_bit_cast(unsigned, __builtin_convertvector(v, bf2)); }
DI f4 mfma32(v4u a, v4u b, f4 c) { return __builtin_amdgcn_mfma_f32_16x16x32_bf16(__builtin_bit_cast(bf8, a), __builtin_bit_cast(bf8, b), c, 0, 0, 0); }
template <bool FIRST> DI void mac(f4& c, v4u a, v4u b) {
  if constexpr (FIRST) asm volatile("v_mfma_f32_16x16x32_bf16 %0, %1, %2, 0" : "=&v"(c) : "v"(a), "v"(b));
  else asm volatile("v_mfma_f32_16x16x32_bf16 %0, %1, %2, %0" : "+v"(c) : "v"(a), "v"(b));
}
#define MAC(first, c, a, b) do { if (first) mac<true>(c, a, b); else mac<false>(c, a, b); } while (0)
#define MFMA_DONE() do { asm volatile("s_nop 15" ::: "memory"); __builtin_amdgcn_sched_barrier(0); } while (0)
DI f4 mfma16(v2u a, v2u b, f4 c) { return __builtin_amdgcn_mfma_f32_16x16x16bf16_1k(__builtin_bit_cast(s4, a), __builtin_bit_cast(s4, b), c, 0, 0, 0); }
template <int CTRL> DI float dpp(float x) { return __builtin_bit_cast(float, __builtin_amdgcn_update_dpp(0, __builtin_bit_cast(int, x), CTRL, 0xf, 0xf, true)); }
DI float swap16_sum(float x) { auto r = __builtin_amdgcn_permlane16_swap(__float_as_uint(x), __float_as_uint(x), false, false); return __uint_as_float(r[0]) + __uint_as_float(r[1]); }
DI float swap32_sum(float x) { auto r = __builtin_amdgcn_permlane32_swap(__float_as_uint(x), __float_as_uint(x), false, false); return __uint_as_float(r[0]) + __uint_as_float(r[1]); }
DI float swap16_max(float x) { auto r = __builtin_amdgcn_permlane16_swap(__float_as_uint(x), __float_as_uint(x), false, false); return fmaxf(__uint_as_float(r[0]), __uint_as_float(r[1])); }
DI float swap32_max(float x) { auto r = __builtin_amdgcn_permlane32_swap(__float_as_uint(x), __float_as_uint(x), false, false); return fmaxf(__uint_as_float(r[0]), __uint_as_float(r[1])); }
DI float max2(float a, float b) { return __builtin_amdgcn_fmed3f(a, b, __builtin_inff()); }
template <bool MAX> DI float gather4(const float (&x)[4]) {
  auto a = __builtin_amdgcn_permlane16_swap(__float_as_uint(x[0]), __float_as_uint(x[1]), false, false);
  auto b = __builtin_amdgcn_permlane16_swap(__float_as_uint(x[2]), __float_as_uint(x[3]), false, false);
  const float u = MAX ? max2(__uint_as_float(a[0]), __uint_as_float(a[1])) : __uint_as_float(a[0]) + __uint_as_float(a[1]);
  const float v = MAX ? max2(__uint_as_float(b[0]), __uint_as_float(b[1])) : __uint_as_float(b[0]) + __uint_as_float(b[1]);
  auto c = __builtin_amdgcn_permlane32_swap(__float_as_uint(u), __float_as_uint(v), false, false);
  return MAX ? max2(__uint_as_float(c[0]), __uint_as_float(c[1])) : __uint_as_float(c[0]) + __uint_as_float(c[1]);
}
DI void spread4(float t, float (&x)[4]) {
  auto c = __builtin_amdgcn_permlane32_swap(__float_as_uint(t), __float_as_uint(t), false, false);
  auto a = __builtin_amdgcn_permlane16_swap(c[0], c[0], false, false);
  auto b = __builtin_amdgcn_permlane16_swap(c[1], c[1], false, false);
  x[0] = __uint_as_float(a[0]); x[1] = __uint_as_float(a[1]); x[2] = __uint_as_float(b[0]); x[3] = __uint_as_float(b[1]);
}
DI float sum8(float x) { x += dpp<0xB1>(x); x += dpp<0x4E>(x); x += dpp<0x141>(x); return x; }
DI float sum16(float x) { x = sum8(x); x += dpp<0x140>(x); return x; }

#define BAR() asm volatile("s_waitcnt lgkmcnt(0)\n\ts_barrier" ::: "memory")
#define WAITV(n) do { asm volatile("s_waitcnt vmcnt(%0)" ::"i"(n) : "memory"); if ((n) == 0) __builtin_amdgcn_s_waitcnt(0x0F70); } while (0)
#define WAITV_BAR(n) do { asm volatile("s_waitcnt vmcnt(%0) lgkmcnt(0)\n\ts_barrier" ::"i"(n) : "memory"); if ((n) == 0) __builtin_amdgcn_s_waitcnt(0x0F70); } while (0)
#define SBAR() asm volatile("s_barrier" ::: "memory")
#define USE(x) asm volatile("" ::"v"(x))

template <int N> DI void dma(v4u rs, unsigned voff, unsigned soff, unsigned lds) {
  unsigned keep;
  if constexpr (N == 1)
    asm volatile("s_mov_b32 %0, m0\n\ts_mov_b32 m0, %2\n\ts_nop 0\n\t"
                 "buffer_load_dwordx4 %1, %3, %4 offen sc1 lds\n\t"
                 "s_mov_b32 m0, %0" : "=&s"(keep) : "v"(voff), "s"(lds), "s"(rs), "s"(soff) : "memory");
  else if constexpr (N == 2)
    asm volatile("s_mov_b32 %0, m0\n\ts_mov_b32 m0, %2\n\ts_nop 0\n\t"
                 "buffer_load_dwordx4 %1, %3, %4 offen sc1 lds\n\t"
                 "buffer_load_dwordx4 %1, %3, %4 offen offset:1024 sc1 lds\n\t"
                 "s_mov_b32 m0, %0" : "=&s"(keep) : "v"(voff), "s"(lds), "s"(rs), "s"(soff) : "memory");
  else
    asm volatile("s_mov_b32 %0, m0\n\ts_mov_b32 m0, %2\n\ts_nop 0\n\t"
                 "buffer_load_dwordx4 %1, %3, %4 offen sc1 lds\n\t"
                 "buffer_load_dwordx4 %1, %3, %4 offen offset:1024 sc1 lds\n\t"
                 "buffer_load_dwordx4 %1, %3, %4 offen offset:2048 sc1 lds\n\t"
                 "buffer_load_dwordx4 %1, %3, %4 offen offset:3072 sc1 lds\n\t"
                 "s_mov_b32 m0, %0" : "=&s"(keep) : "v"(voff), "s"(lds), "s"(rs), "s"(soff) : "memory");
}
DI void dma3(v4u rs, unsigned voff, unsigned soff, unsigned lds) {
  unsigned keep;
  asm volatile("s_mov_b32 %0, m0\n\ts_mov_b32 m0, %2\n\ts_nop 0\n\t"
               "buffer_load_dwordx4 %1, %3, %4 offen lds\n\t"
               "buffer_load_dwordx4 %1, %3, %4 offen offset:1024 lds\n\t"
               "buffer_load_dwordx4 %1, %3, %4 offen offset:2048 lds\n\t"
               "s_mov_b32 m0, %0" : "=&s"(keep) : "v"(voff), "s"(lds), "s"(rs), "s"(soff) : "memory");
}
DI v4u trfrag(LAS char* p) {
  v2u lo = __builtin_bit_cast(v2u, __builtin_amdgcn_ds_read_tr16_b64_v4i16((LAS s4*)p));
  v2u hi = __builtin_bit_cast(v2u, __builtin_amdgcn_ds_read_tr16_b64_v4i16((LAS s4*)(p + 512)));
  return v4u{lo.x, lo.y, hi.x, hi.y};
}
DI int lane_id() { int r; asm volatile("v_mbcnt_lo_u32_b32 %0, -1, 0\n\tv_mbcnt_hi_u32_b32 %0, -1, %0" : "=v"(r)); return r; }
DI int fresh(int x) { asm volatile("" : "+v"(x)); return x; }
#define SB() __builtin_amdgcn_sched_barrier(0)
DI void dma_nt(v4u rs, unsigned voff, unsigned soff, unsigned lds) {
  unsigned keep;
  asm volatile("s_mov_b32 %0, m0\n\ts_mov_b32 m0, %2\n\ts_nop 0\n\tbuffer_load_dwordx4 %1, %3, %4 offen nt lds\n\ts_mov_b32 m0, %0"
               : "=&s"(keep) : "v"(voff), "s"(lds), "s"(rs), "s"(soff) : "memory");
}
DI void gdma4(const GAS void* p, unsigned lds) {
  unsigned keep;
  asm volatile("s_mov_b32 %0, m0\n\ts_mov_b32 m0, %2\n\ts_nop 0\n\tglobal_load_lds_dword %1, off\n\ts_mov_b32 m0, %0"
               : "=&s"(keep) : "v"(p), "s"(lds) : "memory");
}
typedef __amdgpu_buffer_rsrc_t rsrc_t;
DI v4u ldf(rsrc_t rs, unsigned voff, unsigned soff) { return __builtin_amdgcn_raw_buffer_load_b128(rs, voff, soff, 16); }
#define LDW(n, base) ldw_rt(c.rs, l * 16, base, n)
DI v4u ldw_rt(rsrc_t rs, unsigned voff, unsigned base, int n) { return __builtin_amdgcn_raw_buffer_load_b128(rs, voff + (n & 3) * 1024, base + (n >> 2) * 4096, 16); }
DI v4u ld_rflags(rsrc_t rs, int l) { return ldf(rs, (l >> 1) * 128 + (l & 1) * 16, OFF_RFLAG); }
DI void stf(rsrc_t rs, v4u v, unsigned voff, unsigned soff) { __builtin_amdgcn_raw_buffer_store_b128(v, rs, voff + soff, 0, 16); }

DI void meet4(LAS char* L, int at, int me, unsigned tag, int l) {
  volatile LAS unsigned* f = (volatile LAS unsigned*)(L + at);
  f[me] = tag;
  _Pragma("nounroll") for (unsigned s = 0; s < 16 * SPIN_CAP; ++s)
    if (__all(f[l & 3] == tag)) break;
  asm volatile("" ::: "memory");
}
DI void post(LAS char* L, int at, unsigned tag) { *(volatile LAS unsigned*)(L + at) = tag; }
DI void wait4(LAS char* L, int at, unsigned tag, int l) {
  volatile LAS unsigned* f = (volatile LAS unsigned*)(L + at);
  _Pragma("nounroll") for (unsigned s = 0; s < 16 * SPIN_CAP; ++s) {
    if (__all(f[l & 3] == tag)) break;
    __builtin_amdgcn_s_sleep(1);
  }
  asm volatile("" ::: "memory");
}
DI void lds_wait(LAS char* L, int at, unsigned tag) {
  volatile LAS unsigned* f = (volatile LAS unsigned*)(L + at);
  _Pragma("nounroll") for (unsigned s = 0; s < 16 * SPIN_CAP; ++s) {
    if (*f == tag) break;
    __builtin_amdgcn_s_sleep(1);
  }
  asm volatile("" ::: "memory");
}
DI void raise(rsrc_t rs, unsigned line, unsigned tag, int l) { if (l < 8) stf(rs, v4u{tag, tag, tag, tag}, l * 16, line); }
template <int DELAY> DI void poll8(gu32* fl, unsigned tag, int l) {
  __builtin_amdgcn_s_sleep(DELAY);
  _Pragma("nounroll") for (unsigned s = 0; s < SPIN_CAP; ++s) {
    unsigned f = __hip_atomic_load(fl + (l & 7) * FSTR, RLX_AGENT);
    if (__all(f == tag)) break;
    __builtin_amdgcn_s_sleep(PINT);
  }
  __builtin_amdgcn_fence(__ATOMIC_ACQUIRE, "wavefront");
}

DI unsigned gelu2(f2 a, f2 k1, f2 k2, f2 ir) {
  const f2 t = a * fma2(a * a, k2, k1);
  const f2 d = fma2(f2{__builtin_amdgcn_exp2f(t.x), __builtin_amdgcn_exp2f(t.y)}, ir, ir);
  return pk2(a * f2{__builtin_amdgcn_rcpf(d.x), __builtin_amdgcn_rcpf(d.y)});
}

DI void mod_get(rsrc_t rs, unsigned voff, v4u (&v)[4]) {
#pragma unroll
  for (int k = 0; k < 4; ++k) v[k] = ldf(rs, voff, OFF_MODP + k * 12288);
}
DI v2u mod_sum(rsrc_t rs, unsigned voff, v4u (&v)[4], unsigned tag, float plus) {
  const unsigned tb = tag & 0xffu;
  _Pragma("nounroll") for (unsigned s = 0; s < SPIN_CAP; ++s) {
#pragma unroll
    for (int k = 0; k < 4; ++k) v[k] ^= tb;
    const v4u d = (v[0] | v[1]) | (v[2] | v[3]);
    if (__all(((d.x | d.y) | (d.z | d.w)) << 24 == 0u)) break;
    __builtin_amdgcn_s_sleep(1);
    asm volatile("" ::: "memory");
    mod_get(rs, voff, v);
    WAITV(0);
  }
  const f4 s = (__builtin_bit_cast(f4, v[0]) + __builtin_bit_cast(f4, v[1])) + (__builtin_bit_cast(f4, v[2]) + __builtin_bit_cast(f4, v[3]));
  const f2 p2 = {plus, plus};
  return v2u{pk2(unpk(pk(s.x, s.y)) + p2), pk2(unpk(pk(s.z, s.w)) + p2)};
}
DI f4 widen(v2u p) { return f4{bflo(p.x), bfhi(p.x), bflo(p.y), bfhi(p.y)}; }

#define STP
#define STA

DI v4u mkrs(const void* p, unsigned bytes) { return v4u{(unsigned)(uintptr_t)p, (unsigned)((uintptr_t)p >> 32) & 0xffffu, bytes, 0x00020000u}; }
DI void mod_loads(const u16* c_, const u16* wmod_, bool on, int mcb, int mks, int v, int l, v2u& cw, v4u (&wm)[4]) {
  const int r0 = 128 * mks + 32 * v + 4 * (l >> 3);
  const v4u rc = mkrs(c_, on ? 1024u : 0u), rw = mkrs(wmod_, on ? 512u * 6144u : 0u);
  asm volatile("buffer_load_dwordx2 %0, %1, %2, 0 offen" : "=v"(cw) : "v"(r0 * 2), "s"(rc));
#pragma unroll
  for (int e = 0; e < 4; ++e) asm volatile("buffer_load_dwordx4 %0, %1, %2, %3 offen nt" : "=v"(wm[e]) : "v"(r0 * 6144 + 128 * mcb + 16 * (l & 7)), "s"(rw), "s"(e * 6144));
}
#define MOD_WAIT(n) asm volatile("s_waitcnt vmcnt(%5)" : "+v"(cw), "+v"(wm[0]), "+v"(wm[1]), "+v"(wm[2]), "+v"(wm[3]) : "i"(n) : "memory")
DI v4u mod_job(LAS char* L, int red, int v, int l, unsigned tag, v2u cw, const v4u (&wm)[4]) {
  v4u res = {0u, 0u, 0u, 0u};
  f2 a2[4];
#pragma unroll
  for (int e = 0; e < 4; ++e) {
    const float cv = (e & 1) ? bfhi(cw[e >> 1]) : bflo(cw[e >> 1]);
    float sv = cv * __builtin_amdgcn_rcpf(1.0f + __builtin_amdgcn_exp2f(-1.4426950409f * cv));
    sv = bflo(pk(sv, sv));
    const f2 s2 = {sv, sv};
#pragma unroll
    for (int k = 0; k < 4; ++k) a2[k] = e ? fma2(s2, unpk(wm[e][k]), a2[k]) : s2 * unpk(wm[e][k]);
  }
  float a[4];
#pragma unroll
  for (int k = 0; k < 4; ++k) {
    auto r = __builtin_amdgcn_permlane32_swap(__float_as_uint(a2[k >> 1][k & 1]), __float_as_uint(a2[2 + (k >> 1)][k & 1]), false, false);
    a[k] = __uint_as_float(r[0]) + __uint_as_float(r[1]);
  }
#pragma unroll
  for (int k = 0; k < 2; ++k) {
    auto r = __builtin_amdgcn_permlane16_swap(__float_as_uint(a[k]), __float_as_uint(a[k + 2]), false, false);
    a[k] = __uint_as_float(r[0]) + __uint_as_float(r[1]);
    a[k] += dpp<0x128>(a[k]);
  }
  if (!(l & 8)) *(LAS f2*)(L + red + v * 256 + (8 * (l & 7) + 2 * ((l >> 4) & 1) + 4 * (l >> 5)) * 4) = f2{a[0], a[1]};
  asm volatile("s_waitcnt lgkmcnt(0)" ::: "memory");
  volatile LAS unsigned* mf = (volatile LAS unsigned*)(L + red + 1024);
  if (v < 3) mf[v] = tag;
  else {
    _Pragma("nounroll") for (unsigned s = 0; s < SPIN_CAP; ++s)
      if (__all(mf[l < 3 ? l : 0] == tag)) break;
    asm volatile("" ::: "memory");
    f4 s = *(LAS f4*)(L + red + (l & 15) * 16);
#pragma unroll
    for (int ww = 1; ww < 4; ++ww) s += *(LAS f4*)(L + red + ww * 256 + (l & 15) * 16);
    res = (__builtin_bit_cast(v4u, s) & 0xffffff00u) | (tag & 0xffu);
  }
  return res;
}
DI void mod_publish(rsrc_t rs, int mcb, int mks, int v, int l, v4u res) {
  if (v == 3 && l < 16) stf(rs, res, l * 16, OFF_MODP + mks * 12288 + mcb * 256);
}

struct Ctx {
  rsrc_t rs; v4u rsv; LAS char* L; unsigned Lb, tag; int g, j, w, t03, tile0; gu32* gfl;
  const u16 *x_, *b1_, *b2_; u16* out_; gu32* epoch;
};

template <int T> DI void col_terms(const Ctx& c, bool two, int l, const v4u (&w3a)[16], const v4u (&w3b)[16], float b1a, float b1b, float (&uvt)[T]) {
  l = fresh(l);
  const int i = l & 15, q = l >> 4, ty = i >> 2;
  const int base = L_UV + (ty < 2 ? ty * 2048 + c.j * 128 : 2048 + ty * 1024) + q * 16;
  f4 a[2][T];
  v4u am[16];
#pragma unroll
  for (int s = 0; s < 16; ++s) am[s] = *(LAS v4u*)(c.L + base + s * 64);
  SB();
#pragma unroll
  for (int s = 0; s < 16; ++s) MAC(s < 2, a[s & 1][0], am[s], w3a[s]);
  if (T == 2 && two) {
#pragma unroll
    for (int s = 0; s < 16; ++s) MAC(s < 2, a[s & 1][T - 1], am[s], w3b[s]);
  }
  MFMA_DONE();
  uvt[0] = a[0][0].x + a[1][0].x + (q == 3 ? b1a : 0.f);
  if (T == 2) uvt[T - 1] = a[0][T - 1].x + a[1][T - 1].x + (q == 3 ? b1b : 0.f);
}

template <int T, int KIND>
DI void mlp1_loop(const Ctx& c, int l, const v4u (&w3a)[16], const v4u (&w3b)[16], f4 (&acc)[4][T], v4u (&w4)[44], unsigned w4off, bool two,
                  const float (&S)[5] STP) {
  LAS char* L = c.L;
  const int tid = c.w * 64 + l;
  auto xa_slot = [&](int slot) { dma<4>(c.rsv, l * 16, OFF_XA + (c.g * 16 + ((slot + 2 * c.j) & 15)) * 4096, c.Lb + slot * 4096); };
  v4u a[2][4];
#pragma unroll
  for (int r = 0; r < 4; ++r) a[0][r] = *(LAS v4u*)(L + r * 1024 + l * 16);
#pragma unroll
  for (int s = 0; s < 16; ++s) {
    if (s == 1 || s == 5) {
      if (KIND == 2 && c.w == 3) {
        if (s == 1) WAITV(16);
        if (s == 5) WAITV(16);
      } else if (KIND == 2 && s == 5) WAITV(0);
      SBAR();
      SL(22 + s / 4);
      if (KIND == 2 && c.w == 3) {
        if (s == 1) {
#pragma unroll
          for (int p = 0; p < 4; ++p) dma<4>(c.rsv, l * 16, OFF_STAT + c.g * 16384 + p * 4096, c.Lb + L_STAT + p * 4096);
        }
        if (s == 5) {
          float sx, sxx, sp, spp, spa, sps;
          _Pragma("nounroll") for (unsigned it = 0; it < SPIN_CAP; ++it) {
            WAITV(0);
            v4u a4[8]; v2u b2[8];
            unsigned d = 0;
#pragma unroll
            for (int p = 0; p < 8; ++p) {
              a4[p] = *(LAS v4u*)(L + L_STAT + p * 2048 + l * 16) ^ (c.tag & 0xffu);
              b2[p] = *(LAS v2u*)(L + L_STAT + p * 2048 + 1024 + l * 16) ^ (c.tag & 0xffu);
              d |= (a4[p].x | a4[p].y) | (a4[p].z | a4[p].w) | (b2[p].x | b2[p].y);
            }
            f4 ta = __builtin_bit_cast(f4, a4[0]); f2 tb = __builtin_bit_cast(f2, b2[0]);
#pragma unroll
            for (int p = 1; p < 8; ++p) { ta += __builtin_bit_cast(f4, a4[p]); tb += __builtin_bit_cast(f2, b2[p]); }
            sx = ta.x; sxx = ta.y; sp = ta.z; spp = ta.w; spa = tb.x; sps = tb.y;
            if (__all(d << 24 == 0u)) break;
            asm volatile("s_waitcnt lgkmcnt(0)" ::: "memory");
#pragma unroll
            for (int p = 0; p < 4; ++p) dma<4>(c.rsv, l * 16, OFF_STAT + c.g * 16384 + p * 4096, c.Lb + L_STAT + p * 4096);
          }
          const float inv = 1.0f / 512.0f;
          const float m = sx * inv, var = fmaxf(sxx * inv - m * m, 0.f);
          const float sd = __builtin_amdgcn_sqrtf(var + 1e-5f), r = __builtin_amdgcn_rcpf(sd);
          const float m2 = (r * (sp - m * S[0]) + S[1]) * inv;
          const float e2 = (r * r * (spp - 2.f * m * spa + m * m * S[2]) + 2.f * r * (sps - m * S[3]) + S[4]) * inv;
          const float sd2 = __builtin_amdgcn_sqrtf(fmaxf(e2 - m2 * m2, 0.f) + 1e-5f);
          const float R = r * __builtin_amdgcn_rcpf(sd2), k1 = -2.302208198f * R;
          *(LAS f4*)(L + L_COEF + l * 16) = f4{-m, sd, -m2 * sd, sd * sd2};
          *(LAS f4*)(L + L_RR + l * 16) = f4{k1, 0.044715f * k1 * R * R, sd * sd2, 0.f};
        }
      }
    }
    SB();
#pragma unroll
    for (int r = 0; r < 4; ++r) {
      MAC(s == 0, acc[r][0], w3a[s], a[s & 1][r]);
      SB();
      if (s < 15) a[(s + 1) & 1][r] = *(LAS v4u*)(L + ((s + 1) * 4 + r) * 1024 + l * 16);
      SB();
    }
    if (T == 2 && two) {
#pragma unroll
      for (int r = 0; r < 4; ++r) MAC(s == 0, acc[r][T - 1], w3b[s], a[s & 1][r]);
      SB();
    }
    if (KIND == 1) {
      const int n0 = s < 4 ? 2 * s : 3 * s - 4;
      w4[n0] = LDW(n0, w4off);
      w4[n0 + 1] = LDW(n0 + 1, w4off);
      if (s >= 4) w4[n0 + 2] = LDW(n0 + 2, w4off);
      SB();
    }
  }
  MFMA_DONE();
}

template <int T> DI void mlp1_epi(const Ctx& c, bool two, int l, const float (&uvt)[T], f4 (&acc)[4][T]) {
  l = fresh(l);
  const int i = l & 15, q = l >> 4;
  float cf[4]; f4 rr[4];
#pragma unroll
  for (int r = 0; r < 4; ++r) {
    cf[r] = *(LAS float*)(c.L + L_COEF + (r * 16 + i) * 16 + q * 4);
    rr[r] = *(LAS f4*)(c.L + L_RR + (r * 16 + i) * 16);
  }
#pragma unroll
  for (int t = 0; t < T; ++t) {
    if (t && !two) break;
#pragma unroll
    for (int r = 0; r < 4; ++r) acc[r][t] = __builtin_amdgcn_mfma_f32_16x16x4f32(uvt[t], cf[r], acc[r][t], 0, 0, 0);
  }
  SB();
#pragma unroll
  for (int t = 0; t < T; ++t) {
    if (t && !two) break;
    const int tl = c.tile0 + t;
#pragma unroll
    for (int pp = 0; pp < 2; ++pp) {
      unsigned h[2][2];
#pragma unroll
      for (int r2 = 0; r2 < 2; ++r2) {
        const int r = 2 * pp + r2;
        const f2 k1 = {rr[r].x, rr[r].x}, k2 = {rr[r].y, rr[r].y}, ir = {rr[r].z, rr[r].z};
        h[r2][0] = gelu2(f2{acc[r][t].x, acc[r][t].y}, k1, k2, ir);
        h[r2][1] = gelu2(f2{acc[r][t].z, acc[r][t].w}, k1, k2, ir);
      }
      auto s0 = __builtin_amdgcn_permlane16_swap(h[0][0], h[1][0], false, false);
      auto s1 = __builtin_amdgcn_permlane16_swap(h[0][1], h[1][1], false, false);
      stf(c.rs, v4u{s0[0], s1[0], s0[1], s1[1]}, (q & 1) * 1024 + ((q >> 1) * 16 + i) * 16,
          OFF_H1 + ((c.g * 43 + (tl >> 1)) * 4 + 2 * pp) * 1024 + (tl & 1) * 512);
      SB();
    }
  }
}

DI void dma_in(v4u rs, unsigned voff, unsigned soff, unsigned lds) {
  unsigned keep;
  asm volatile("s_mov_b32 %0, m0\n\ts_mov_b32 m0, %2\n\ts_nop 0\n\tbuffer_load_dwordx4 %1, %3, %4 offen lds\n\ts_mov_b32 m0, %0"
               : "=&s"(keep) : "v"(voff), "s"(lds), "s"(rs), "s"(soff) : "memory");
}
DI void sum8x6(float& a, float& b, float& c, float& d, float& e, float& f) {
#define S6(ctl) "v_add_f32_dpp %0, %0, %0 " ctl "\n\tv_add_f32_dpp %1, %1, %1 " ctl "\n\tv_add_f32_dpp %2, %2, %2 " ctl "\n\t" \
                "v_add_f32_dpp %3, %3, %3 " ctl "\n\tv_add_f32_dpp %4, %4, %4 " ctl "\n\tv_add_f32_dpp %5, %5, %5 " ctl "\n\t"
  asm volatile("s_nop 1\n\t" S6("quad_perm:[1,0,3,2] row_mask:0xf bank_mask:0xf") S6("quad_perm:[2,3,0,1] row_mask:0xf bank_mask:0xf")
               S6("row_half_mirror row_mask:0xf bank_mask:0xf") "s_nop 0" : "+v"(a), "+v"(b), "+v"(c), "+v"(d), "+v"(e), "+v"(f));
#undef S6
}
DI void red_write(const Ctx& c, int l, int nh, int kh, const f4 (&acc)[4][2]) {
  l = fresh(l);
  const int rb = L_RED + kh * 17408 + (l & 15) * 272 + nh * 128 + (l >> 4) * 16;
#pragma unroll
  for (int r = 0; r < 4; ++r)
#pragma unroll
    for (int t = 0; t < 2; ++t) *(LAS f4*)(c.L + rb + r * 16 * 272 + t * 64) = acc[r][t];
  asm volatile("s_waitcnt lgkmcnt(0)" ::: "memory");
}
DI void red_read(const Ctx& c, int tok, int f8, f2 (&y)[4]) {
  const int ra = L_RED + tok * 272 + f8 * 32;
  const f4 y0 = *(LAS f4*)(c.L + ra) + *(LAS f4*)(c.L + ra + 17408), y1 = *(LAS f4*)(c.L + ra + 16) + *(LAS f4*)(c.L + ra + 17408 + 16);
  y[0] = f2{y0.x, y0.y}; y[1] = f2{y0.z, y0.w}; y[2] = f2{y1.x, y1.y}; y[3] = f2{y1.z, y1.w};
}
DI void ec_read(const Ctx& c, int sec, int f8, f2 (&y)[4]) {
  const f4 y0 = *(LAS f4*)(c.L + L_EC + sec * 256 + f8 * 32), y1 = *(LAS f4*)(c.L + L_EC + sec * 256 + f8 * 32 + 16);
  y[0] = f2{y0.x, y0.y}; y[1] = f2{y0.z, y0.w}; y[2] = f2{y1.x, y1.y}; y[3] = f2{y1.z, y1.w};
}
DI void epi2(const Ctx& c, int t, f2 (&x1)[2][4]) {
  t = fresh(t);
  const int tk = t >> 3, f8 = t & 7;
  f2 ga[4], a2[4], sb[4], p[2][4];
  ec_read(c, 0, f8, ga); ec_read(c, 1, f8, a2); ec_read(c, 2, f8, sb);
#pragma unroll
  for (int k = 0; k < 2; ++k) {
    f2 y[4];
    red_read(c, tk + 32 * k, f8, y);
#pragma unroll
    for (int e = 0; e < 4; ++e) { x1[k][e] = fma2(ga[e], y[e], x1[k][e]); p[k][e] = x1[k][e] * a2[e]; }
    stf(c.rs, v4u{pk2(p[k][0]), pk2(p[k][1]), pk2(p[k][2]), pk2(p[k][3])},
        ((f8 >> 2) * 4 + (tk >> 4) + 2 * k) * 1024 + ((f8 & 3) * 16 + (tk & 15)) * 16, OFF_XA + (c.g * 16 + 2 * c.j) * 4096);
  }
  SB();
#pragma unroll
  for (int k = 0; k < 2; ++k) {
    f2 vx = x1[k][0], vxx = x1[k][0] * x1[k][0], vp = p[k][0], vpp = p[k][0] * p[k][0], vpa = p[k][0] * a2[0], vps = p[k][0] * sb[0];
#pragma unroll
    for (int e = 1; e < 4; ++e) {
      vx += x1[k][e]; vxx = fma2(x1[k][e], x1[k][e], vxx); vp += p[k][e]; vpp = fma2(p[k][e], p[k][e], vpp);
      vpa = fma2(p[k][e], a2[e], vpa); vps = fma2(p[k][e], sb[e], vps);
    }
    float sx = vx.x + vx.y, sxx = vxx.x + vxx.y, sp = vp.x + vp.y, spp = vpp.x + vpp.y, spa = vpa.x + vpa.y, sps = vps.x + vps.y;
    sum8x6(sx, sxx, sp, spp, spa, sps);
    const v4u sw = (__builtin_bit_cast(v4u, f8 == 0 ? f4{sx, sxx, sp, spp} : f4{spa, sps, spa, sps}) & 0xffffff00u) | (c.tag & 0xffu);
    if (f8 < 2) stf(c.rs, sw, (tk + 32 * k) * 16 + f8 * 1024, OFF_STAT + (c.g * 8 + c.j) * 2048);
  }
}
DI void epi4(const Ctx& c, int t, v4u b2v, const f2 (&x1)[2][4]) {
  t = fresh(t);
  const int tk = t >> 3, f8 = t & 7;
  const rsrc_t ro = __builtin_amdgcn_make_buffer_rsrc((void*)c.out_, 0, 2048 * 512 * 2, 0x00020000);
  f2 gb[4];
  ec_read(c, 3, f8, gb);
#pragma unroll
  for (int k = 0; k < 2; ++k) {
    f2 y[4];
    red_read(c, tk + 32 * k, f8, y);
    v4u o;
#pragma unroll
    for (int e = 0; e < 4; ++e) o[e] = pk2(fma2(gb[e], y[e] + unpk(b2v[e]), x1[k][e]));
    __builtin_amdgcn_raw_buffer_store_b128(o, ro, (tk + 32 * k) * 1024 + f8 * 16 + c.g * 65536 + c.j * 128, 0, 0);
  }
}

DI void tail_c(const Ctx& c, bool two, int tid STP) {
  tid = fresh(tid);
  const int l = tid & 63, i = l & 15, w = c.w, g = c.g, j = c.j;
  LAS char* L = c.L;
  const unsigned w3off = OFF_W3T + c.tile0 * 16384;
  v4u w3a[16], w3b[16], w4[44];
  WAITV(0);
  meet4(L, L_W + 32, w, c.tag, l);
  if (w == 2) raise(c.rs, OFF_GFLAG + (g * 8 + j) * 128, c.tag, l);
  ST(11);
  auto o_slot = [&](int slot) { dma<4>(c.rsv, l * 16, OFF_O + (g * 16 + ((slot + 2 * j) & 15)) * 4096, c.Lb + slot * 4096); };
  if (w < 2) o_slot(w);
  const float b1a = bflo(((const GAS u16*)c.b1_)[c.tile0 * 16 + i]), b1b = bflo(((const GAS u16*)c.b1_)[(c.tile0 + (two ? 1 : 0)) * 16 + i]);
  if (w == 3) poll8<PD1>(c.gfl, c.tag, l);
  if (w == 2) {
    __builtin_amdgcn_s_sleep(14);
    _Pragma("nounroll") for (unsigned s = 0; s < SPIN_CAP; ++s) {
      const v4u rf = ld_rflags(c.rs, l);
      if (__all(rf.x == c.tag && rf.y == c.tag && rf.z == c.tag && rf.w == c.tag)) break;
      __builtin_amdgcn_s_sleep(8);
      asm volatile("" ::: "memory");
    }
    if (g == 0 && j == 0 && l == 0) *c.epoch = c.tag;
  }
  WAITV(0);
  ST(17);
  SBAR();
  ST(8);
  o_slot(2 + w); o_slot(6 + w);
  v4u m1[4], m2[4];
  const unsigned o1 = (1536 + 256 * w + 4 * l) * 4, o2 = (((l & 16) ? 2560 : 1024) + 64 * j + 4 * i) * 4;
  WAITV_BAR(4); SL(20);
  o_slot(10 + w); o_slot(14 + (w & 1));
  WAITV_BAR(8);
  mod_get(c.rs, o1, m1);
  mod_get(c.rs, o2, m2);
  WAITV_BAR(12);
#pragma unroll
  for (int n = 0; n < 4; ++n) w3a[n] = LDW(n, w3off);
  WAITV_BAR(12); SL(21);
  {
    const v2u p = mod_sum(c.rs, o1, m1, c.tag, w < 2 ? 0.f : 1.f);
    const int u = L_UV + (w < 2 ? 2048 : 0) + (w & 1) * 512 + l * 8;
    *(LAS v2u*)(L + u) = p; *(LAS v2u*)(L + u + 1024) = p;
    if ((w & 1) * 4 + (l >> 4) == j) *(LAS f4*)(L + L_EC + (w < 2 ? 512 : 256) + i * 16) = widen(p);
    const v2u p2 = mod_sum(c.rs, o2, m2, c.tag, 0.f);
    if (w == 3 && l < 32) *(LAS f4*)(L + L_EC + ((l & 16) ? 768 : 0) + i * 16) = widen(p2);
    if (w < 2) { const unsigned f = w ? 0u : 0x3f803f80u; *(LAS v4u*)(L + L_UV + 4096 + w * 1024 + l * 16) = v4u{f, f, f, f}; }
    asm volatile("s_waitcnt lgkmcnt(0)" ::: "memory");
    post(L, L_W + 48 + 4 * w, c.tag);
  }
#pragma unroll
  for (int n = 4; n < 16; ++n) w3a[n] = LDW(n, w3off);
  if (two) {
#pragma unroll
    for (int n = 0; n < 16; ++n) w3b[n] = LDW(16 + n, w3off);
  }
  wait4(L, L_W + 48, c.tag, l);
  float S[5] = {0.f, 0.f, 0.f, 0.f, 0.f};
  if (w == 3) {
    const v4u av = *(LAS v4u*)(L + L_UV + l * 16), sv = *(LAS v4u*)(L + L_UV + 2048 + l * 16);
#pragma unroll
    for (int e = 0; e < 4; ++e) {
      const float a0 = bflo(av[e]), a1 = bfhi(av[e]), s0 = bflo(sv[e]), s1 = bfhi(sv[e]);
      S[0] += a0 + a1; S[1] += s0 + s1; S[2] += a0 * a0 + a1 * a1; S[3] += a0 * s0 + a1 * s1; S[4] += s0 * s0 + s1 * s1;
    }
#pragma unroll
    for (int e = 0; e < 5; ++e) S[e] = swap32_sum(swap16_sum(sum16(S[e])));
  }
  WAITV(0);
  ST(9);
  float uvt[2];
  col_terms<2>(c, two, l, w3a, w3b, b1a, b1b, uvt);
  USE(uvt[0]); USE(uvt[1]);
  if (w == 3) {
    wait4(L, L_W + 96, c.tag, l);
    poll8<PD2>(c.gfl + GSEAM, c.tag, l);
    ST(15);
#pragma unroll
    for (int k = 2; k < 10; ++k) dma<4>(c.rsv, l * 16, OFF_XA + (g * 16 + ((k + 2 * j) & 15)) * 4096, c.Lb + k * 4096);
  }
  SBAR();
  if (w < 3) {
#pragma unroll
    for (int k = 0; k < 2; ++k) dma<4>(c.rsv, l * 16, OFF_XA + (g * 16 + ((10 + 2 * w + k + 2 * j) & 15)) * 4096, c.Lb + (10 + 2 * w + k) * 4096);
  }
  ST(10);
  f4 acc[4][2];
  mlp1_loop<2, 2>(c, l, w3a, w3b, acc, w4, 0, two, S STA);
  BAR();
  ST(12);
  mlp1_epi<2>(c, two, l, uvt, acc);
  ST(18);
  WAITV_BAR(0);
  ST(13);
  const int rot4 = (c.t03 + 1) >> 1;
  auto h_slot = [&](int sg) {
    int k = rot4 + sg; if (k >= 43) k -= 43;
    dma<4>(c.rsv, l * 16, OFF_H1 + (g * 43 + k) * 4096, c.Lb + (sg >= 38 ? sg - 38 : sg) * 4096);
  };
  if (w == 3) {
    poll8<PD3>(c.gfl + 2 * GSEAM, c.tag, l);
    h_slot(5); h_slot(6); h_slot(7);
  } else {
    h_slot(w); if (w < 2) h_slot(3 + w);
    WAITV(0);
  }
  SBAR();
  ST(14);
  h_slot(8 + w); h_slot(12 + w);
#pragma unroll
  for (int m = 1; m < 10; ++m) {
    if (m == 1 || m == 7) WAITV_BAR(8);
    else if (m < 7) WAITV_BAR(12);
    else if (m == 8) WAITV_BAR(4);
    else WAITV_BAR(0);
    if (m == 1) SL(26);
    if (m == 5) SL(27);
    if (m == 9) SL(28);
    if (m == 1) h_slot(16 + w);
    if (m + 4 < 10 || (m + 4 == 10 && w < 3)) h_slot(4 * (m + 4) + w);
  }
}

extern "C" __global__ void __launch_bounds__(512, 2)
fused_block(const u16* __restrict__ x_, const u16* __restrict__ wqkv_, const u16* __restrict__ wo_, const u16* __restrict__ w1_,
            const u16* __restrict__ b1_, const u16* __restrict__ w2_, const u16* __restrict__ b2_, const u16* __restrict__ c_,
            const u16* __restrict__ wmod_, u16* __restrict__ out_, unsigned char* __restrict__ ws_) {
  extern __shared__ __attribute__((aligned(16))) char smem[];
  asm volatile("" ::"s"(x_), "s"(wqkv_), "s"(wo_), "s"(w1_), "s"(b1_), "s"(w2_), "s"(b2_), "s"(c_), "s"(wmod_), "s"(out_), "s"(ws_));
  unsigned long long pc;
  asm volatile("s_getpc_b64 %0" : "=s"(pc));
  LAS char* L = (LAS char*)smem;
  const unsigned Lb = (unsigned)(uintptr_t)L;
  const int tid0 = threadIdx.x, w = __builtin_amdgcn_readfirstlane(tid0 >> 6);
  const int b = blockIdx.x, xcd = b & 7, idx = b >> 3, g = xcd * 4 + (idx >> 3), j = idx & 7;
  GAS unsigned char* ws = (GAS unsigned char*)ws_;
  const rsrc_t rs = __builtin_amdgcn_make_buffer_rsrc((void*)ws_, 0, WS_USED, 0x00020000);
  const v4u rsv = {(unsigned)(uintptr_t)ws_, (unsigned)((uintptr_t)ws_ >> 32) & 0xffffu, WS_USED, 0x00020000u};
  const unsigned long long tstart_ = __builtin_amdgcn_s_memrealtime();
  ST(0);
  unsigned epoch = *(const __attribute__((address_space(4))) unsigned*)(uintptr_t)(ws_ + OFF_E);
#define GET_TAG asm volatile("" : "+s"(epoch) : : "memory"); const unsigned tag = epoch + 1u;
  const int rq = xcd >> 1;
  const int nt3 = 11 - (j >= 6), t03 = 11 * j - (j == 7);
  gu32* gfl = (gu32*)(ws + OFF_GFLAG) + g * 8 * FSTR;
  const v4u rsq = {(unsigned)(uintptr_t)wqkv_, (unsigned)((uintptr_t)wqkv_ >> 32) & 0xffffu, 512u * 1536u * 2u, 0x00020000u};
#define W2POS(s) (L_W2 + (s) * 4096)
#define WSV(stride) ((8 * (w & 3) + (l >> 3)) * (stride) + 128 * j + 16 * ((l & 7) ^ ((((l >> 4) & 1) | ((w & 1) << 1)) << 1)))
#define LANE_IDS const int l = lane_id(), tid = w * 64 + l, i = l & 15, q = l >> 4; const unsigned w1v = WSV(3072);
#define RETILE_IDS(v) \
    const int u = 4 * (b < 64 ? b : b >= 192 ? b - 128 : b + 64) + v; \
    const bool retile = u < 696, isw1 = u < 352; \
    const int rcb = isw1 ? u >> 4 : (int)((unsigned)(u - 352) / 43u), rks = isw1 ? (u & 15) : u - 352 - 43 * rcb; \
    const int rnt = (isw1 && rcb == 21) ? 2 : 4, rst = v < 3 ? L_RT + v * 4096 : L_EC; \

  v4u xr[5][4];
  if (w < 4) {
    const int l = lane_id();
    unsigned dummy;
    v2u cw; v4u wm[4];
    const int mcb = b & 15, mks = b >> 4;
    if (b < 64) { __builtin_amdgcn_s_setprio(3); mod_loads(c_, wmod_, true, mcb, mks, w, l, cw, wm); }
    asm volatile("s_load_dword %0, %1, 0x0\n\ts_load_dword %0, %1, 0x40\n\ts_load_dword %0, %1, 0x80\n\t"
                 "s_load_dword %0, %1, 0xc0\n\ts_load_dword %0, %1, 0x100\n\ts_load_dword %0, %1, 0x140"
                 : "=&s"(dummy) : "s"((pc & ~255ull) + idx * CODE_STEP + w * 384));
    if (b < 64) {
      asm volatile("" : "+s"(epoch) : : "memory");
      MOD_WAIT(0);
      mod_publish(rs, mcb, mks, w, l, mod_job(L, L_MRED, w, l, epoch + 1u, cw, wm));
      __builtin_amdgcn_s_setprio(0);
      SBAR();
    }
    ST(29);
    const unsigned w1v = WSV(3072);
#pragma unroll
    for (int s0 = 0; s0 < 11; ++s0) {
      const unsigned t = s0 + 2 * (xcd & 1), s = s0 >= 6 ? s0 : t < t - 6u ? t : t - 6u;
      dma3(rsq, w1v, ((s + 4 * rq) & 15) * (32 * 3072), Lb + (s0 >= 6 ? L_R1 + (s0 - 6) * 12288 : s * 12288) + w * 3072);
    }
    asm volatile("s_waitcnt lgkmcnt(0)" : "+s"(dummy) : : "memory");
    ST(30);
    WAITV(9);
  } else {
    const int l = lane_id();
    __builtin_amdgcn_s_setprio(3);
#pragma unroll
    for (int p = 0; p < 5; ++p) {
      int tok = 64 * g - 16 + 20 * (w & 3) + (l >> 4) + 4 * p;
      tok = tok < 0 ? 0 : tok;
#pragma unroll
      for (int n = 0; n < 4; ++n)
        asm volatile("global_load_dwordx4 %0, %1, %2" : "=v"(xr[p][n]) : "v"(tok * 1024 + 256 * ((n + rq) & 3) + 16 * (l & 15)), "s"(x_));
      if (p == XFIRST - 1 && b < 64) SBAR();
    }
    if (w == 7 && l < 32) gdma4((const GAS char*)wo_ + (b * 32 + l) * 64, Lb + L_EC);
    ST(29);
    SB();
  }
  if (w < 4) {
    asm volatile("; matrix waves" ::: "memory");
    LANE_IDS
    GET_TAG
    ST(2);
    meet4(L, L_SYNC, w, tag, l);
    ST(3);
    const int tb_ = q * 3072 + (i >> 2) * 128 + ((w ^ ((i >> 3) | ((q & 1) << 1))) * 32) + (i & 3) * 8;
    const int tb1 = fresh(tb_ + L_R1);
    v4u wf[16][3];
#pragma unroll
    for (int s = 0; s < 8; ++s)
#pragma unroll
      for (int sg = 0; sg < 3; ++sg) wf[s][sg] = trfrag(L + (s < 6 ? tb_ + s * 12288 : tb1 + (s - 6) * 12288) + sg * 1024);
    {
      const int v = w;
      RETILE_IDS(v)
    {
      const int nc = isw1 ? 2752 : 1024;
      const v4u rr = mkrs(isw1 ? w1_ : w2_, retile ? 1376u * 1024u : 0u);
      const unsigned vo = (32 * rks + (l >> 3)) * nc + 128 * rcb + 16 * (rnt == 2 ? (l & 3) : (l & 7));
#pragma unroll
      for (int e = 0; e < 4; ++e) dma_nt(rr, vo, 8 * e * nc, Lb + rst + e * 1024);
    }
    }
    ST(7);
    BAR();
    ST(4);
    f4 aq[4], ak[5], av[5];
    const int a0 = l * 16 ^ (q << 4), a1 = l * 16 ^ ((q | 4) << 4), a0h = fresh(a0 + 61440), a1h = fresh(a1 + 61440);
    v4u a[2][5];
    SBAR();
    ST(5);
#pragma unroll
    for (int r = 0; r < 5; ++r) a[0][r] = *(LAS v4u*)(L + r * 1024 + a0);
#pragma unroll
    for (int s = 0; s < 16; ++s) {
      if (s == 7) WAITV(4);
      if (s == 10) WAITV(0);
      if (s == 3 || s == 7 || s == 10 || s == 12 || s == 14) SBAR();
      if (s == 7) SL(31);
      if (s == 10) SL(34);
      SB();
#pragma unroll
      for (int r = 0; r < 5; ++r) {
        MAC(s == 0, ak[r], wf[s][1], a[s & 1][r]);
        SB();
        if (s < 11) a[(s + 1) & 1][r] = *(LAS v4u*)(L + ((s + 1) * 5 + r) * 1024 + (((s + 1) & 1) ? a1 : a0));
        else if (s < 15) a[(s + 1) & 1][r] = *(LAS v4u*)(L + ((s - 11) * 5 + r) * 1024 + (((s + 1) & 1) ? a1h : a0h));
        SB();
        MAC(s == 0, av[r], a[s & 1][r], wf[s][2]);
        SB();
        if (s >= 7 && s < 15 && r < 3) { wf[s + 1][r] = trfrag(L + (s < 10 ? tb1 + (s - 5) * 12288 : s < 12 ? tb1 + (s - 10) * 12288 : s < 14 ? tb_ + (s - 12) * 12288 : tb1 + 2 * 12288) + r * 1024); SB(); }
        if (r) { MAC(s == 0, aq[r - 1], wf[s][0], a[s & 1][r]); SB(); }
      }
    }
    MFMA_DONE();
    post(L, L_W + 16 + 4 * w, tag);
    SB();
    ST(6);
    {
      v2u qb[4], kb[5], vb[5];
#pragma unroll
      for (int r = 0; r < 5; ++r) {
        kb[r] = v2u{pk(ak[r].x, ak[r].y), pk(ak[r].z, ak[r].w)};
        vb[r] = v2u{pk(av[r].x, av[r].y), pk(av[r].z, av[r].w)};
        if (r) qb[r - 1] = v2u{pk(aq[r - 1].x, aq[r - 1].y), pk(aq[r - 1].z, aq[r - 1].w)};
      }
      const bool odd = i & 1;
      const bool cur0 = 4 * q + (i & 1) <= i, cur1 = 4 * q + 2 + (i & 1) <= i;
      const float sc = 0.25f * 1.4426950409f;
      const int hd = 4 * j + w;
      const f4 z = {0.f, 0.f, 0.f, 0.f};
      f4 sp[4], scu[4];
#pragma unroll
      for (int r = 0; r < 4; ++r) { sp[r] = mfma16(kb[r], qb[r], z); scu[r] = mfma16(kb[r + 1], qb[r], z); }
      const unsigned lo = odd ? 0x01000c0cu : 0x0c0c0100u, hi = odd ? 0x03020c0cu : 0x0c0c0302u, no = 0x0c0c0c0cu;
      const unsigned sel0 = cur0 ? no : lo, sel1 = cur1 ? no : hi, sel2 = cur0 ? lo : no, sel3 = cur1 ? hi : no;
      float v0[4], v1[4], m[4];
#pragma unroll
      for (int r = 0; r < 4; ++r) {
        float p0 = odd ? sp[r].y : sp[r].x, p1 = odd ? sp[r].w : sp[r].z;
        const float c0 = odd ? scu[r].y : scu[r].x, c1 = odd ? scu[r].w : scu[r].z;
        if (r == 0 && g == 0) { p0 = -4e9f; p1 = -4e9f; }
        v0[r] = cur0 ? c0 : p0; v1[r] = cur1 ? c1 : p1;
        m[r] = max2(v0[r], v1[r]);
      }
      spread4(gather4<true>(m) * sc, m);
#pragma unroll
      for (int r = 0; r < 4; ++r) {
        v0[r] = __builtin_amdgcn_exp2f(__builtin_fmaf(v0[r], sc, -m[r])); v1[r] = __builtin_amdgcn_exp2f(__builtin_fmaf(v1[r], sc, -m[r]));
        m[r] = v0[r] + v1[r];
      }
      spread4(__builtin_amdgcn_rcpf(gather4<false>(m)), m);
      f4 o[4];
#pragma unroll
      for (int r = 0; r < 4; ++r) {
        const unsigned pp = pk(v0[r] * m[r], v1[r] * m[r]);
        const v4u pf = {__builtin_amdgcn_perm(0u, pp, sel0), __builtin_amdgcn_perm(0u, pp, sel1), __builtin_amdgcn_perm(0u, pp, sel2), __builtin_amdgcn_perm(0u, pp, sel3)};
        o[r] = mfma32(v4u{vb[r].x, vb[r].y, vb[r + 1].x, vb[r + 1].y}, pf, z);
      }
      unsigned h[4][2];
#pragma unroll
      for (int r = 0; r < 4; ++r) { h[r][0] = pk(o[r].x, o[r].y); h[r][1] = pk(o[r].z, o[r].w); }
#pragma unroll
      for (int pp = 0; pp < 2; ++pp) {
        auto s0 = __builtin_amdgcn_permlane16_swap(h[2 * pp][0], h[2 * pp + 1][0], false, false);
        auto s1 = __builtin_amdgcn_permlane16_swap(h[2 * pp][1], h[2 * pp + 1][1], false, false);
        stf(rs, v4u{s0[0], s1[0], s0[1], s1[1]}, (q & 1) * 1024 + ((q >> 1) * 16 + i) * 16,
            OFF_O + ((g * 16 + (hd >> 1)) * 4 + 2 * pp) * 1024 + (hd & 1) * 512);
      }
    }
    ST(19);
    const bool two = w < nt3 - 8;
    const Ctx c = {rs, rsv, L, Lb, tag, g, j, w, t03, t03 + (two ? 2 * w : nt3 - 8 + w), gfl, x_, b1_, b2_, out_, (gu32*)(ws + OFF_E)};
    tail_c(c, two, tid STA);
    ST(16);
    SLF();
    while (__builtin_amdgcn_s_memrealtime() - tstart_ < 3000ull) __builtin_amdgcn_s_sleep(1);
    __builtin_amdgcn_endpgm();
  } else {
    asm volatile("; helper waves" ::: "memory");
    LANE_IDS
    const int v = w - 4;
    const int row0 = 20 * v + q;
    GET_TAG
    ST(1);
    f2 xn[5][16];
    v4u mv[4];
#pragma unroll
    for (int p = 0; p < 5; ++p) {
      f2 s2 = {0.f, 0.f}, q2 = {0.f, 0.f};
      asm volatile("s_waitcnt vmcnt(%4)" : "+v"(xr[p][0]), "+v"(xr[p][1]), "+v"(xr[p][2]), "+v"(xr[p][3]) : "i"(16 - 4 * p) : "memory");
#pragma unroll
      for (int e = 0; e < 16; ++e) {
        xn[p][e] = unpk(xr[p][e >> 2][e & 3]);
        s2 += xn[p][e];
        q2 = fma2(xn[p][e], xn[p][e], q2);
      }
      const float m = sum16(s2.x + s2.y) * (1.0f / 512.0f);
      const float r = __builtin_amdgcn_rsqf(fmaxf(sum16(q2.x + q2.y) * (1.0f / 512.0f) - m * m, 0.f) + 1e-5f);
      const f2 r2 = {r, r}, mr2 = {-m * r, -m * r};
#pragma unroll
      for (int e = 0; e < 16; ++e) { xn[p][e] = fma2(xn[p][e], r2, mr2); asm volatile("" : "+v"(xn[p][e])); }
    }
    int ha[5];
#pragma unroll
    for (int p = 0; p < 5; ++p) {
      const int r = row0 + 4 * p, qq = i & 3;
      ha[p] = ((i >> 2) * 5 + (r >> 4)) * 1024 + (qq * 16 + ((r & 15) ^ (qq | (i & 4)))) * 16;
      asm volatile("" : "+v"(ha[p]));
    }
    ST(2);
    mod_get(rs, (256 * v + 4 * l) * 4, mv);
    SB();
    RETILE_IDS(v)
    int rdst[4];
#pragma unroll
    for (int t = 0; t < 4; ++t) {
      const int tl = 4 * rcb + t;
      const int j3 = tl < 66 ? (int)((unsigned)tl / 11u) : (tl < 76 ? 6 : 7);
      int s4 = rks - (rcb < 6 ? (11 * rcb + 1) >> 1 : 3 + 5 * rcb); if (s4 < 0) s4 += 43;
      rdst[t] = isw1 ? OFF_W3T + (tl * 16 + ((rks - 2 * j3) & 15)) * 1024
                     : OFF_W4T + ((((rcb * 2 + (t >> 1)) * 2 + (s4 & 1)) * 22 + (s4 >> 1)) * 2 + (t & 1)) * 1024;
      if (!retile || t >= rnt) rdst[t] = 0x10000000;
      asm volatile("" : "+s"(rdst[t]));
    }
    *(LAS f4*)(L + L_MODF + (256 * v + 4 * l) * 4) = widen(mod_sum(rs, (256 * v + 4 * l) * 4, mv, tag, v < 2 ? 0.f : 1.f));
    WAITV(0);
    asm volatile("s_waitcnt lgkmcnt(0)" ::: "memory");
    ST(7);
    post(L, L_SYNC + 16 + 4 * v, tag);
    wait4(L, L_SYNC + 16, tag, l);
    v4u ho[4][5];
    {
      f4 md[2][4];
      auto get = [&](int n) {
        const int ch = L_MODF + (16 * ((n + rq) & 3) + i) * 32;
        md[n & 1][0] = *(LAS f4*)(L + ch); md[n & 1][1] = *(LAS f4*)(L + ch + 16); md[n & 1][2] = *(LAS f4*)(L + ch + 2048); md[n & 1][3] = *(LAS f4*)(L + ch + 2064);
      };
      get(0);
#pragma unroll
      for (int n = 0; n < 4; ++n) {
        if (n < 3) get(n + 1);
        SB();
        const f4 (&m)[4] = md[n & 1];
        const f2 sa2[4] = {{m[0].x, m[0].y}, {m[0].z, m[0].w}, {m[1].x, m[1].y}, {m[1].z, m[1].w}}, ca2[4] = {{m[2].x, m[2].y}, {m[2].z, m[2].w}, {m[3].x, m[3].y}, {m[3].z, m[3].w}};
#pragma unroll
        for (int p = 0; p < 5; ++p)
#pragma unroll
          for (int e = 0; e < 4; ++e) xn[p][4 * n + e] = fma2(xn[p][4 * n + e], ca2[e], sa2[e]);
        SB();
#pragma unroll
        for (int p = 0; p < 5; ++p)
#pragma unroll
          for (int e = 0; e < 4; ++e) ho[n][p][e] = pk2(xn[p][4 * n + e]);
        SB();
      }
#pragma unroll
      for (int n = 0; n < 4; ++n)
#pragma unroll
        for (int p = 0; p < 5; ++p) asm volatile("" : "+v"(ho[n][p]));
    }
    __builtin_amdgcn_s_setprio(0);
    ST(3);
    SBAR();
    ST(4);
#pragma unroll
    for (int p = 0; p < 5; ++p) *(LAS v4u*)(L + ha[p]) = ho[0][p];
    BAR();
    ST(5);
    auto w1_step = [&](int s, int at) { dma3(rsq, w1v, ((s + 4 * rq) & 15) * (32 * 3072), Lb + at + v * 3072); };
#pragma unroll
    for (int n = 1; n < 4; ++n) {
#pragma unroll
      for (int p = 0; p < 5; ++p) *(LAS v4u*)(L + ha[p] + n * 20480) = ho[n][p];
      if (n == 1) { w1_step(11, L_R1); w1_step(12, L_R1 + 12288); BAR(); }
    }
    BAR();
    w1_step(13, 0); w1_step(14, 12288);
    ST(34);
    WAITV(6);
    SBAR();
    w1_step(15, L_R1 + 2 * 12288);
#pragma unroll
    for (int t = 0; t < 4; ++t) stf(rs, trfrag(L + rst + (8 * q + (i >> 2)) * 128 + t * 32 + (i & 3) * 8), l * 16, rdst[t]);
    WAITV(7);
    SBAR();
    const bool job2 = b >= 64 && b < 192;
    v2u cw; v4u wm[4];
    mod_loads(c_, wmod_, job2, 16 + ((b - 64) & 31), ((b - 64) >> 5) & 3, v, fresh(l), cw, wm);
    WAITV(9);
    SBAR();
    ST(6);
    const Ctx c = {rs, rsv, L, Lb, tag, g, j, w, t03, t03 + nt3 - 8 + w, gfl, x_, b1_, b2_, out_, (gu32*)(ws + OFF_E)};
    const int nh = v & 1, kh = v >> 1;
    v4u w2[16], w3a[16], w3b[16], w4[44];
    WAITV(5);
    meet4(L, L_W + 128, v, tag, l);
    if (tid == 320) __hip_atomic_store((gu32*)(ws + OFF_RFLAG) + 32 * g + j, tag, RLX_AGENT);
    ST(35);
    wait4(L, L_W + 16, tag, l);
    {
      const v4u rso = {(unsigned)(uintptr_t)wo_, (unsigned)((uintptr_t)wo_ >> 32) & 0xffffu, 512u * 512u * 2u, 0x00020000u};
      const unsigned w2v = WSV(1024);
#pragma unroll
      for (int s = 0; s < 16; ++s) dma_in(rso, w2v, ((s + 2 * j) & 15) * 32768, Lb + W2POS(s) + v * 1024);
    }
    v4u xq[2];
    {
      const rsrc_t rx = __builtin_amdgcn_make_buffer_rsrc((void*)x_, 0, 2048 * 512 * 2, 0x00020000);
      const int t = tid - 256;
#pragma unroll
      for (int k = 0; k < 2; ++k) xq[k] = __builtin_amdgcn_raw_buffer_load_b128(rx, ((t >> 3) + 32 * k) * 1024 + (t & 7) * 16, g * 65536 + j * 128, 0);
    }
    const float b1a = bflo(((const GAS u16*)b1_)[c.tile0 * 16 + i]);
    SB();
    MOD_WAIT(19);
    v4u res;
    if (job2) res = mod_job(L, L_MODF + 2048, v, fresh(l), tag, cw, wm);
    WAITV(0);
    if (job2) mod_publish(rs, 16 + ((b - 64) & 31), (b - 64) >> 5, v, l, res);
    ST(31);
    meet4(L, L_W + 64, v, tag, l);
    {
      const int tb2 = fresh(L_W2 + kh * 4096 + (8 * q + (i >> 2)) * 128 + (i & 3) * 8), f = (i >> 3) | ((q & 1) << 1);
      const int t0 = tb2 + ((2 * nh) ^ f) * 32, t1 = tb2 + ((2 * nh + 1) ^ f) * 32;
#pragma unroll
      for (int n = 0; n < 8; ++n) { w2[2 * n] = trfrag(L + t0 + n * 8192); w2[2 * n + 1] = trfrag(L + t1 + n * 8192); }
    }
    f2 x1[2][4];
#pragma unroll
    for (int k = 0; k < 2; ++k)
#pragma unroll
      for (int e = 0; e < 4; ++e) { x1[k][e] = unpk(xq[k][e]); asm volatile("" : "+v"(x1[k][e])); }
    ST(19);
    BAR();
    ST(8);
    {
      f4 acc[4][2];
      v4u a2[2][4];
      const int ab = kh * 4096 + l * 16;
#pragma unroll
      for (int r = 0; r < 4; ++r) a2[0][r] = *(LAS v4u*)(L + ab + r * 1024);
#pragma unroll
      for (int n = 0; n < 8; ++n) {
        if (!(n & 1)) SBAR();
        if (n == 0) SL(20);
        SB();
#pragma unroll
        for (int r = 0; r < 4; ++r) {
          MAC(n == 0, acc[r][0], w2[2 * n], a2[n & 1][r]);
          SB();
          if (n < 7) a2[(n + 1) & 1][r] = *(LAS v4u*)(L + ab + ((n + 1) * 8 + r) * 1024);
          SB();
          MAC(n == 0, acc[r][1], w2[2 * n + 1], a2[n & 1][r]);
          SB();
        }
      }
      MFMA_DONE();
      SL(21);
      red_write(c, l, nh, kh, acc);
    }
    meet4(L, L_W + 80, v, tag, l);
    ST(36);
    wait4(L, L_W + 48, tag, l);
    ST(25);
    __builtin_amdgcn_s_setprio(1);
    epi2(c, tid - 256, x1);
    __builtin_amdgcn_s_setprio(0);
    ST(17);
    asm volatile("" ::: "memory");
    const unsigned w3off = OFF_W3T + c.tile0 * 16384, w4off = OFF_W4T + ((j * 2 + nh) * 2 + kh) * 44 * 1024;
#pragma unroll
    for (int n = 0; n < 6; ++n) w3a[n] = LDW(n, w3off);
    WAITV(8);
    meet4(L, L_W + 96, v, tag, l);
    if (w == 6) raise(rs, OFF_GFLAG + (256 + g * 8 + j) * 128, tag, l);
    ST(9);
    if (v < 2) dma<4>(rsv, l * 16, OFF_XA + (g * 16 + 2 * j + v) * 4096, Lb + v * 4096);
#pragma unroll
    for (int n = 6; n < 16; ++n) w3a[n] = LDW(n, w3off);
    SB();
    float uvt[1];
    col_terms<1>(c, false, l, w3a, w3b, b1a, b1a, uvt);
    USE(uvt[0]);
    if (v < 2) WAITV(0);
    SBAR();
    ST(10);
    {
      f4 acc[4][1];
      const float S[5] = {0.f, 0.f, 0.f, 0.f, 0.f};
      mlp1_loop<1, 1>(c, l, w3a, w3b, acc, w4, w4off, false, S STA);
      BAR();
      ST(12);
      mlp1_epi<1>(c, false, l, uvt, acc);
    }
    asm volatile("" ::: "memory");
    const rsrc_t rb2 = __builtin_amdgcn_make_buffer_rsrc((void*)b2_, 0, 1024, 0x00020000);
    const v4u b2v = __builtin_amdgcn_raw_buffer_load_b128(rb2, (l & 7) * 16, j * 128, 0);
    ST(18);
    WAITV_BAR(1);
    if (w == 4) raise(rs, OFF_GFLAG + (512 + g * 8 + j) * 128, tag, l);
    ST(13);
    {
      f4 acc[4][2];
      v4u a4[2][4];
      const int ab = kh * 4096 + l * 16, abh = fresh(ab + 65536), abx = fresh(ab + 131072);
      SBAR();
      ST(14);
#pragma unroll
      for (int r = 0; r < 4; ++r) a4[0][r] = *(LAS v4u*)(L + ab + r * 1024);
#pragma unroll
      for (int n = 0; n < 22; ++n) {
        if ((n & 1) && n < 19) SBAR();
        if (n == 1) SL(26);
        if (n == 9) SL(27);
        if (n == 17) SL(28);
        SB();
        const int sp = 2 * (n + 1) >= 38 ? 2 * (n + 1) - 38 : 2 * (n + 1);
        if (n < 21 || kh == 0) {
#pragma unroll
          for (int r = 0; r < 4; ++r) {
            MAC(n == 0, acc[r][0], w4[2 * n], a4[n & 1][r]);
            SB();
            if (n < 21) a4[(n + 1) & 1][r] = *(LAS v4u*)(L + (sp < 16 ? ab : sp < 32 ? abh : abx) + (sp & 15) * 4096 + r * 1024);
            SB();
            MAC(n == 0, acc[r][1], w4[2 * n + 1], a4[n & 1][r]);
            SB();
          }
        }
      }
      MFMA_DONE();
      ST(15);
      red_write(c, l, nh, kh, acc);
    }
    meet4(L, L_W + 112, v, tag, l);
    epi4(c, tid - 256, b2v, x1);
    ST(16);
    SLF();
    __builtin_amdgcn_s_waitcnt(0x0F70);
    while (__builtin_amdgcn_s_memrealtime() - tstart_ < 3000ull) __builtin_amdgcn_s_sleep(1);
    __builtin_amdgcn_endpgm();
  }
}

__global__ void k_first(unsigned long long* p) { if (threadIdx.x == 0) *p = __builtin_amdgcn_s_memrealtime(); }
extern "C" void kernel_launch(void* const* d_in, const int* in_sizes, int n_in, void* d_out, int out_size, void* d_ws, size_t ws_size,
                              hipStream_t stream) {
  (void)hipFuncSetAttribute((const void*)fused_block, hipFuncAttributeMaxDynamicSharedMemorySize, LDS_BYTES);
  fused_block<<<256, 512, LDS_BYTES, stream>>>((const u16*)d_in[0], (const u16*)d_in[1], (const u16*)d_in[2], (const u16*)d_in[4],
                                               (const u16*)d_in[5], (const u16*)d_in[6], (const u16*)d_in[7], (const u16*)d_in[8],
                                               (const u16*)d_in[9], (u16*)d_out, (unsigned char*)d_ws);
  k_first<<<1, 64, 0, stream>>>((unsigned long long*)((char*)d_ws + (32 << 20)));
}
```

```cpp
#include <hip/hip_runtime.h>
#include <stdint.h>

#define GAS __attribute__((address_space(1)))
#define LAS __attribute__((address_space(3)))
#define DI __device__ __forceinline__
typedef unsigned short u16;
typedef unsigned v4u __attribute__((ext_vector_type(4)));
typedef unsigned v2u __attribute__((ext_vector_type(2)));
typedef float f4 __attribute__((ext_vector_type(4)));
typedef float f2 __attribute__((ext_vector_type(2)));
typedef __bf16 bf2 __attribute__((ext_vector_type(2)));
typedef __bf16 bf8 __attribute__((ext_vector_type(8)));
typedef short s4 __attribute__((ext_vector_type(4)));
typedef GAS unsigned gu32;
typedef GAS unsigned long long gu64;
#define RLX_AGENT __ATOMIC_RELAXED, __HIP_MEMORY_SCOPE_AGENT

constexpr unsigned OFF_E = 0;
constexpr unsigned OFF_RFLAG = 768u << 10;
constexpr unsigned OFF_GFLAG = 800u << 10;
constexpr unsigned OFF_MODP = 16384;
constexpr unsigned OFF_STAT = 65536;
constexpr unsigned OFF_W3T = 1u << 20;
constexpr unsigned OFF_W4T = OFF_W3T + 1376 * 1024;
constexpr unsigned OFF_O = OFF_W4T + 1408 * 1024;
constexpr unsigned OFF_XA = OFF_O + 2048 * 1024;
constexpr unsigned OFF_H1 = OFF_XA + 2048 * 1024;
constexpr unsigned WS_USED = OFF_H1 + 32 * 43 * 4096;
constexpr unsigned OFF_STAMP = 64u << 20;

constexpr int L_R1 = 81920;
constexpr int L_W2 = 81920;
constexpr int L_RED = 65536;
constexpr int L_STAT = 102400;
constexpr int L_UV = 147456;
constexpr int L_RT = 143360;
constexpr int L_MODF = 155648;
constexpr int L_EC = 159744;
constexpr int L_MRED = 77824;
constexpr int L_SYNC = 79872;
constexpr int L_COEF = L_MODF;
constexpr int L_RR = L_COEF + 1024;
constexpr int L_W = L_MODF + 3584;
constexpr int LDS_BYTES = 163840;

constexpr int CODE_STEP = 1216;
constexpr int FSTR = 32, GSEAM = 32 * 8 * 32;
#define PD1 14
#define PD2 18
#define PD3 16
#define PINT 1
#define W4MODE 0
#define XFIRST 2
constexpr unsigned SPIN_CAP = 1u << 18;

#define ST(n)
#define SL(n)
#define SRT(n)
#define SLF()

DI float bflo(unsigned u) { return __uint_as_float(u << 16); }
DI float bfhi(unsigned u) { return __uint_as_float(u & 0xffff0000u); }
DI unsigned pk(float a, float b) { f2 v = {a, b}; return __builtin_bit_cast(unsigned, __builtin_convertvector(v, bf2)); }
DI f2 unpk(unsigned u) { return f2{bflo(u), bfhi(u)}; }
DI f2 fma2(f2 a, f2 b, f2 c) { return __builtin_elementwise_fma(a, b, c); }
DI unsigned pk2(f2 v) { return __builtin_bit_cast(unsigned, __builtin_convertvector(v, bf2)); }
DI f4 mfma32(v4u a, v4u b, f4 c) { return __builtin_amdgcn_mfma_f32_16x16x32_bf16(__builtin_bit_cast(bf8, a), __builtin_bit_cast(bf8, b), c, 0, 0, 0); }
template <bool FIRST> DI void mac(f4& c, v4u a, v4u b) {
  if constexpr (FIRST) asm volatile("v_mfma_f32_16x16x32_bf16 %0, %1, %2, 0" : "=&v"(c) : "v"(a), "v"(b));
  else asm volatile("v_mfma_f32_16x16x32_bf16 %0, %1, %2, %0" : "+v"(c) : "v"(a), "v"(b));
}
#define MAC(first, c, a, b) do { if (first) mac<true>(c, a, b); else mac<false>(c, a, b); } while (0)
#define MFMA_DONE() do { asm volatile("s_nop 15" ::: "memory"); __builtin_amdgcn_sched_barrier(0); } while (0)
DI f4 mfma16(v2u a, v2u b, f4 c) { return __builtin_amdgcn_mfma_f32_16x16x16bf16_1k(__builtin_bit_cast(s4, a), __builtin_bit_cast(s4, b), c, 0, 0, 0); }
template <int CTRL> DI float dpp(float x) { return __builtin_bit_cast(float, __builtin_amdgcn_update_dpp(0, __builtin_bit_cast(int, x), CTRL, 0xf, 0xf, true)); }
DI float swap16_sum(float x) { auto r = __builtin_amdgcn_permlane16_swap(__float_as_uint(x), __float_as_uint(x), false, false); return __uint_as_float(r[0]) + __uint_as_float(r[1]); }
DI float swap32_sum(float x) { auto r = __builtin_amdgcn_permlane32_swap(__float_as_uint(x), __float_as_uint(x), false, false); return __uint_as_float(r[0]) + __uint_as_float(r[1]); }
DI float swap16_max(float x) { auto r = __builtin_amdgcn_permlane16_swap(__float_as_uint(x), __float_as_uint(x), false, false); return fmaxf(__uint_as_float(r[0]), __uint_as_float(r[1])); }
DI float swap32_max(float x) { auto r = __builtin_amdgcn_permlane32_swap(__float_as_uint(x), __float_as_uint(x), false, false); return fmaxf(__uint_as_float(r[0]), __uint_as_float(r[1])); }
DI float max2(float a, float b) { return __builtin_amdgcn_fmed3f(a, b, __builtin_inff()); }
template <bool MAX> DI float gather4(const float (&x)[4]) {
  auto a = __builtin_amdgcn_permlane16_swap(__float_as_uint(x[0]), __float_as_uint(x[1]), false, false);
  auto b = __builtin_amdgcn_permlane16_swap(__float_as_uint(x[2]), __float_as_uint(x[3]), false, false);
  const float u = MAX ? max2(__uint_as_float(a[0]), __uint_as_float(a[1])) : __uint_as_float(a[0]) + __uint_as_float(a[1]);
  const float v = MAX ? max2(__uint_as_float(b[0]), __uint_as_float(b[1])) : __uint_as_float(b[0]) + __uint_as_float(b[1]);
  auto c = __builtin_amdgcn_permlane32_swap(__float_as_uint(u), __float_as_uint(v), false, false);
  return MAX ? max2(__uint_as_float(c[0]), __uint_as_float(c[1])) : __uint_as_float(c[0]) + __uint_as_float(c[1]);
}
DI void spread4(float t, float (&x)[4]) {
  auto c = __builtin_amdgcn_permlane32_swap(__float_as_uint(t), __float_as_uint(t), false, false);
  auto a = __builtin_amdgcn_permlane16_swap(c[0], c[0], false, false);
  auto b = __builtin_amdgcn_permlane16_swap(c[1], c[1], false, false);
  x[0] = __uint_as_float(a[0]); x[1] = __uint_as_float(a[1]); x[2] = __uint_as_float(b[0]); x[3] = __uint_as_float(b[1]);
}
DI float sum8(float x) { x += dpp<0xB1>(x); x += dpp<0x4E>(x); x += dpp<0x141>(x); return x; }
DI float sum16(float x) { x = sum8(x); x += dpp<0x140>(x); return x; }

#define BAR() asm volatile("s_waitcnt lgkmcnt(0)\n\ts_barrier" ::: "memory")
#define WAITV(n) do { asm volatile("s_waitcnt vmcnt(%0)" ::"i"(n) : "memory"); if ((n) == 0) __builtin_amdgcn_s_waitcnt(0x0F70); } while (0)
#define WAITV_BAR(n) do { asm volatile("s_waitcnt vmcnt(%0) lgkmcnt(0)\n\ts_barrier" ::"i"(n) : "memory"); if ((n) == 0) __builtin_amdgcn_s_waitcnt(0x0F70); } while (0)
#define SBAR() asm volatile("s_barrier" ::: "memory")
#define USE(x) asm volatile("" ::"v"(x))

template <int N> DI void dma(v4u rs, unsigned voff, unsigned soff, unsigned lds) {
  unsigned keep;
  if constexpr (N == 1)
    asm volatile("s_mov_b32 %0, m0\n\ts_mov_b32 m0, %2\n\ts_nop 0\n\t"
                 "buffer_load_dwordx4 %1, %3, %4 offen sc1 lds\n\t"
                 "s_mov_b32 m0, %0" : "=&s"(keep) : "v"(voff), "s"(lds), "s"(rs), "s"(soff) : "memory");
  else if constexpr (N == 2)
    asm volatile("s_mov_b32 %0, m0\n\ts_mov_b32 m0, %2\n\ts_nop 0\n\t"
                 "buffer_load_dwordx4 %1, %3, %4 offen sc1 lds\n\t"
                 "buffer_load_dwordx4 %1, %3, %4 offen offset:1024 sc1 lds\n\t"
                 "s_mov_b32 m0, %0" : "=&s"(keep) : "v"(voff), "s"(lds), "s"(rs), "s"(soff) : "memory");
  else
    asm volatile("s_mov_b32 %0, m0\n\ts_mov_b32 m0, %2\n\ts_nop 0\n\t"
                 "buffer_load_dwordx4 %1, %3, %4 offen sc1 lds\n\t"
                 "buffer_load_dwordx4 %1, %3, %4 offen offset:1024 sc1 lds\n\t"
                 "buffer_load_dwordx4 %1, %3, %4 offen offset:2048 sc1 lds\n\t"
                 "buffer_load_dwordx4 %1, %3, %4 offen offset:3072 sc1 lds\n\t"
                 "s_mov_b32 m0, %0" : "=&s"(keep) : "v"(voff), "s"(lds), "s"(rs), "s"(soff) : "memory");
}
DI void dma3(v4u rs, unsigned voff, unsigned soff, unsigned lds) {
  unsigned keep;
  asm volatile("s_mov_b32 %0, m0\n\ts_mov_b32 m0, %2\n\ts_nop 0\n\t"
               "buffer_load_dwordx4 %1, %3, %4 offen lds\n\t"
               "buffer_load_dwordx4 %1, %3, %4 offen offset:1024 lds\n\t"
               "buffer_load_dwordx4 %1, %3, %4 offen offset:2048 lds\n\t"
               "s_mov_b32 m0, %0" : "=&s"(keep) : "v"(voff), "s"(lds), "s"(rs), "s"(soff) : "memory");
}
DI v4u trfrag(LAS char* p) {
  v2u lo = __builtin_bit_cast(v2u, __builtin_amdgcn_ds_read_tr16_b64_v4i16((LAS s4*)p));
  v2u hi = __builtin_bit_cast(v2u, __builtin_amdgcn_ds_read_tr16_b64_v4i16((LAS s4*)(p + 512)));
  return v4u{lo.x, lo.y, hi.x, hi.y};
}
DI int lane_id() { int r; asm volatile("v_mbcnt_lo_u32_b32 %0, -1, 0\n\tv_mbcnt_hi_u32_b32 %0, -1, %0" : "=v"(r)); return r; }
DI int fresh(int x) { asm volatile("" : "+v"(x)); return x; }
#define SB() __builtin_amdgcn_sched_barrier(0)
DI void dma_nt(v4u rs, unsigned voff, unsigned soff, unsigned lds) {
  unsigned keep;
  asm volatile("s_mov_b32 %0, m0\n\ts_mov_b32 m0, %2\n\ts_nop 0\n\tbuffer_load_dwordx4 %1, %3, %4 offen nt lds\n\ts_mov_b32 m0, %0"
               : "=&s"(keep) : "v"(voff), "s"(lds), "s"(rs), "s"(soff) : "memory");
}
DI void gdma4(const GAS void* p, unsigned lds) {
  unsigned keep;
  asm volatile("s_mov_b32 %0, m0\n\ts_mov_b32 m0, %2\n\ts_nop 0\n\tglobal_load_lds_dword %1, off\n\ts_mov_b32 m0, %0"
               : "=&s"(keep) : "v"(p), "s"(lds) : "memory");
}
typedef __amdgpu_buffer_rsrc_t rsrc_t;
DI v4u ldf(rsrc_t rs, unsigned voff, unsigned soff) { return __builtin_amdgcn_raw_buffer_load_b128(rs, voff, soff, 16); }
#define LDW(n, base) ldw_rt(c.rs, l * 16, base, n)
DI v4u ldw_rt(rsrc_t rs, unsigned voff, unsigned base, int n) { return __builtin_amdgcn_raw_buffer_load_b128(rs, voff + (n & 3) * 1024, base + (n >> 2) * 4096, 16); }
DI v4u ld_rflags(rsrc_t rs, int l) { return ldf(rs, (l >> 1) * 128 + (l & 1) * 16, OFF_RFLAG); }
DI void stf(rsrc_t rs, v4u v, unsigned voff, unsigned soff) { __builtin_amdgcn_raw_buffer_store_b128(v, rs, voff + soff, 0, 16); }

DI void meet4(LAS char* L, int at, int me, unsigned tag, int l) {
  volatile LAS unsigned* f = (volatile LAS unsigned*)(L + at);
  f[me] = tag;
  _Pragma("nounroll") for (unsigned s = 0; s < 16 * SPIN_CAP; ++s)
    if (__all(f[l & 3] == tag)) break;
  asm volatile("" ::: "memory");
}
DI void post(LAS char* L, int at, unsigned tag) { *(volatile LAS unsigned*)(L + at) = tag; }
DI void wait4(LAS char* L, int at, unsigned tag, int l) {
  volatile LAS unsigned* f = (volatile LAS unsigned*)(L + at);
  _Pragma("nounroll") for (unsigned s = 0; s < 16 * SPIN_CAP; ++s) {
    if (__all(f[l & 3] == tag)) break;
    __builtin_amdgcn_s_sleep(1);
  }
  asm volatile("" ::: "memory");
}
DI void lds_wait(LAS char* L, int at, unsigned tag) {
  volatile LAS unsigned* f = (volatile LAS unsigned*)(L + at);
  _Pragma("nounroll") for (unsigned s = 0; s < 16 * SPIN_CAP; ++s) {
    if (*f == tag) break;
    __builtin_amdgcn_s_sleep(1);
  }
  asm volatile("" ::: "memory");
}
DI void raise(rsrc_t rs, unsigned line, unsigned tag, int l) { if (l < 8) stf(rs, v4u{tag, tag, tag, tag}, l * 16, line); }
template <int DELAY> DI void poll8(gu32* fl, unsigned tag, int l) {
  __builtin_amdgcn_s_sleep(DELAY);
  _Pragma("nounroll") for (unsigned s = 0; s < SPIN_CAP; ++s) {
    unsigned f = __hip_atomic_load(fl + (l & 7) * FSTR, RLX_AGENT);
    if (__all(f == tag)) break;
    __builtin_amdgcn_s_sleep(PINT);
  }
  __builtin_amdgcn_fence(__ATOMIC_ACQUIRE, "wavefront");
}

DI unsigned gelu2(f2 a, f2 k1, f2 k2, f2 ir) {
  const f2 t = a * fma2(a * a, k2, k1);
  const f2 d = fma2(f2{__builtin_amdgcn_exp2f(t.x), __builtin_amdgcn_exp2f(t.y)}, ir, ir);
  return pk2(a * f2{__builtin_amdgcn_rcpf(d.x), __builtin_amdgcn_rcpf(d.y)});
}

DI void mod_get(rsrc_t rs, unsigned voff, v4u (&v)[4]) {
#pragma unroll
  for (int k = 0; k < 4; ++k) v[k] = ldf(rs, voff, OFF_MODP + k * 12288);
}
DI v2u mod_sum(rsrc_t rs, unsigned voff, v4u (&v)[4], unsigned tag, float plus) {
  const unsigned tb = tag & 0xffu;
  _Pragma("nounroll") for (unsigned s = 0; s < SPIN_CAP; ++s) {
#pragma unroll
    for (int k = 0; k < 4; ++k) v[k] ^= tb;
    const v4u d = (v[0] | v[1]) | (v[2] | v[3]);
    if (__all(((d.x | d.y) | (d.z | d.w)) << 24 == 0u)) break;
    __builtin_amdgcn_s_sleep(1);
    asm volatile("" ::: "memory");
    mod_get(rs, voff, v);
    WAITV(0);
  }
  const f4 s = (__builtin_bit_cast(f4, v[0]) + __builtin_bit_cast(f4, v[1])) + (__builtin_bit_cast(f4, v[2]) + __builtin_bit_cast(f4, v[3]));
  const f2 p2 = {plus, plus};
  return v2u{pk2(unpk(pk(s.x, s.y)) + p2), pk2(unpk(pk(s.z, s.w)) + p2)};
}
DI f4 widen(v2u p) { return f4{bflo(p.x), bfhi(p.x), bflo(p.y), bfhi(p.y)}; }

#define STP
#define STA

DI v4u mkrs(const void* p, unsigned bytes) { return v4u{(unsigned)(uintptr_t)p, (unsigned)((uintptr_t)p >> 32) & 0xffffu, bytes, 0x00020000u}; }
DI void mod_loads(const u16* c_, const u16* wmod_, bool on, int mcb, int mks, int v, int l, v2u& cw, v4u (&wm)[4]) {
  const int r0 = 128 * mks + 32 * v + 4 * (l >> 3);
  const v4u rc = mkrs(c_, on ? 1024u : 0u), rw = mkrs(wmod_, on ? 512u * 6144u : 0u);
  asm volatile("buffer_load_dwordx2 %0, %1, %2, 0 offen" : "=v"(cw) : "v"(r0 * 2), "s"(rc));
#pragma unroll
  for (int e = 0; e < 4; ++e) asm volatile("buffer_load_dwordx4 %0, %1, %2, %3 offen nt" : "=v"(wm[e]) : "v"(r0 * 6144 + 128 * mcb + 16 * (l & 7)), "s"(rw), "s"(e * 6144));
}
#define MOD_WAIT(n) asm volatile("s_waitcnt vmcnt(%5)" : "+v"(cw), "+v"(wm[0]), "+v"(wm[1]), "+v"(wm[2]), "+v"(wm[3]) : "i"(n) : "memory")
DI v4u mod_job(LAS char* L, int red, int v, int l, unsigned tag, v2u cw, const v4u (&wm)[4]) {
  v4u res = {0u, 0u, 0u, 0u};
  f2 a2[4];
#pragma unroll
  for (int e = 0; e < 4; ++e) {
    const float cv = (e & 1) ? bfhi(cw[e >> 1]) : bflo(cw[e >> 1]);
    float sv = cv * __builtin_amdgcn_rcpf(1.0f + __builtin_amdgcn_exp2f(-1.4426950409f * cv));
    sv = bflo(pk(sv, sv));
    const f2 s2 = {sv, sv};
#pragma unroll
    for (int k = 0; k < 4; ++k) a2[k] = e ? fma2(s2, unpk(wm[e][k]), a2[k]) : s2 * unpk(wm[e][k]);
  }
  float a[4];
#pragma unroll
  for (int k = 0; k < 4; ++k) {
    auto r = __builtin_amdgcn_permlane32_swap(__float_as_uint(a2[k >> 1][k & 1]), __float_as_uint(a2[2 + (k >> 1)][k & 1]), false, false);
    a[k] = __uint_as_float(r[0]) + __uint_as_float(r[1]);
  }
#pragma unroll
  for (int k = 0; k < 2; ++k) {
    auto r = __builtin_amdgcn_permlane16_swap(__float_as_uint(a[k]), __float_as_uint(a[k + 2]), false, false);
    a[k] = __uint_as_float(r[0]) + __uint_as_float(r[1]);
    a[k] += dpp<0x128>(a[k]);
  }
  if (!(l & 8)) *(LAS f2*)(L + red + v * 256 + (8 * (l & 7) + 2 * ((l >> 4) & 1) + 4 * (l >> 5)) * 4) = f2{a[0], a[1]};
  asm volatile("s_waitcnt lgkmcnt(0)" ::: "memory");
  volatile LAS unsigned* mf = (volatile LAS unsigned*)(L + red + 1024);
  if (v < 3) mf[v] = tag;
  else {
    _Pragma("nounroll") for (unsigned s = 0; s < SPIN_CAP; ++s)
      if (__all(mf[l < 3 ? l : 0] == tag)) break;
    asm volatile("" ::: "memory");
    f4 s = *(LAS f4*)(L + red + (l & 15) * 16);
#pragma unroll
    for (int ww = 1; ww < 4; ++ww) s += *(LAS f4*)(L + red + ww * 256 + (l & 15) * 16);
    res = (__builtin_bit_cast(v4u, s) & 0xffffff00u) | (tag & 0xffu);
  }
  return res;
}
DI void mod_publish(rsrc_t rs, int mcb, int mks, int v, int l, v4u res) {
  if (v == 3 && l < 16) stf(rs, res, l * 16, OFF_MODP + mks * 12288 + mcb * 256);
}

struct Ctx {
  rsrc_t rs; v4u rsv; LAS char* L; unsigned Lb, tag; int g, j, w, t03, tile0; gu32* gfl;
  const u16 *x_, *b1_, *b2_; u16* out_; gu32* epoch;
};

template <int T> DI void col_terms(const Ctx& c, bool two, int l, const v4u (&w3a)[16], const v4u (&w3b)[16], float b1a, float b1b, float (&uvt)[T]) {
  l = fresh(l);
  const int i = l & 15, q = l >> 4, ty = i >> 2;
  const int base = L_UV + (ty < 2 ? ty * 2048 + c.j * 128 : 2048 + ty * 1024) + q * 16;
  f4 a[2][T];
  v4u am[16];
#pragma unroll
  for (int s = 0; s < 16; ++s) am[s] = *(LAS v4u*)(c.L + base + s * 64);
  SB();
#pragma unroll
  for (int s = 0; s < 16; ++s) MAC(s < 2, a[s & 1][0], am[s], w3a[s]);
  if (T == 2 && two) {
#pragma unroll
    for (int s = 0; s < 16; ++s) MAC(s < 2, a[s & 1][T - 1], am[s], w3b[s]);
  }
  MFMA_DONE();
  uvt[0] = a[0][0].x + a[1][0].x + (q == 3 ? b1a : 0.f);
  if (T == 2) uvt[T - 1] = a[0][T - 1].x + a[1][T - 1].x + (q == 3 ? b1b : 0.f);
}

template <int T, int KIND>
DI void mlp1_loop(const Ctx& c, int l, const v4u (&w3a)[16], const v4u (&w3b)[16], f4 (&acc)[4][T], v4u (&w4)[44], unsigned w4off, bool two,
                  const float (&S)[5] STP) {
  LAS char* L = c.L;
  const int tid = c.w * 64 + l;
  auto xa_slot = [&](int slot) { dma<4>(c.rsv, l * 16, OFF_XA + (c.g * 16 + ((slot + 2 * c.j) & 15)) * 4096, c.Lb + slot * 4096); };
  v4u a[2][4];
#pragma unroll
  for (int r = 0; r < 4; ++r) a[0][r] = *(LAS v4u*)(L + r * 1024 + l * 16);
#pragma unroll
  for (int s = 0; s < 16; ++s) {
    if (s == 1 || s == 5) {
      if (KIND == 2 && c.w == 3) {
        if (s == 1) WAITV(16);
        if (s == 5) WAITV(16);
      } else if (KIND == 2 && s == 5) WAITV(0);
      SBAR();
      SL(22 + s / 4);
      if (KIND == 2 && c.w == 3) {
        if (s == 1) {
#pragma unroll
          for (int p = 0; p < 4; ++p) dma<4>(c.rsv, l * 16, OFF_STAT + c.g * 16384 + p * 4096, c.Lb + L_STAT + p * 4096);
        }
        if (s == 5) {
          float sx, sxx, sp, spp, spa, sps;
          _Pragma("nounroll") for (unsigned it = 0; it < SPIN_CAP; ++it) {
            WAITV(0);
            v4u a4[8]; v2u b2[8];
            unsigned d = 0;
#pragma unroll
            for (int p = 0; p < 8; ++p) {
              a4[p] = *(LAS v4u*)(L + L_STAT + p * 2048 + l * 16) ^ (c.tag & 0xffu);
              b2[p] = *(LAS v2u*)(L + L_STAT + p * 2048 + 1024 + l * 16) ^ (c.tag & 0xffu);
              d |= (a4[p].x | a4[p].y) | (a4[p].z | a4[p].w) | (b2[p].x | b2[p].y);
            }
            f4 ta = __builtin_bit_cast(f4, a4[0]); f2 tb = __builtin_bit_cast(f2, b2[0]);
#pragma unroll
            for (int p = 1; p < 8; ++p) { ta += __builtin_bit_cast(f4, a4[p]); tb += __builtin_bit_cast(f2, b2[p]); }
            sx = ta.x; sxx = ta.y; sp = ta.z; spp = ta.w; spa = tb.x; sps = tb.y;
            if (__all(d << 24 == 0u)) break;
            asm volatile("s_waitcnt lgkmcnt(0)" ::: "memory");
#pragma unroll
            for (int p = 0; p < 4; ++p) dma<4>(c.rsv, l * 16, OFF_STAT + c.g * 16384 + p * 4096, c.Lb + L_STAT + p * 4096);
          }
          const float inv = 1.0f / 512.0f;
          const float m = sx * inv, var = fmaxf(sxx * inv - m * m, 0.f);
          const float sd = __builtin_amdgcn_sqrtf(var + 1e-5f), r = __builtin_amdgcn_rcpf(sd);
          const float m2 = (r * (sp - m * S[0]) + S[1]) * inv;
          const float e2 = (r * r * (spp - 2.f * m * spa + m * m * S[2]) + 2.f * r * (sps - m * S[3]) + S[4]) * inv;
          const float sd2 = __builtin_amdgcn_sqrtf(fmaxf(e2 - m2 * m2, 0.f) + 1e-5f);
          const float R = r * __builtin_amdgcn_rcpf(sd2), k1 = -2.302208198f * R;
          *(LAS f4*)(L + L_COEF + l * 16) = f4{-m, sd, -m2 * sd, sd * sd2};
          *(LAS f4*)(L + L_RR + l * 16) = f4{k1, 0.044715f * k1 * R * R, sd * sd2, 0.f};
        }
      }
    }
    SB();
#pragma unroll
    for (int r = 0; r < 4; ++r) {
      MAC(s == 0, acc[r][0], w3a[s], a[s & 1][r]);
      SB();
      if (s < 15) a[(s + 1) & 1][r] = *(LAS v4u*)(L + ((s + 1) * 4 + r) * 1024 + l * 16);
      SB();
    }
    if (T == 2 && two) {
#pragma unroll
      for (int r = 0; r < 4; ++r) MAC(s == 0, acc[r][T - 1], w3b[s], a[s & 1][r]);
      SB();
    }
    if (KIND == 1) {
      const int n0 = s < 4 ? 2 * s : 3 * s - 4;
      w4[n0] = LDW(n0, w4off);
      w4[n0 + 1] = LDW(n0 + 1, w4off);
      if (s >= 4) w4[n0 + 2] = LDW(n0 + 2, w4off);
      SB();
    }
  }
  MFMA_DONE();
}

template <int T> DI void mlp1_epi(const Ctx& c, bool two, int l, const float (&uvt)[T], f4 (&acc)[4][T]) {
  l = fresh(l);
  const int i = l & 15, q = l >> 4;
  float cf[4]; f4 rr[4];
#pragma unroll
  for (int r = 0; r < 4; ++r) {
    cf[r] = *(LAS float*)(c.L + L_COEF + (r * 16 + i) * 16 + q * 4);
    rr[r] = *(LAS f4*)(c.L + L_RR + (r * 16 + i) * 16);
  }
#pragma unroll
  for (int t = 0; t < T; ++t) {
    if (t && !two) break;
#pragma unroll
    for (int r = 0; r < 4; ++r) acc[r][t] = __builtin_amdgcn_mfma_f32_16x16x4f32(uvt[t], cf[r], acc[r][t], 0, 0, 0);
  }
  SB();
#pragma unroll
  for (int t = 0; t < T; ++t) {
    if (t && !two) break;
    const int tl = c.tile0 + t;
#pragma unroll
    for (int pp = 0; pp < 2; ++pp) {
      unsigned h[2][2];
#pragma unroll
      for (int r2 = 0; r2 < 2; ++r2) {
        const int r = 2 * pp + r2;
        const f2 k1 = {rr[r].x, rr[r].x}, k2 = {rr[r].y, rr[r].y}, ir = {rr[r].z, rr[r].z};
        h[r2][0] = gelu2(f2{acc[r][t].x, acc[r][t].y}, k1, k2, ir);
        h[r2][1] = gelu2(f2{acc[r][t].z, acc[r][t].w}, k1, k2, ir);
      }
      auto s0 = __builtin_amdgcn_permlane16_swap(h[0][0], h[1][0], false, false);
      auto s1 = __builtin_amdgcn_permlane16_swap(h[0][1], h[1][1], false, false);
      stf(c.rs, v4u{s0[0], s1[0], s0[1], s1[1]}, (q & 1) * 1024 + ((q >> 1) * 16 + i) * 16,
          OFF_H1 + ((c.g * 43 + (tl >> 1)) * 4 + 2 * pp) * 1024 + (tl & 1) * 512);
      SB();
    }
  }
}

DI void dma_in(v4u rs, unsigned voff, unsigned soff, unsigned lds) {
  unsigned keep;
  asm volatile("s_mov_b32 %0, m0\n\ts_mov_b32 m0, %2\n\ts_nop 0\n\tbuffer_load_dwordx4 %1, %3, %4 offen lds\n\ts_mov_b32 m0, %0"
               : "=&s"(keep) : "v"(voff), "s"(lds), "s"(rs), "s"(soff) : "memory");
}
DI void sum8x6(float& a, float& b, float& c, float& d, float& e, float& f) {
#define S6(ctl) "v_add_f32_dpp %0, %0, %0 " ctl "\n\tv_add_f32_dpp %1, %1, %1 " ctl "\n\tv_add_f32_dpp %2, %2, %2 " ctl "\n\t" \
                "v_add_f32_dpp %3, %3, %3 " ctl "\n\tv_add_f32_dpp %4, %4, %4 " ctl "\n\tv_add_f32_dpp %5, %5, %5 " ctl "\n\t"
  asm volatile("s_nop 1\n\t" S6("quad_perm:[1,0,3,2] row_mask:0xf bank_mask:0xf") S6("quad_perm:[2,3,0,1] row_mask:0xf bank_mask:0xf")
               S6("row_half_mirror row_mask:0xf bank_mask:0xf") "s_nop 0" : "+v"(a), "+v"(b), "+v"(c), "+v"(d), "+v"(e), "+v"(f));
#undef S6
}
DI void red_write(const Ctx& c, int l, int nh, int kh, const f4 (&acc)[4][2]) {
  l = fresh(l);
  const int rb = L_RED + kh * 17408 + (l & 15) * 272 + nh * 128 + (l >> 4) * 16;
#pragma unroll
  for (int r = 0; r < 4; ++r)
#pragma unroll
    for (int t = 0; t < 2; ++t) *(LAS f4*)(c.L + rb + r * 16 * 272 + t * 64) = acc[r][t];
  asm volatile("s_waitcnt lgkmcnt(0)" ::: "memory");
}
DI void red_read(const Ctx& c, int tok, int f8, f2 (&y)[4]) {
  const int ra = L_RED + tok * 272 + f8 * 32;
  const f4 y0 = *(LAS f4*)(c.L + ra) + *(LAS f4*)(c.L + ra + 17408), y1 = *(LAS f4*)(c.L + ra + 16) + *(LAS f4*)(c.L + ra + 17408 + 16);
  y[0] = f2{y0.x, y0.y}; y[1] = f2{y0.z, y0.w}; y[2] = f2{y1.x, y1.y}; y[3] = f2{y1.z, y1.w};
}
DI void ec_read(const Ctx& c, int sec, int f8, f2 (&y)[4]) {
  const f4 y0 = *(LAS f4*)(c.L + L_EC + sec * 256 + f8 * 32), y1 = *(LAS f4*)(c.L + L_EC + sec * 256 + f8 * 32 + 16);
  y[0] = f2{y0.x, y0.y}; y[1] = f2{y0.z, y0.w}; y[2] = f2{y1.x, y1.y}; y[3] = f2{y1.z, y1.w};
}
DI void epi2(const Ctx& c, int t, f2 (&x1)[2][4]) {
  t = fresh(t);
  const int tk = t >> 3, f8 = t & 7;
  f2 ga[4], a2[4], sb[4], p[2][4];
  ec_read(c, 0, f8, ga); ec_read(c, 1, f8, a2); ec_read(c, 2, f8, sb);
#pragma unroll
  for (int k = 0; k < 2; ++k) {
    f2 y[4];
    red_read(c, tk + 32 * k, f8, y);
#pragma unroll
    for (int e = 0; e < 4; ++e) { x1[k][e] = fma2(ga[e], y[e], x1[k][e]); p[k][e] = x1[k][e] * a2[e]; }
    stf(c.rs, v4u{pk2(p[k][0]), pk2(p[k][1]), pk2(p[k][2]), pk2(p[k][3])},
        ((f8 >> 2) * 4 + (tk >> 4) + 2 * k) * 1024 + ((f8 & 3) * 16 + (tk & 15)) * 16, OFF_XA + (c.g * 16 + 2 * c.j) * 4096);
  }
  SB();
#pragma unroll
  for (int k = 0; k < 2; ++k) {
    f2 vx = x1[k][0], vxx = x1[k][0] * x1[k][0], vp = p[k][0], vpp = p[k][0] * p[k][0], vpa = p[k][0] * a2[0], vps = p[k][0] * sb[0];
#pragma unroll
    for (int e = 1; e < 4; ++e) {
      vx += x1[k][e]; vxx = fma2(x1[k][e], x1[k][e], vxx); vp += p[k][e]; vpp = fma2(p[k][e], p[k][e], vpp);
      vpa = fma2(p[k][e], a2[e], vpa); vps = fma2(p[k][e], sb[e], vps);
    }
    float sx = vx.x + vx.y, sxx = vxx.x + vxx.y, sp = vp.x + vp.y, spp = vpp.x + vpp.y, spa = vpa.x + vpa.y, sps = vps.x + vps.y;
    sum8x6(sx, sxx, sp, spp, spa, sps);
    const v4u sw = (__builtin_bit_cast(v4u, f8 == 0 ? f4{sx, sxx, sp, spp} : f4{spa, sps, spa, sps}) & 0xffffff00u) | (c.tag & 0xffu);
    if (f8 < 2) stf(c.rs, sw, (tk + 32 * k) * 16 + f8 * 1024, OFF_STAT + (c.g * 8 + c.j) * 2048);
  }
}
DI void epi4(const Ctx& c, int t, v4u b2v, const f2 (&x1)[2][4]) {
  t = fresh(t);
  const int tk = t >> 3, f8 = t & 7;
  const rsrc_t ro = __builtin_amdgcn_make_buffer_rsrc((void*)c.out_, 0, 2048 * 512 * 2, 0x00020000);
  f2 gb[4];
  ec_read(c, 3, f8, gb);
#pragma unroll
  for (int k = 0; k < 2; ++k) {
    f2 y[4];
    red_read(c, tk + 32 * k, f8, y);
    v4u o;
#pragma unroll
    for (int e = 0; e < 4; ++e) o[e] = pk2(fma2(gb[e], y[e] + unpk(b2v[e]), x1[k][e]));
    __builtin_amdgcn_raw_buffer_store_b128(o, ro, (tk + 32 * k) * 1024 + f8 * 16 + c.g * 65536 + c.j * 128, 0, 0);
  }
}

DI void tail_c(const Ctx& c, bool two, int tid STP) {
  tid = fresh(tid);
  const int l = tid & 63, i = l & 15, w = c.w, g = c.g, j = c.j;
  LAS char* L = c.L;
  const unsigned w3off = OFF_W3T + c.tile0 * 16384;
  v4u w3a[16], w3b[16], w4[44];
  WAITV(0);
  meet4(L, L_W + 32, w, c.tag, l);
  if (w == 2) raise(c.rs, OFF_GFLAG + (g * 8 + j) * 128, c.tag, l);
  ST(11);
  auto o_slot = [&](int slot) { dma<4>(c.rsv, l * 16, OFF_O + (g * 16 + ((slot + 2 * j) & 15)) * 4096, c.Lb + slot * 4096); };
  if (w < 2) o_slot(w);
  const float b1a = bflo(((const GAS u16*)c.b1_)[c.tile0 * 16 + i]), b1b = bflo(((const GAS u16*)c.b1_)[(c.tile0 + (two ? 1 : 0)) * 16 + i]);
  if (w == 3) poll8<PD1>(c.gfl, c.tag, l);
  if (w == 2) {
    __builtin_amdgcn_s_sleep(14);
    _Pragma("nounroll") for (unsigned s = 0; s < SPIN_CAP; ++s) {
      const v4u rf = ld_rflags(c.rs, l);
      if (__all(rf.x == c.tag && rf.y == c.tag && rf.z == c.tag && rf.w == c.tag)) break;
      __builtin_amdgcn_s_sleep(8);
      asm volatile("" ::: "memory");
    }
    if (g == 0 && j == 0 && l == 0) *c.epoch = c.tag;
  }
  WAITV(0);
  ST(17);
  SBAR();
  ST(8);
  o_slot(2 + w); o_slot(6 + w);
  v4u m1[4], m2[4];
  const unsigned o1 = (1536 + 256 * w + 4 * l) * 4, o2 = (((l & 16) ? 2560 : 1024) + 64 * j + 4 * i) * 4;
  WAITV_BAR(4); SL(20);
  o_slot(10 + w); o_slot(14 + (w & 1));
  WAITV_BAR(8);
  mod_get(c.rs, o1, m1);
  mod_get(c.rs, o2, m2);
  WAITV_BAR(12);
#pragma unroll
  for (int n = 0; n < 4; ++n) w3a[n] = LDW(n, w3off);
  WAITV_BAR(12); SL(21);
  {
    const v2u p = mod_sum(c.rs, o1, m1, c.tag, w < 2 ? 0.f : 1.f);
    const int u = L_UV + (w < 2 ? 2048 : 0) + (w & 1) * 512 + l * 8;
    *(LAS v2u*)(L + u) = p; *(LAS v2u*)(L + u + 1024) = p;
    if ((w & 1) * 4 + (l >> 4) == j) *(LAS f4*)(L + L_EC + (w < 2 ? 512 : 256) + i * 16) = widen(p);
    const v2u p2 = mod_sum(c.rs, o2, m2, c.tag, 0.f);
    if (w == 3 && l < 32) *(LAS f4*)(L + L_EC + ((l & 16) ? 768 : 0) + i * 16) = widen(p2);
    if (w < 2) { const unsigned f = w ? 0u : 0x3f803f80u; *(LAS v4u*)(L + L_UV + 4096 + w * 1024 + l * 16) = v4u{f, f, f, f}; }
    asm volatile("s_waitcnt lgkmcnt(0)" ::: "memory");
    post(L, L_W + 48 + 4 * w, c.tag);
  }
#pragma unroll
  for (int n = 4; n < 16; ++n) w3a[n] = LDW(n, w3off);
  if (two) {
#pragma unroll
    for (int n = 0; n < 16; ++n) w3b[n] = LDW(16 + n, w3off);
  }
  wait4(L, L_W + 48, c.tag, l);
  float S[5] = {0.f, 0.f, 0.f, 0.f, 0.f};
  if (w == 3) {
    const v4u av = *(LAS v4u*)(L + L_UV + l * 16), sv = *(LAS v4u*)(L + L_UV + 2048 + l * 16);
#pragma unroll
    for (int e = 0; e < 4; ++e) {
      const float a0 = bflo(av[e]), a1 = bfhi(av[e]), s0 = bflo(sv[e]), s1 = bfhi(sv[e]);
      S[0] += a0 + a1; S[1] += s0 + s1; S[2] += a0 * a0 + a1 * a1; S[3] += a0 * s0 + a1 * s1; S[4] += s0 * s0 + s1 * s1;
    }
#pragma unroll
    for (int e = 0; e < 5; ++e) S[e] = swap32_sum(swap16_sum(sum16(S[e])));
  }
  WAITV(0);
  ST(9);
  float uvt[2];
  col_terms<2>(c, two, l, w3a, w3b, b1a, b1b, uvt);
  USE(uvt[0]); USE(uvt[1]);
  if (w == 3) {
    wait4(L, L_W + 96, c.tag, l);
    poll8<PD2>(c.gfl + GSEAM, c.tag, l);
    ST(15);
#pragma unroll
    for (int k = 2; k < 10; ++k) dma<4>(c.rsv, l * 16, OFF_XA + (g * 16 + ((k + 2 * j) & 15)) * 4096, c.Lb + k * 4096);
  }
  SBAR();
  if (w < 3) {
#pragma unroll
    for (int k = 0; k < 2; ++k) dma<4>(c.rsv, l * 16, OFF_XA + (g * 16 + ((10 + 2 * w + k + 2 * j) & 15)) * 4096, c.Lb + (10 + 2 * w + k) * 4096);
  }
  ST(10);
  f4 acc[4][2];
  mlp1_loop<2, 2>(c, l, w3a, w3b, acc, w4, 0, two, S STA);
  BAR();
  ST(12);
  mlp1_epi<2>(c, two, l, uvt, acc);
  ST(18);
  WAITV_BAR(0);
  ST(13);
  const int rot4 = (c.t03 + 1) >> 1;
  auto h_slot = [&](int sg) {
    int k = rot4 + sg; if (k >= 43) k -= 43;
    dma<4>(c.rsv, l * 16, OFF_H1 + (g * 43 + k) * 4096, c.Lb + (sg >= 38 ? sg - 38 : sg) * 4096);
  };
  if (w == 3) {
    poll8<PD3>(c.gfl + 2 * GSEAM, c.tag, l);
    h_slot(5); h_slot(6); h_slot(7);
  } else {
    h_slot(w); if (w < 2) h_slot(3 + w);
    WAITV(0);
  }
  SBAR();
  ST(14);
  h_slot(8 + w); h_slot(12 + w);
#pragma unroll
  for (int m = 1; m < 10; ++m) {
    if (m == 1 || m == 7) WAITV_BAR(8);
    else if (m < 7) WAITV_BAR(12);
    else if (m == 8) WAITV_BAR(4);
    else WAITV_BAR(0);
    if (m == 1) SL(26);
    if (m == 5) SL(27);
    if (m == 9) SL(28);
    if (m == 1) h_slot(16 + w);
    if (m + 4 < 10 || (m + 4 == 10 && w < 3)) h_slot(4 * (m + 4) + w);
  }
}

extern "C" __global__ void __launch_bounds__(512, 2)
fused_block(const u16* __restrict__ x_, const u16* __restrict__ wqkv_, const u16* __restrict__ wo_, const u16* __restrict__ w1_,
            const u16* __restrict__ b1_, const u16* __restrict__ w2_, const u16* __restrict__ b2_, const u16* __restrict__ c_,
            const u16* __restrict__ wmod_, u16* __restrict__ out_, unsigned char* __restrict__ ws_) {
  extern __shared__ __attribute__((aligned(16))) char smem[];
  asm volatile("" ::"s"(x_), "s"(wqkv_), "s"(wo_), "s"(w1_), "s"(b1_), "s"(w2_), "s"(b2_), "s"(c_), "s"(wmod_), "s"(out_), "s"(ws_));
  unsigned long long pc;
  asm volatile("s_getpc_b64 %0" : "=s"(pc));
  LAS char* L = (LAS char*)smem;
  const unsigned Lb = (unsigned)(uintptr_t)L;
  const int tid0 = threadIdx.x, w = __builtin_amdgcn_readfirstlane(tid0 >> 6);
  const int b = blockIdx.x, xcd = b & 7, idx = b >> 3, g = xcd * 4 + (idx >> 3), j = idx & 7;
  GAS unsigned char* ws = (GAS unsigned char*)ws_;
  const rsrc_t rs = __builtin_amdgcn_make_buffer_rsrc((void*)ws_, 0, WS_USED, 0x00020000);
  const v4u rsv = {(unsigned)(uintptr_t)ws_, (unsigned)((uintptr_t)ws_ >> 32) & 0xffffu, WS_USED, 0x00020000u};
  SRT(32);
  ST(0);
  unsigned epoch = *(const __attribute__((address_space(4))) unsigned*)(uintptr_t)(ws_ + OFF_E);
#define GET_TAG asm volatile("" : "+s"(epoch) : : "memory"); const unsigned tag = epoch + 1u;
  const int rq = xcd >> 1;
  const int nt3 = 11 - (j >= 6), t03 = 11 * j - (j == 7);
  gu32* gfl = (gu32*)(ws + OFF_GFLAG) + g * 8 * FSTR;
  const v4u rsq = {(unsigned)(uintptr_t)wqkv_, (unsigned)((uintptr_t)wqkv_ >> 32) & 0xffffu, 512u * 1536u * 2u, 0x00020000u};
#define W2POS(s) (L_W2 + (s) * 4096)
#define WSV(stride) ((8 * (w & 3) + (l >> 3)) * (stride) + 128 * j + 16 * ((l & 7) ^ ((((l >> 4) & 1) | ((w & 1) << 1)) << 1)))
#define LANE_IDS const int l = lane_id(), tid = w * 64 + l, i = l & 15, q = l >> 4; const unsigned w1v = WSV(3072);
#define RETILE_IDS(v) \
    const int u = 4 * (b < 64 ? b : b >= 192 ? b - 128 : b + 64) + v; \
    const bool retile = u < 696, isw1 = u < 352; \
    const int rcb = isw1 ? u >> 4 : (int)((unsigned)(u - 352) / 43u), rks = isw1 ? (u & 15) : u - 352 - 43 * rcb; \
    const int rnt = (isw1 && rcb == 21) ? 2 : 4, rst = v < 3 ? L_RT + v * 4096 : L_EC; \

  v4u xr[5][4];
  if (w < 4) {
    const int l = lane_id();
    unsigned dummy;
    v2u cw; v4u wm[4];
    const int mcb = b & 15, mks = b >> 4;
    if (b < 64) { __builtin_amdgcn_s_setprio(3); mod_loads(c_, wmod_, true, mcb, mks, w, l, cw, wm); }
    asm volatile("s_load_dword %0, %1, 0x0\n\ts_load_dword %0, %1, 0x40\n\ts_load_dword %0, %1, 0x80\n\t"
                 "s_load_dword %0, %1, 0xc0\n\ts_load_dword %0, %1, 0x100\n\ts_load_dword %0, %1, 0x140"
                 : "=&s"(dummy) : "s"((pc & ~255ull) + idx * CODE_STEP + w * 384));
    if (b < 64) {
      asm volatile("" : "+s"(epoch) : : "memory");
      MOD_WAIT(0);
      mod_publish(rs, mcb, mks, w, l, mod_job(L, L_MRED, w, l, epoch + 1u, cw, wm));
      __builtin_amdgcn_s_setprio(0);
      SBAR();
    }
    ST(29);
    const unsigned w1v = WSV(3072);
#pragma unroll
    for (int s0 = 0; s0 < 11; ++s0) {
      const unsigned t = s0 + 2 * (xcd & 1), s = s0 >= 6 ? s0 : t < t - 6u ? t : t - 6u;
      dma3(rsq, w1v, ((s + 4 * rq) & 15) * (32 * 3072), Lb + (s0 >= 6 ? L_R1 + (s0 - 6) * 12288 : s * 12288) + w * 3072);
    }
    asm volatile("s_waitcnt lgkmcnt(0)" : "+s"(dummy) : : "memory");
    ST(30);
    WAITV(9);
  } else {
    const int l = lane_id();
    __builtin_amdgcn_s_setprio(3);
#pragma unroll
    for (int p = 0; p < 5; ++p) {
      int tok = 64 * g - 16 + 20 * (w & 3) + (l >> 4) + 4 * p;
      tok = tok < 0 ? 0 : tok;
#pragma unroll
      for (int n = 0; n < 4; ++n)
        asm volatile("global_load_dwordx4 %0, %1, %2" : "=v"(xr[p][n]) : "v"(tok * 1024 + 256 * ((n + rq) & 3) + 16 * (l & 15)), "s"(x_));
      if (p == XFIRST - 1 && b < 64) SBAR();
    }
    if (w == 7 && l < 32) gdma4((const GAS char*)wo_ + (b * 32 + l) * 64, Lb + L_EC);
    ST(29);
    SB();
  }
  if (w < 4) {
    asm volatile("; matrix waves" ::: "memory");
    LANE_IDS
    GET_TAG
    ST(2);
    meet4(L, L_SYNC, w, tag, l);
    ST(3);
    const int tb_ = q * 3072 + (i >> 2) * 128 + ((w ^ ((i >> 3) | ((q & 1) << 1))) * 32) + (i & 3) * 8;
    const int tb1 = fresh(tb_ + L_R1);
    v4u wf[16][3];
#pragma unroll
    for (int s = 0; s < 8; ++s)
#pragma unroll
      for (int sg = 0; sg < 3; ++sg) wf[s][sg] = trfrag(L + (s < 6 ? tb_ + s * 12288 : tb1 + (s - 6) * 12288) + sg * 1024);
    {
      const int v = w;
      RETILE_IDS(v)
    {
      const int nc = isw1 ? 2752 : 1024;
      const v4u rr = mkrs(isw1 ? w1_ : w2_, retile ? 1376u * 1024u : 0u);
      const unsigned vo = (32 * rks + (l >> 3)) * nc + 128 * rcb + 16 * (rnt == 2 ? (l & 3) : (l & 7));
#pragma unroll
      for (int e = 0; e < 4; ++e) dma_nt(rr, vo, 8 * e * nc, Lb + rst + e * 1024);
    }
    }
    ST(7);
    BAR();
    ST(4);
    f4 aq[4], ak[5], av[5];
    const int a0 = l * 16 ^ (q << 4), a1 = l * 16 ^ ((q | 4) << 4), a0h = fresh(a0 + 61440), a1h = fresh(a1 + 61440);
    v4u a[2][5];
    SBAR();
    ST(5);
#pragma unroll
    for (int r = 0; r < 5; ++r) a[0][r] = *(LAS v4u*)(L + r * 1024 + a0);
#pragma unroll
    for (int s = 0; s < 16; ++s) {
      if (s == 7) WAITV(4);
      if (s == 10) WAITV(0);
      if (s == 3 || s == 7 || s == 10 || s == 12 || s == 14) SBAR();
      if (s == 7) SL(31);
      if (s == 10) SL(34);
      SB();
#pragma unroll
      for (int r = 0; r < 5; ++r) {
        MAC(s == 0, ak[r], wf[s][1], a[s & 1][r]);
        SB();
        if (s < 11) a[(s + 1) & 1][r] = *(LAS v4u*)(L + ((s + 1) * 5 + r) * 1024 + (((s + 1) & 1) ? a1 : a0));
        else if (s < 15) a[(s + 1) & 1][r] = *(LAS v4u*)(L + ((s - 11) * 5 + r) * 1024 + (((s + 1) & 1) ? a1h : a0h));
        SB();
        MAC(s == 0, av[r], a[s & 1][r], wf[s][2]);
        SB();
        if (s >= 7 && s < 15 && r < 3) { wf[s + 1][r] = trfrag(L + (s < 10 ? tb1 + (s - 5) * 12288 : s < 12 ? tb1 + (s - 10) * 12288 : s < 14 ? tb_ + (s - 12) * 12288 : tb1 + 2 * 12288) + r * 1024); SB(); }
        if (r) { MAC(s == 0, aq[r - 1], wf[s][0], a[s & 1][r]); SB(); }
      }
    }
    MFMA_DONE();
    post(L, L_W + 16 + 4 * w, tag);
    SB();
    ST(6);
    {
      v2u qb[4], kb[5], vb[5];
#pragma unroll
      for (int r = 0; r < 5; ++r) {
        kb[r] = v2u{pk(ak[r].x, ak[r].y), pk(ak[r].z, ak[r].w)};
        vb[r] = v2u{pk(av[r].x, av[r].y), pk(av[r].z, av[r].w)};
        if (r) qb[r - 1] = v2u{pk(aq[r - 1].x, aq[r - 1].y), pk(aq[r - 1].z, aq[r - 1].w)};
      }
      const bool odd = i & 1;
      const bool cur0 = 4 * q + (i & 1) <= i, cur1 = 4 * q + 2 + (i & 1) <= i;
      const float sc = 0.25f * 1.4426950409f;
      const int hd = 4 * j + w;
      const f4 z = {0.f, 0.f, 0.f, 0.f};
      f4 sp[4], scu[4];
#pragma unroll
      for (int r = 0; r < 4; ++r) { sp[r] = mfma16(kb[r], qb[r], z); scu[r] = mfma16(kb[r + 1], qb[r], z); }
      const unsigned lo = odd ? 0x01000c0cu : 0x0c0c0100u, hi = odd ? 0x03020c0cu : 0x0c0c0302u, no = 0x0c0c0c0cu;
      const unsigned sel0 = cur0 ? no : lo, sel1 = cur1 ? no : hi, sel2 = cur0 ? lo : no, sel3 = cur1 ? hi : no;
      float v0[4], v1[4], m[4];
#pragma unroll
      for (int r = 0; r < 4; ++r) {
        float p0 = odd ? sp[r].y : sp[r].x, p1 = odd ? sp[r].w : sp[r].z;
        const float c0 = odd ? scu[r].y : scu[r].x, c1 = odd ? scu[r].w : scu[r].z;
        if (r == 0 && g == 0) { p0 = -4e9f; p1 = -4e9f; }
        v0[r] = cur0 ? c0 : p0; v1[r] = cur1 ? c1 : p1;
        m[r] = max2(v0[r], v1[r]);
      }
      spread4(gather4<true>(m) * sc, m);
#pragma unroll
      for (int r = 0; r < 4; ++r) {
        v0[r] = __builtin_amdgcn_exp2f(__builtin_fmaf(v0[r], sc, -m[r])); v1[r] = __builtin_amdgcn_exp2f(__builtin_fmaf(v1[r], sc, -m[r]));
        m[r] = v0[r] + v1[r];
      }
      spread4(__builtin_amdgcn_rcpf(gather4<false>(m)), m);
      f4 o[4];
#pragma unroll
      for (int r = 0; r < 4; ++r) {
        const unsigned pp = pk(v0[r] * m[r], v1[r] * m[r]);
        const v4u pf = {__builtin_amdgcn_perm(0u, pp, sel0), __builtin_amdgcn_perm(0u, pp, sel1), __builtin_amdgcn_perm(0u, pp, sel2), __builtin_amdgcn_perm(0u, pp, sel3)};
        o[r] = mfma32(v4u{vb[r].x, vb[r].y, vb[r + 1].x, vb[r + 1].y}, pf, z);
      }
      unsigned h[4][2];
#pragma unroll
      for (int r = 0; r < 4; ++r) { h[r][0] = pk(o[r].x, o[r].y); h[r][1] = pk(o[r].z, o[r].w); }
#pragma unroll
      for (int pp = 0; pp < 2; ++pp) {
        auto s0 = __builtin_amdgcn_permlane16_swap(h[2 * pp][0], h[2 * pp + 1][0], false, false);
        auto s1 = __builtin_amdgcn_permlane16_swap(h[2 * pp][1], h[2 * pp + 1][1], false, false);
        stf(rs, v4u{s0[0], s1[0], s0[1], s1[1]}, (q & 1) * 1024 + ((q >> 1) * 16 + i) * 16,
            OFF_O + ((g * 16 + (hd >> 1)) * 4 + 2 * pp) * 1024 + (hd & 1) * 512);
      }
    }
    ST(19);
    const bool two = w < nt3 - 8;
    const Ctx c = {rs, rsv, L, Lb, tag, g, j, w, t03, t03 + (two ? 2 * w : nt3 - 8 + w), gfl, x_, b1_, b2_, out_, (gu32*)(ws + OFF_E)};
    tail_c(c, two, tid STA);
    ST(16);
    SLF();
    __builtin_amdgcn_endpgm();
  } else {
    asm volatile("; helper waves" ::: "memory");
    LANE_IDS
    const int v = w - 4;
    const int row0 = 20 * v + q;
    GET_TAG
    ST(1);
    f2 xn[5][16];
    v4u mv[4];
#pragma unroll
    for (int p = 0; p < 5; ++p) {
      f2 s2 = {0.f, 0.f}, q2 = {0.f, 0.f};
      asm volatile("s_waitcnt vmcnt(%4)" : "+v"(xr[p][0]), "+v"(xr[p][1]), "+v"(xr[p][2]), "+v"(xr[p][3]) : "i"(16 - 4 * p) : "memory");
#pragma unroll
      for (int e = 0; e < 16; ++e) {
        xn[p][e] = unpk(xr[p][e >> 2][e & 3]);
        s2 += xn[p][e];
        q2 = fma2(xn[p][e], xn[p][e], q2);
      }
      const float m = sum16(s2.x + s2.y) * (1.0f / 512.0f);
      const float r = __builtin_amdgcn_rsqf(fmaxf(sum16(q2.x + q2.y) * (1.0f / 512.0f) - m * m, 0.f) + 1e-5f);
      const f2 r2 = {r, r}, mr2 = {-m * r, -m * r};
#pragma unroll
      for (int e = 0; e < 16; ++e) { xn[p][e] = fma2(xn[p][e], r2, mr2); asm volatile("" : "+v"(xn[p][e])); }
    }
    int ha[5];
#pragma unroll
    for (int p = 0; p < 5; ++p) {
      const int r = row0 + 4 * p, qq = i & 3;
      ha[p] = ((i >> 2) * 5 + (r >> 4)) * 1024 + (qq * 16 + ((r & 15) ^ (qq | (i & 4)))) * 16;
      asm volatile("" : "+v"(ha[p]));
    }
    ST(2);
    mod_get(rs, (256 * v + 4 * l) * 4, mv);
    SB();
    RETILE_IDS(v)
    int rdst[4];
#pragma unroll
    for (int t = 0; t < 4; ++t) {
      const int tl = 4 * rcb + t;
      const int j3 = tl < 66 ? (int)((unsigned)tl / 11u) : (tl < 76 ? 6 : 7);
      int s4 = rks - (rcb < 6 ? (11 * rcb + 1) >> 1 : 3 + 5 * rcb); if (s4 < 0) s4 += 43;
      rdst[t] = isw1 ? OFF_W3T + (tl * 16 + ((rks - 2 * j3) & 15)) * 1024
                     : OFF_W4T + ((((rcb * 2 + (t >> 1)) * 2 + (s4 & 1)) * 22 + (s4 >> 1)) * 2 + (t & 1)) * 1024;
      if (!retile || t >= rnt) rdst[t] = 0x10000000;
      asm volatile("" : "+s"(rdst[t]));
    }
    *(LAS f4*)(L + L_MODF + (256 * v + 4 * l) * 4) = widen(mod_sum(rs, (256 * v + 4 * l) * 4, mv, tag, v < 2 ? 0.f : 1.f));
    WAITV(0);
    asm volatile("s_waitcnt lgkmcnt(0)" ::: "memory");
    ST(7);
    post(L, L_SYNC + 16 + 4 * v, tag);
    wait4(L, L_SYNC + 16, tag, l);
    v4u ho[4][5];
    {
      f4 md[2][4];
      auto get = [&](int n) {
        const int ch = L_MODF + (16 * ((n + rq) & 3) + i) * 32;
        md[n & 1][0] = *(LAS f4*)(L + ch); md[n & 1][1] = *(LAS f4*)(L + ch + 16); md[n & 1][2] = *(LAS f4*)(L + ch + 2048); md[n & 1][3] = *(LAS f4*)(L + ch + 2064);
      };
      get(0);
#pragma unroll
      for (int n = 0; n < 4; ++n) {
        if (n < 3) get(n + 1);
        SB();
        const f4 (&m)[4] = md[n & 1];
        const f2 sa2[4] = {{m[0].x, m[0].y}, {m[0].z, m[0].w}, {m[1].x, m[1].y}, {m[1].z, m[1].w}}, ca2[4] = {{m[2].x, m[2].y}, {m[2].z, m[2].w}, {m[3].x, m[3].y}, {m[3].z, m[3].w}};
#pragma unroll
        for (int p = 0; p < 5; ++p)
#pragma unroll
          for (int e = 0; e < 4; ++e) xn[p][4 * n + e] = fma2(xn[p][4 * n + e], ca2[e], sa2[e]);
        SB();
#pragma unroll
        for (int p = 0; p < 5; ++p)
#pragma unroll
          for (int e = 0; e < 4; ++e) ho[n][p][e] = pk2(xn[p][4 * n + e]);
        SB();
      }
#pragma unroll
      for (int n = 0; n < 4; ++n)
#pragma unroll
        for (int p = 0; p < 5; ++p) asm volatile("" : "+v"(ho[n][p]));
    }
    __builtin_amdgcn_s_setprio(0);
    ST(3);
    SBAR();
    ST(4);
#pragma unroll
    for (int p = 0; p < 5; ++p) *(LAS v4u*)(L + ha[p]) = ho[0][p];
    BAR();
    ST(5);
    auto w1_step = [&](int s, int at) { dma3(rsq, w1v, ((s + 4 * rq) & 15) * (32 * 3072), Lb + at + v * 3072); };
#pragma unroll
    for (int n = 1; n < 4; ++n) {
#pragma unroll
      for (int p = 0; p < 5; ++p) *(LAS v4u*)(L + ha[p] + n * 20480) = ho[n][p];
      if (n == 1) { w1_step(11, L_R1); w1_step(12, L_R1 + 12288); BAR(); }
    }
    BAR();
    w1_step(13, 0); w1_step(14, 12288);
    ST(34);
    WAITV(6);
    SBAR();
    w1_step(15, L_R1 + 2 * 12288);
#pragma unroll
    for (int t = 0; t < 4; ++t) stf(rs, trfrag(L + rst + (8 * q + (i >> 2)) * 128 + t * 32 + (i & 3) * 8), l * 16, rdst[t]);
    WAITV(7);
    SBAR();
    const bool job2 = b >= 64 && b < 192;
    v2u cw; v4u wm[4];
    mod_loads(c_, wmod_, job2, 16 + ((b - 64) & 31), ((b - 64) >> 5) & 3, v, fresh(l), cw, wm);
    WAITV(9);
    SBAR();
    ST(6);
    const Ctx c = {rs, rsv, L, Lb, tag, g, j, w, t03, t03 + nt3 - 8 + w, gfl, x_, b1_, b2_, out_, (gu32*)(ws + OFF_E)};
    const int nh = v & 1, kh = v >> 1;
    v4u w2[16], w3a[16], w3b[16], w4[44];
    WAITV(5);
    meet4(L, L_W + 128, v, tag, l);
    if (tid == 320) __hip_atomic_store((gu32*)(ws + OFF_RFLAG) + 32 * g + j, tag, RLX_AGENT);
    ST(35);
    wait4(L, L_W + 16, tag, l);
    {
      const v4u rso = {(unsigned)(uintptr_t)wo_, (unsigned)((uintptr_t)wo_ >> 32) & 0xffffu, 512u * 512u * 2u, 0x00020000u};
      const unsigned w2v = WSV(1024);
#pragma unroll
      for (int s = 0; s < 16; ++s) dma_in(rso, w2v, ((s + 2 * j) & 15) * 32768, Lb + W2POS(s) + v * 1024);
    }
    v4u xq[2];
    {
      const rsrc_t rx = __builtin_amdgcn_make_buffer_rsrc((void*)x_, 0, 2048 * 512 * 2, 0x00020000);
      const int t = tid - 256;
#pragma unroll
      for (int k = 0; k < 2; ++k) xq[k] = __builtin_amdgcn_raw_buffer_load_b128(rx, ((t >> 3) + 32 * k) * 1024 + (t & 7) * 16, g * 65536 + j * 128, 0);
    }
    const float b1a = bflo(((const GAS u16*)b1_)[c.tile0 * 16 + i]);
    SB();
    MOD_WAIT(19);
    v4u res;
    if (job2) res = mod_job(L, L_MODF + 2048, v, fresh(l), tag, cw, wm);
    WAITV(0);
    if (job2) mod_publish(rs, 16 + ((b - 64) & 31), (b - 64) >> 5, v, l, res);
    ST(31);
    meet4(L, L_W + 64, v, tag, l);
    {
      const int tb2 = fresh(L_W2 + kh * 4096 + (8 * q + (i >> 2)) * 128 + (i & 3) * 8), f = (i >> 3) | ((q & 1) << 1);
      const int t0 = tb2 + ((2 * nh) ^ f) * 32, t1 = tb2 + ((2 * nh + 1) ^ f) * 32;
#pragma unroll
      for (int n = 0; n < 8; ++n) { w2[2 * n] = trfrag(L + t0 + n * 8192); w2[2 * n + 1] = trfrag(L + t1 + n * 8192); }
    }
    f2 x1[2][4];
#pragma unroll
    for (int k = 0; k < 2; ++k)
#pragma unroll
      for (int e = 0; e < 4; ++e) { x1[k][e] = unpk(xq[k][e]); asm volatile("" : "+v"(x1[k][e])); }
    ST(19);
    BAR();
    ST(8);
    {
      f4 acc[4][2];
      v4u a2[2][4];
      const int ab = kh * 4096 + l * 16;
#pragma unroll
      for (int r = 0; r < 4; ++r) a2[0][r] = *(LAS v4u*)(L + ab + r * 1024);
#pragma unroll
      for (int n = 0; n < 8; ++n) {
        if (!(n & 1)) SBAR();
        if (n == 0) SL(20);
        SB();
#pragma unroll
        for (int r = 0; r < 4; ++r) {
          MAC(n == 0, acc[r][0], w2[2 * n], a2[n & 1][r]);
          SB();
          if (n < 7) a2[(n + 1) & 1][r] = *(LAS v4u*)(L + ab + ((n + 1) * 8 + r) * 1024);
          SB();
          MAC(n == 0, acc[r][1], w2[2 * n + 1], a2[n & 1][r]);
          SB();
        }
      }
      MFMA_DONE();
      SL(21);
      red_write(c, l, nh, kh, acc);
    }
    meet4(L, L_W + 80, v, tag, l);
    ST(36);
    wait4(L, L_W + 48, tag, l);
    ST(25);
    __builtin_amdgcn_s_setprio(1);
    epi2(c, tid - 256, x1);
    __builtin_amdgcn_s_setprio(0);
    ST(17);
    asm volatile("" ::: "memory");
    const unsigned w3off = OFF_W3T + c.tile0 * 16384, w4off = OFF_W4T + ((j * 2 + nh) * 2 + kh) * 44 * 1024;
#pragma unroll
    for (int n = 0; n < 6; ++n) w3a[n] = LDW(n, w3off);
    WAITV(8);
    meet4(L, L_W + 96, v, tag, l);
    if (w == 6) raise(rs, OFF_GFLAG + (256 + g * 8 + j) * 128, tag, l);
    ST(9);
    if (v < 2) dma<4>(rsv, l * 16, OFF_XA + (g * 16 + 2 * j + v) * 4096, Lb + v * 4096);
#pragma unroll
    for (int n = 6; n < 16; ++n) w3a[n] = LDW(n, w3off);
    SB();
    float uvt[1];
    col_terms<1>(c, false, l, w3a, w3b, b1a, b1a, uvt);
    USE(uvt[0]);
    if (v < 2) WAITV(0);
    SBAR();
    ST(10);
    {
      f4 acc[4][1];
      const float S[5] = {0.f, 0.f, 0.f, 0.f, 0.f};
      mlp1_loop<1, 1>(c, l, w3a, w3b, acc, w4, w4off, false, S STA);
      BAR();
      ST(12);
      mlp1_epi<1>(c, false, l, uvt, acc);
    }
    asm volatile("" ::: "memory");
    const rsrc_t rb2 = __builtin_amdgcn_make_buffer_rsrc((void*)b2_, 0, 1024, 0x00020000);
    const v4u b2v = __builtin_amdgcn_raw_buffer_load_b128(rb2, (l & 7) * 16, j * 128, 0);
    ST(18);
    WAITV_BAR(1);
    if (w == 4) raise(rs, OFF_GFLAG + (512 + g * 8 + j) * 128, tag, l);
    ST(13);
    {
      f4 acc[4][2];
      v4u a4[2][4];
      const int ab = kh * 4096 + l * 16, abh = fresh(ab + 65536), abx = fresh(ab + 131072);
      SBAR();
      ST(14);
#pragma unroll
      for (int r = 0; r < 4; ++r) a4[0][r] = *(LAS v4u*)(L + ab + r * 1024);
#pragma unroll
      for (int n = 0; n < 22; ++n) {
        if ((n & 1) && n < 19) SBAR();
        if (n == 1) SL(26);
        if (n == 9) SL(27);
        if (n == 17) SL(28);
        SB();
        const int sp = 2 * (n + 1) >= 38 ? 2 * (n + 1) - 38 : 2 * (n + 1);
        if (n < 21 || kh == 0) {
#pragma unroll
          for (int r = 0; r < 4; ++r) {
            MAC(n == 0, acc[r][0], w4[2 * n], a4[n & 1][r]);
            SB();
            if (n < 21) a4[(n + 1) & 1][r] = *(LAS v4u*)(L + (sp < 16 ? ab : sp < 32 ? abh : abx) + (sp & 15) * 4096 + r * 1024);
            SB();
            MAC(n == 0, acc[r][1], w4[2 * n + 1], a4[n & 1][r]);
            SB();
          }
        }
      }
      MFMA_DONE();
      ST(15);
      red_write(c, l, nh, kh, acc);
    }
    meet4(L, L_W + 112, v, tag, l);
    epi4(c, tid - 256, b2v, x1);
    ST(16);
    SLF();
    __builtin_amdgcn_s_waitcnt(0x0F70);
    __builtin_amdgcn_endpgm();
  }
}

extern "C" void kernel_launch(void* const* d_in, const int* in_sizes, int n_in, void* d_out, int out_size, void* d_ws, size_t ws_size,
                              hipStream_t stream) {
  (void)hipFuncSetAttribute((const void*)fused_block, hipFuncAttributeMaxDynamicSharedMemorySize, LDS_BYTES);
  fused_block<<<256, 512, LDS_BYTES, stream>>>((const u16*)d_in[0], (const u16*)d_in[1], (const u16*)d_in[2], (const u16*)d_in[4],
                                               (const u16*)d_in[5], (const u16*)d_in[6], (const u16*)d_in[7], (const u16*)d_in[8],
                                               (const u16*)d_in[9], (u16*)d_out, (unsigned char*)d_ws);
}
```
